```python
import torch

def _softmax(x, axis=-1):
    return torch.softmax(x, dim=axis)
def _gelu(x):
    return torch.nn.functional.gelu(x, approximate="tanh")
def _silu(x):
    return torch.nn.functional.silu(x)
def _sigmoid(x):
    return torch.sigmoid(x)
def _layernorm(x, axis=-1, eps=1e-5):
    m = x.mean(dim=axis, keepdim=True)
    v = x.var(dim=axis, keepdim=True, unbiased=False)
    return (x - m) / torch.sqrt(v + eps)
def _scatter_add(tgt, idx, src, axis=0):
    return tgt.clone().index_add_(axis, idx, src)
def _segment_sum(src, seg, n):
    out = torch.zeros((n,) + src.shape[1:], dtype=src.dtype)
    return out.index_add_(0, seg, src)
def _pad(x, pw):
    flat = tuple(v for pair in reversed(pw) for v in pair)
    return torch.nn.functional.pad(x, flat)

kernel_name = 'concat_heads__sliding_block__ssm_conv_h3__prompt'

def setup_inputs():

    _w = (lambda a: (lambda t: t.to(torch.bfloat16) if t.is_floating_point() else t)(torch.as_tensor(a)))
    def _A_setup():
        B, S, D = 1, 2048, 576
        return {
            "x": _w((lambda *s: torch.randn(*s, dtype=torch.bfloat16) * 0.2)(B, S, D)),
            "wqkv": _w((lambda *s: torch.randn(*s, dtype=torch.bfloat16) * 0.2)(D, 3 * D)),
            "wo": _w((lambda *s: torch.randn(*s, dtype=torch.bfloat16) * 0.05)(D, D)),
        }
    _A_in = _A_setup()

    def _B_setup():
        B, S, D = 1, 2048, 576
        ks = 4
        return {
            "q": _w((lambda *s: torch.randn(*s, dtype=torch.bfloat16) * 0.05)(B, S, D)),
            "k": _w((lambda *s: torch.randn(*s, dtype=torch.bfloat16) * 0.8)(B, S, D)),
            "v": _w((lambda *s: torch.randn(*s, dtype=torch.bfloat16) * 0.8)(B, S, D)),
            "shift_k": _w((lambda *s: torch.randn(*s, dtype=torch.bfloat16) * 0.8)(D, ks) * 0.5),
            "ssm_k": _w((lambda *s: torch.randn(*s, dtype=torch.bfloat16) * 3.2)(D, S) * (1.0 / S ** 0.5)),
            "ssm_D": _w((lambda *s: torch.randn(*s, dtype=torch.bfloat16) * 0.8)(D)),
        }
    _B_in = _B_setup()

    return {
        "A_x": _A_in["x"],
        "A_wqkv": _A_in["wqkv"],
        "A_wo": _A_in["wo"],
        "B_q": _B_in["q"],
        "B_k": _B_in["k"],
        "B_v": _B_in["v"],
        "B_shift_k": _B_in["shift_k"],
        "B_ssm_k": _B_in["ssm_k"],
        "B_ssm_D": _B_in["ssm_D"],
    }

def reference(A_x, A_wqkv, A_wo, B_q, B_k, B_v, B_shift_k, B_ssm_k, B_ssm_D):
    def _A_ref(x, wqkv, wo):
        B, S, D = x.shape
        H = 32; Dh = D // H; W = 256
        qkv = (lambda x, *d: x.permute(*d))((x @ wqkv).reshape(B, S, 3, H, Dh), 2, 0, 3, 1, 4)
        q, k, v = qkv[0], qkv[1], qkv[2]
        scores = torch.einsum("bhqd,bhkd->bhqk", q, k) * (Dh ** -0.5)
        idx = torch.arange(S)
        mask = (idx[:, None] - idx[None, :] >= 0) & (idx[:, None] - idx[None, :] < W)
        scores = torch.where(mask[None, None], scores, -1e9)
        p = _softmax(scores, axis=-1)
        o = torch.einsum("bhqk,bhkd->bhqd", p, v)
        return (lambda x, *d: x.permute(*d))(o, 0, 2, 1, 3).reshape(B, S, D) @ wo

    def _B_ref(q, k, v, shift_k, ssm_k, ssm_D):
        B, S, D = q.shape
        ks = shift_k.shape[-1]
        k_pad = _pad(k, ((0, 0), (ks - 1, 0), (0, 0)))
        k_shift = torch.zeros_like(k)
        for t in range(ks):
            k_shift = k_shift + k_pad[:, t:t + S, :] * shift_k[:, ks - 1 - t][None, None, :]
        kv = k_shift * v
        L = 2 * S
        kv_pad = _pad(kv, ((0, 0), (0, S), (0, 0)))
        kf_pad = _pad(ssm_k, ((0, 0), (0, S)))
        kv_f = (lambda x, axes=(-2,-1): torch.fft.rfft2(x.float(), dim=axes))(kv_pad[:, None, :, :], axes=(1, 2))
        kf_f = (lambda x, axes=(-2,-1): torch.fft.rfft2(x.float(), dim=axes))(kf_pad[:, None, :], axes=(1, 2))
        kf_f = (lambda x, *d: x.permute(*d))(kf_f, 1, 2, 0)
        y_f = kv_f * kf_f[None, :, :, :]
        y = (lambda x, s, axes=(-2,-1): torch.fft.irfft2(x, s=s, dim=axes).to(torch.bfloat16))(y_f, (1, L), axes=(1, 2))[:, 0, :S, :]
        y = y + kv * ssm_D[None, None, :]
        return q * y

    a = _A_ref(A_x, A_wqkv, A_wo)
    b = _B_ref(B_q, B_k, B_v, B_shift_k, B_ssm_k, B_ssm_D)
    return torch.concatenate([a, b], axis=-1)
```

```cpp
#include <hip/hip_runtime.h>
#include <stdint.h>

#define GAS __attribute__((address_space(1)))
#define LAS __attribute__((address_space(3)))
#define DEVI __device__ __forceinline__
typedef unsigned long long u64;
typedef __attribute__((ext_vector_type(8))) __bf16 bf16x8;
typedef __attribute__((ext_vector_type(2))) __bf16 bf16x2;
typedef __attribute__((ext_vector_type(2))) float f32x2;
typedef __attribute__((ext_vector_type(4))) float f32x4;
typedef __attribute__((ext_vector_type(16))) float f32x16;
typedef __attribute__((ext_vector_type(4))) unsigned u32x4;
typedef __attribute__((ext_vector_type(2))) unsigned u32x2;
typedef __attribute__((ext_vector_type(4))) short s16x4;

constexpr int WS_KFLAG = 256, WS_KVFLAG = 4096, WS_OFLAG = 8192, WS_YFLAG = 12288;
constexpr int WS_R0 = 32768;
constexpr int WS_KV = WS_R0 + 2359296;
constexpr int WS_OM = WS_KV + 4849664;
constexpr int WS_OR = WS_OM + 2097152;
constexpr int WS_Y = WS_OR + 262144;
constexpr int WS_END = WS_Y + 2359296;
constexpr int WS_TS = 16 << 20;
constexpr int L_B1K = 131072, L_B1V = 141312;
constexpr int KVB = 18944, HEADB = 9472;
constexpr int L_OWN = 2 * KVB, L_Q = 3 * KVB;
constexpr int L_KC = 66048, KCS = 4160, KCH = 4 * KCS;
constexpr int L_RB = 131072, RCS = 4576, RCH = 2 * RCS;
constexpr int L_WO = 66048;
constexpr int LDS_BYTES = 163840;
constexpr unsigned SPIN_CAP = 1u << 17;

#define STAMP(k) do { } while (0)

DEVI f32x4 mfma16(u32x4 a, u32x4 b, f32x4 c) { return __builtin_amdgcn_mfma_f32_16x16x32_bf16(__builtin_bit_cast(bf16x8, a), __builtin_bit_cast(bf16x8, b), c, 0, 0, 0); }
DEVI f32x16 mfma32(u32x4 a, u32x4 b, f32x16 c) { return __builtin_amdgcn_mfma_f32_32x32x16_bf16(__builtin_bit_cast(bf16x8, a), __builtin_bit_cast(bf16x8, b), c, 0, 0, 0); }
DEVI unsigned pk(float lo, float hi) { f32x2 v = {lo, hi}; return __builtin_bit_cast(unsigned, __builtin_convertvector(v, bf16x2)); }
DEVI float blo(unsigned v) { return __builtin_bit_cast(float, v << 16); }
DEVI float bhi(unsigned v) { return __builtin_bit_cast(float, v & 0xffff0000u); }
DEVI float rndbf(float x) { return blo(pk(x, x)); }
DEVI u32x4 lds128(unsigned a) { return *(LAS const u32x4*)(uintptr_t)a; }
DEVI u32x2 lds64(unsigned a) { return *(LAS const u32x2*)a; }
DEVI unsigned lds32(unsigned a) { return *(LAS const unsigned*)a; }
DEVI void st128(unsigned a, u32x4 v) { *(LAS u32x4*)a = v; }
DEVI void st64(unsigned a, u32x2 v) { *(LAS u32x2*)a = v; }
DEVI void st32(unsigned a, unsigned v) { *(LAS unsigned*)a = v; }
DEVI void st16(unsigned a, unsigned short v) { *(LAS unsigned short*)a = v; }
DEVI u32x2 ldstr(unsigned a) { return __builtin_bit_cast(u32x2, __builtin_amdgcn_ds_read_tr16_b64_v4i16((LAS s16x4*)a)); }
DEVI void dma16(const void* g, unsigned lds) {
    unsigned sv;
    asm volatile("s_mov_b32 %0, m0\n\ts_mov_b32 m0, %2\n\ts_nop 0\n\tglobal_load_lds_dwordx4 %1, off\n\ts_mov_b32 m0, %0" : "=&s"(sv) : "v"(g), "s"(lds) : "memory");
}
DEVI u32x4 gld128(const void* p) { u32x4 v; asm volatile("global_load_dwordx4 %0, %1, off" : "=v"(v) : "v"(p) : "memory"); return v; }
#define WAITVM(n) asm volatile("s_waitcnt vmcnt(%0)" ::"i"(n) : "memory")
#define TIE(x) asm volatile("" : "+v"(x))
#define BAR() do { asm volatile("s_waitcnt lgkmcnt(0)\n\ts_barrier" ::: "memory"); } while (0)
#define RLX __ATOMIC_RELAXED, __HIP_MEMORY_SCOPE_AGENT

DEVI void poll_flags(const GAS u64* f, int n, u64 nonce, int lane) {
    for (unsigned spins = 0; spins < SPIN_CAP; ++spins) {
        u64 v = nonce;
        if (lane < n) v = __hip_atomic_load(f + lane, RLX);
        if (__all(v == nonce)) break;
        __builtin_amdgcn_s_sleep(2);
    }
    __builtin_amdgcn_fence(__ATOMIC_ACQUIRE, "wavefront");
    asm volatile("" ::: "memory");
}

__global__ __launch_bounds__(512, 2) void fused_kernel(const unsigned short* __restrict__ x, const unsigned short* __restrict__ wqkv, const unsigned short* __restrict__ wo,
                                                       const unsigned short* __restrict__ bq, const unsigned short* __restrict__ bk, const unsigned short* __restrict__ bv,
                                                       const unsigned short* __restrict__ shk, const unsigned short* __restrict__ ssk, const unsigned short* __restrict__ ssd,
                                                       unsigned short* __restrict__ out, char* __restrict__ ws) {
    __shared__ __attribute__((aligned(16))) char smem[LDS_BYTES];
    const unsigned L0 = (unsigned)(uintptr_t)(LAS char*)smem;
    const int tid = threadIdx.x, lane = tid & 63, w = __builtin_amdgcn_readfirstlane(tid >> 6);
    const int b = blockIdx.x, xcd = b & 7, jj = b >> 3;
    const int bi = 4 * (xcd >> 1) + (jj & 3), gp = 8 * (xcd & 1) + (jj >> 2);
    const int br = 4 * xcd + (jj & 3), bc = jj >> 2;
    const int l15 = lane & 15, g4 = lane >> 4;
    const __amdgpu_buffer_rsrc_t wsr = __builtin_amdgcn_make_buffer_rsrc(ws, 0, WS_END, 0x00020000);
    STAMP(0);

    u32x2 ep; asm volatile("global_load_dwordx2 %0, %1, off sc1" : "=v"(ep) : "v"(ws) : "memory");
    const int nch = (b & 3) == 0 ? 3 : 2;
    const int cu = tid >> 8, cq = tid & 255;
    const int ch01 = 2 * b + cu, ch2 = 512 + (b >> 2);
    u32x4 ka0, ka1, kb0, kb1;
    {
        const char* p = (const char*)ssk + ch01 * 4096 + cq * 16;
        ka0 = gld128(p); ka1 = gld128(p + (cq < 255 ? 16 : 0));
        const char* p2 = (const char*)ssk + ch2 * 4096 + cq * 16;
        kb0 = gld128(p2); kb1 = gld128(p2 + (cq < 255 ? 16 : 0));
    }
    const int cp = tid >> 3, oct = tid & 7;
    const int chB = 72 * bc + 2 * (cp < 36 ? cp : 35);
    u32x4 shv = gld128((const char*)shk + chB * 8);
    for (int u = w; u < 19; u += 8) {
        if (u < 10) {
            int q = u * 64 + lane; q = q < 612 ? q : 611;
            int row = q / 9, cc = q - row * 9; int tok = 64 * br - 4 + row; tok = tok < 0 ? 0 : tok;
            dma16((const char*)bk + tok * 1152 + bc * 144 + cc * 16, L0 + L_B1K + u * 1024);
        } else {
            int q = (u - 10) * 64 + lane; int row = q / 9, cc = q - row * 9;
            dma16((const char*)bv + (64 * br + row) * 1152 + bc * 144 + cc * 16, L0 + L_B1V + (u - 10) * 1024);
        }
    }
    const char* xsrc = (const char*)x + (size_t)(128 * bi + 16 * w + (lane >> 3)) * 1152 + (((lane & 7) ^ (lane >> 3)) * 16);
    const char* wsrc;
    {
        int swz = 2 * (g4 | ((w & 1) << 2));
        int lc = l15 ^ swz; lc = lc < 15 ? lc : 14;
        int sec = lc / 5, cc = lc - sec * 5;
        wsrc = (const char*)wqkv + (size_t)(8 * w + g4) * 3456 + sec * 1152 + 72 * gp - (gp & 1) * 8 + cc * 16;
    }
    auto a1_issue = [&](int s) {
        const unsigned sl = L0 + (s & 3) * 32768;
        dma16(xsrc + s * 128, sl + (16 * w) * 128);
        dma16(xsrc + s * 128 + 8 * 1152, sl + (16 * w + 8) * 128);
        dma16(wsrc + (size_t)s * 64 * 3456, sl + 16384 + (8 * w) * 256);
        dma16(wsrc + (size_t)s * 64 * 3456 + 4 * 3456, sl + 16384 + (8 * w + 4) * 256);
    };
    a1_issue(0); a1_issue(1); a1_issue(2); a1_issue(3);

    WAITVM(16);
    TIE(ka0); TIE(ka1); TIE(kb0); TIE(kb1); TIE(shv); TIE(ep);
    const u64 nonce = (((u64)(unsigned)__builtin_amdgcn_readfirstlane(ep[1]) << 32) | (unsigned)__builtin_amdgcn_readfirstlane(ep[0])) + 1;
    BAR();
    STAMP(1);
    unsigned kvp[8];
    if (tid < 288) {
        float k0[11], k1[11];
#pragma unroll
        for (int q = 0; q < 11; ++q) { unsigned v = lds32(L0 + L_B1K + (8 * oct + 1 + q) * 144 + cp * 4); k0[q] = blo(v); k1[q] = bhi(v); }
        if (br == 0 && oct == 0) { k0[0] = k0[1] = k0[2] = 0.f; k1[0] = k1[1] = k1[2] = 0.f; }
        const float s00 = blo(shv[0]), s01 = bhi(shv[0]), s02 = blo(shv[1]), s03 = bhi(shv[1]);
        const float s10 = blo(shv[2]), s11 = bhi(shv[2]), s12 = blo(shv[3]), s13 = bhi(shv[3]);
#pragma unroll
        for (int t = 0; t < 8; ++t) {
            unsigned v = lds32(L0 + L_B1V + (8 * oct + t) * 144 + cp * 4);
            float a0 = k0[t] * s03; a0 = fmaf(k0[t + 1], s02, a0); a0 = fmaf(k0[t + 2], s01, a0); a0 = fmaf(k0[t + 3], s00, a0);
            float a1 = k1[t] * s13; a1 = fmaf(k1[t + 1], s12, a1); a1 = fmaf(k1[t + 2], s11, a1); a1 = fmaf(k1[t + 3], s10, a1);
            unsigned ks = pk(a0, a1);
            kvp[t] = pk(blo(ks) * blo(v), bhi(ks) * bhi(v));
        }
        u32x4 r0, r1;
#pragma unroll
        for (int d = 0; d < 4; ++d) {
            r0[d] = (kvp[7 - 2 * d] & 0xffffu) | (kvp[6 - 2 * d] << 16);
            r1[d] = (kvp[7 - 2 * d] >> 16) | (kvp[6 - 2 * d] & 0xffff0000u);
        }
        const int off = WS_R0 + chB * 4096 + (2040 - 64 * br - 8 * oct) * 2;
        __builtin_amdgcn_raw_buffer_store_b128(r0, wsr, off, 0, 16);
        __builtin_amdgcn_raw_buffer_store_b128(r1, wsr, off + 4096, 0, 16);
    } else {
#pragma unroll
        for (int t = 0; t < 8; ++t) kvp[t] = 0;
    }
    STAMP(2);

    const int tg = w & 3, nh = w >> 2;
    f32x4 aq[2], ak[2], av[2], ar[2];
#pragma unroll
    for (int t = 0; t < 2; ++t) { aq[t] = f32x4{0, 0, 0, 0}; ak[t] = aq[t]; av[t] = aq[t]; ar[t] = aq[t]; }
    {
        const int q4 = (lane >> 2) & 3, p4 = lane & 3, po = (gp & 1) * 8;
        const int swz = 2 * (q4 | ((g4 & 1) << 2));
        auto woff = [&](int sec, int piece) { int byte = sec * 80 + po + 8 * piece; return (unsigned)(16384 + (8 * g4 + q4) * 256 + (((byte >> 4) ^ swz) * 16) + (byte & 8)); };
        const unsigned wq = woff(0, 5 * nh + p4), wk = woff(1, 5 * nh + p4), wv = woff(2, 5 * nh + p4), wr = woff(p4 < 3 ? p4 : 2, 4);
        const unsigned xo = (32 * tg + l15) * 128;
        const unsigned xs0 = ((g4 ^ (lane & 7)) * 16), xs1 = (((4 + g4) ^ (lane & 7)) * 16);
#pragma unroll
        for (int s = 0; s < 9; ++s) {
            if (s == 0) WAITVM(12); else if (s < 7) WAITVM(8); else if (s == 7) WAITVM(4); else WAITVM(0);
            BAR();
            if (s >= 1 && s + 3 <= 8) a1_issue(s + 3);
            if (s == 3 && tid == 0) __hip_atomic_store((GAS u64*)(ws + WS_KFLAG) + bc * 32 + br, nonce, RLX);
            const unsigned sl = L0 + (s & 3) * 32768;
#pragma unroll
            for (int kk = 0; kk < 2; ++kk) {
                u32x4 xf[2];
                xf[0] = lds128(sl + xo + (kk ? xs1 : xs0));
                xf[1] = lds128(sl + xo + 2048 + (kk ? xs1 : xs0));
                u32x2 a, c;
                u32x4 fq, fk, fv;
                a = ldstr(sl + wq + kk * 8192); c = ldstr(sl + wq + kk * 8192 + 1024); fq = u32x4{a[0], a[1], c[0], c[1]};
                a = ldstr(sl + wk + kk * 8192); c = ldstr(sl + wk + kk * 8192 + 1024); fk = u32x4{a[0], a[1], c[0], c[1]};
                a = ldstr(sl + wv + kk * 8192); c = ldstr(sl + wv + kk * 8192 + 1024); fv = u32x4{a[0], a[1], c[0], c[1]};
#pragma unroll
                for (int t = 0; t < 2; ++t) { aq[t] = mfma16(fq, xf[t], aq[t]); ak[t] = mfma16(fk, xf[t], ak[t]); av[t] = mfma16(xf[t], fv, av[t]); }
                if (nh == 0) {
                    a = ldstr(sl + wr + kk * 8192); c = ldstr(sl + wr + kk * 8192 + 1024);
                    u32x4 fr = u32x4{a[0], a[1], c[0], c[1]};
#pragma unroll
                    for (int t = 0; t < 2; ++t) ar[t] = mfma16(fr, xf[t], ar[t]);
                }
            }
        }
    }
    STAMP(3);
    BAR();
    {
        const float qs = 0.34004634f;
#pragma unroll
        for (int t = 0; t < 2; ++t) {
            const unsigned ko = tg * 1024 + (g4 >> 1) * 512 + (16 * t + l15) * 16 + (g4 & 1) * 8;
            st64(L0 + L_OWN + nh * HEADB + ko, u32x2{pk(ak[t][0], ak[t][1]), pk(ak[t][2], ak[t][3])});
            st64(L0 + L_Q + nh * 4608 + ko, u32x2{pk(aq[t][0] * qs, aq[t][1] * qs), pk(aq[t][2] * qs, aq[t][3] * qs)});
            const int tt = 2 * tg + t;
            st64(L0 + L_OWN + nh * HEADB + 4608 + tt * 608 + (g4 & 1) * 304 + l15 * 16 + (g4 >> 1) * 8, u32x2{pk(av[t][0], av[t][1]), pk(av[t][2], av[t][3])});
            if (nh == 0) {
                const int tok = 32 * tg + 16 * t + l15;
                if (g4 == 0) {
                    st32(L0 + L_Q + 4096 + tok * 4, pk(ar[t][0] * qs, ar[t][1] * qs));
                    st32(L0 + L_Q + 4608 + 4096 + tok * 4, pk(ar[t][2] * qs, ar[t][3] * qs));
                } else if (g4 == 1) {
                    st32(L0 + L_OWN + 4096 + tok * 4, pk(ar[t][0], ar[t][1]));
                    st32(L0 + L_OWN + HEADB + 4096 + tok * 4, pk(ar[t][2], ar[t][3]));
                } else if (g4 == 2) {
                    const unsigned p01 = pk(ar[t][0], ar[t][1]), p23 = pk(ar[t][2], ar[t][3]);
                    const unsigned vb = L0 + L_OWN + 4608 + tt * 608 + ((l15 >> 2) & 1) * 304 + 16 * 16 + (4 * (l15 >> 3) + (l15 & 3)) * 2;
                    st16(vb, (unsigned short)p01); st16(vb + 16, (unsigned short)(p01 >> 16));
                    st16(vb + HEADB, (unsigned short)p23); st16(vb + HEADB + 16, (unsigned short)(p23 >> 16));
                }
            } else if (lane < 4) {
                st128(L0 + L_OWN + (lane >> 1) * HEADB + 4608 + tt * 608 + (lane & 1) * 304 + 18 * 16, u32x4{0x3f803f80u, 0x3f803f80u, 0x3f803f80u, 0x3f803f80u});
            }
        }
    }
    {
        auto kcopy = [&](unsigned kb, u32x4 d0, u32x4 d1) {
            st128(kb + (4 + cq) * 16, d0);
            st128(kb + KCS + (4 + cq) * 16, u32x4{d0[1], d0[2], d0[3], d1[0]});
            st128(kb + 2 * KCS + (4 + cq) * 16, u32x4{d0[2], d0[3], d1[0], d1[1]});
            st128(kb + 3 * KCS + (4 + cq) * 16, u32x4{d0[3], d1[0], d1[1], d1[2]});
            if (cq == 0) {
                st128(kb + 3 * 16, u32x4{0, 0, 0, 0});
                st128(kb + KCS + 3 * 16, u32x4{0, 0, 0, d0[0]});
                st128(kb + 2 * KCS + 3 * 16, u32x4{0, 0, d0[0], d0[1]});
                st128(kb + 3 * KCS + 3 * 16, u32x4{0, d0[0], d0[1], d0[2]});
            } else if (cq < 4) {
#pragma unroll
                for (int c = 0; c < 4; ++c) st128(kb + c * KCS + (cq - 1) * 16, u32x4{0, 0, 0, 0});
            }
        };
        kcopy(L0 + L_KC + cu * KCH, ka0, ka1);
        if (nch == 3 && tid < 256) kcopy(L0 + L_KC + 2 * KCH, kb0, kb1);
    }
    BAR();
    {
        const int gb = WS_KV + (bi * 16 + gp) * KVB;
#pragma unroll
        for (int k = 0; k < 3; ++k) {
            const int q = tid + 512 * k;
            if (q < 1184) __builtin_amdgcn_raw_buffer_store_b128(lds128(L0 + L_OWN + q * 16), wsr, gb + q * 16, 0, 16);
        }
    }
    if (w == 0) {
        poll_flags((const GAS u64*)(ws + WS_KFLAG) + ((2 * b) / 72) * 32, 32, nonce, lane);
        if (nch == 3) poll_flags((const GAS u64*)(ws + WS_KFLAG) + (ch2 / 72) * 32, 32, nonce, lane);
    }
    WAITVM(0);
    BAR();
    if (tid == 0) __hip_atomic_store((GAS u64*)(ws + WS_KVFLAG) + gp * 16 + bi, nonce, RLX);
    STAMP(4);
    {
        auto rcopy = [&](unsigned rb, int ch) {
            const int off = WS_R0 + ch * 4096 + cq * 16;
            u32x4 d = __builtin_amdgcn_raw_buffer_load_b128(wsr, off, 0, 16);
            unsigned nx = 0;
            if (cq < 255) nx = __builtin_amdgcn_raw_buffer_load_b32(wsr, off + 16, 0, 16);
            st128(rb + cq * 16, d);
            st128(rb + RCS + cq * 16, u32x4{__builtin_amdgcn_alignbit(d[1], d[0], 16), __builtin_amdgcn_alignbit(d[2], d[1], 16), __builtin_amdgcn_alignbit(d[3], d[2], 16), __builtin_amdgcn_alignbit(nx, d[3], 16)});
            if (cq < 30) { st128(rb + (256 + cq) * 16, u32x4{0, 0, 0, 0}); st128(rb + RCS + (256 + cq) * 16, u32x4{0, 0, 0, 0}); }
        };
        rcopy(L0 + L_RB + cu * RCH, ch01);
        if (nch == 3 && tid < 256) rcopy(L0 + L_RB + 2 * RCH, ch2);
    }
    BAR();
    STAMP(5);

    {
        const int slot = w < 4 ? (w >> 1) : 2;
        const int rho0 = w < 4 ? 4 * (w & 1) : 2 * (w - 4);
        const int nrho = w < 4 ? 4 : (nch == 3 ? 2 : 0);
        f32x4 acc[8];
#pragma unroll
        for (int n = 0; n < 8; ++n) acc[n] = f32x4{0, 0, 0, 0};
        const unsigned la = L0 + L_KC + slot * KCH + ((l15 + 1) & 3) * KCS + (g4 + ((l15 + 1) >> 2)) * 16;
        const unsigned lb = L0 + L_RB + slot * RCH + ((l15 & 1) ? 0 : RCS) + (252 - 28 * 8 - 4 * (l15 >> 1) + g4) * 16;
        for (int rr = 0; rr < nrho; ++rr) {
            const unsigned ro = (rho0 + rr) * 64;
            u32x4 A[8], B[8];
#pragma unroll
            for (int m = 0; m < 8; ++m) { A[m] = lds128(la + ro + m * 512); B[m] = lds128(lb + ro + (7 - m) * 512); }
#pragma unroll
            for (int nu = 0; nu < 8; ++nu)
#pragma unroll
                for (int mu = 0; mu + nu < 8; ++mu) acc[mu + nu] = mfma16(A[mu], B[nu], acc[mu + nu]);
        }
        BAR();
        const unsigned sc = L0 + L_KC + slot * KCH;
        if (w < 4) {
            const int h = w & 1;
            if (h) {
#pragma unroll
                for (int n = 0; n < 4; ++n) *(LAS f32x4*)(sc + 4096 + n * 1024 + lane * 16) = acc[n];
            } else {
#pragma unroll
                for (int n = 0; n < 4; ++n) *(LAS f32x4*)(sc + n * 1024 + lane * 16) = acc[4 + n];
            }
        } else if (nch == 3) {
            const int h = w - 4;
#pragma unroll
            for (int n = 0; n < 4; ++n) *(LAS f32x4*)(sc + h * 4096 + n * 1024 + lane * 16) = acc[n];
        }
        BAR();
        f32x4 y[4];
        if (w < 4) {
            const int h = w & 1;
            if (h) {
#pragma unroll
                for (int n = 0; n < 4; ++n) y[n] = acc[4 + n] + *(LAS const f32x4*)(sc + n * 1024 + lane * 16);
            } else {
#pragma unroll
                for (int n = 0; n < 4; ++n) y[n] = acc[n] + *(LAS const f32x4*)(sc + 4096 + n * 1024 + lane * 16);
            }
        } else if (nch == 3) {
            const int h = w - 4;
            f32x4 s4 = f32x4{0, 0, 0, 0};
#pragma unroll
            for (int q = 0; q < 4; ++q) s4 += *(LAS const f32x4*)(sc + q * 4096 + h * 1024 + lane * 16);
            y[0] = s4;
        }
        BAR();
        if (w >= 4 && nch == 3) {
            const int h = w - 4;
#pragma unroll
            for (int n = 0; n < 4; ++n) *(LAS f32x4*)(sc + h * 4096 + n * 1024 + lane * 16) = acc[4 + n];
        }
        BAR();
        if (w >= 4 && nch == 3) {
            const int h = w - 4;
            f32x4 s4 = f32x4{0, 0, 0, 0};
#pragma unroll
            for (int q = 0; q < 4; ++q) s4 += *(LAS const f32x4*)(sc + q * 4096 + h * 1024 + lane * 16);
            y[1] = s4;
        }
        BAR();
        const unsigned ys = L0 + L_RB + slot * RCH;
        const unsigned yo = (l15 >> 1) * 64 + g4 * 16 + (l15 & 1) * 8;
        const int chs = slot == 2 ? ch2 : 2 * b + slot;
        if (w < 4) {
            const int h = w & 1;
#pragma unroll
            for (int n = 0; n < 4; ++n) st64(ys + (4 * h + n) * 512 + yo, u32x2{pk(y[n][0], y[n][1]), pk(y[n][2], y[n][3])});
#pragma unroll
            for (int k = 0; k < 2; ++k)
                __builtin_amdgcn_raw_buffer_store_b128(lds128(ys + h * 2048 + k * 1024 + lane * 16), wsr, WS_Y + chs * 4096 + h * 2048 + k * 1024 + lane * 16, 0, 16);
        } else if (nch == 3) {
            const int h = w - 4;
            st64(ys + h * 512 + yo, u32x2{pk(y[0][0], y[0][1]), pk(y[0][2], y[0][3])});
            st64(ys + (4 + h) * 512 + yo, u32x2{pk(y[1][0], y[1][1]), pk(y[1][2], y[1][3])});
            if (lane < 32) __builtin_amdgcn_raw_buffer_store_b128(lds128(ys + h * 512 + lane * 16), wsr, WS_Y + chs * 4096 + h * 512 + lane * 16, 0, 16);
            else __builtin_amdgcn_raw_buffer_store_b128(lds128(ys + (4 + h) * 512 + (lane - 32) * 16), wsr, WS_Y + chs * 4096 + (4 + h) * 512 + (lane - 32) * 16, 0, 16);
        }
    }
    if (w == 0 && bi > 0) poll_flags((const GAS u64*)(ws + WS_KVFLAG) + gp * 16 + (bi > 1 ? bi - 2 : 0), bi > 1 ? 2 : 1, nonce, lane);
    WAITVM(0);
    BAR();
    if (tid < nch) __hip_atomic_store((GAS u64*)(ws + WS_YFLAG) + (tid == 2 ? ch2 : 2 * b + tid), nonce, RLX);
    STAMP(6);
    {
        for (int hb = 0; hb < 2; ++hb) {
            const int ib = bi - 2 + hb;
            if (ib >= 0) {
                const int gb = WS_KV + (ib * 16 + gp) * KVB;
#pragma unroll
                for (int k = 0; k < 3; ++k) {
                    const int q = tid + 512 * k;
                    if (q < 1184) st128(L0 + hb * KVB + q * 16, __builtin_amdgcn_raw_buffer_load_b128(wsr, gb + q * 16, 0, 16));
                }
            }
        }
    }
    for (int u = w; u < 81; u += 8) {
        const int q = u * 64 + lane, row = q / 9, cc = q - row * 9;
        dma16((const char*)wo + row * 1152 + bc * 144 + cc * 16, L0 + L_WO + u * 1024);
    }
    BAR();
    STAMP(7);

    {
        const int hd = w >> 2, qt = w & 3, l31 = lane & 31, hh = lane >> 5;
        const u32x4 qm = lds128(L0 + L_Q + hd * 4608 + qt * 1024 + hh * 512 + l31 * 16);
        const unsigned qrv = lds32(L0 + L_Q + hd * 4608 + 4096 + (32 * qt + l31) * 4);
        const u32x4 qr = u32x4{hh ? 0u : qrv, 0, 0, 0};
        const int vrow = l31 < 19 ? l31 : (l31 == 19 ? 18 : (l31 < 28 ? 0 : 4));
        const unsigned kbase = L0 + hd * HEADB + hh * 512 + l31 * 16, krbase = L0 + hd * HEADB + 4096 + l31 * 4;
        const unsigned vbase = L0 + hd * HEADB + 4608 + hh * 304 + vrow * 16;
        const int qq = l31 - 4 * hh;
        const int t_first = bi >= 2 ? qt : (bi == 1 ? 4 : 8), t_last = 8 + qt;
        const bool has_first = bi >= 2;
        auto qk = [&](int t, f32x16 c) {
            const unsigned bo = (t >> 2) * KVB;
            u32x4 km = lds128(kbase + bo + (t & 3) * 1024);
            unsigned kr = lds32(krbase + bo + (t & 3) * 128);
            c = mfma32(km, qm, c);
            return mfma32(u32x4{kr, 0, 0, 0}, qr, c);
        };
        auto pv = [&](int t, const f32x16& p, f32x16 o) {
            const unsigned bo = (t >> 2) * KVB + (t & 3) * 1216;
            u32x4 v0 = lds128(vbase + bo), v1 = lds128(vbase + bo + 608);
            u32x4 p0 = u32x4{pk(p[0], p[1]), pk(p[2], p[3]), pk(p[4], p[5]), pk(p[6], p[7])};
            u32x4 p1 = u32x4{pk(p[8], p[9]), pk(p[10], p[11]), pk(p[12], p[13]), pk(p[14], p[15])};
            o = mfma32(v0, p0, o);
            return mfma32(v1, p1, o);
        };
        const float NINF = -__builtin_inff();
        f32x16 z16;
#pragma unroll
        for (int r = 0; r < 16; ++r) z16[r] = 0.f;
        f32x16 sd = qk(t_last, z16);
        float m = NINF;
#pragma unroll
        for (int r = 0; r < 16; ++r) { const int c = (r & 3) + 8 * (r >> 2); sd[r] = c <= qq ? sd[r] : NINF; m = fmaxf(m, sd[r]); }
        m = fmaxf(m, __shfl_xor(m, 32));
        f32x16 o;
        float l;
        for (int attempt = 0; attempt < 2; ++attempt) {
            f32x16 nm, p;
#pragma unroll
            for (int r = 0; r < 16; ++r) { nm[r] = -m; p[r] = __builtin_amdgcn_exp2f(sd[r] - m); }
            o = pv(t_last, p, z16);
            for (int t = t_first; t < t_last; ++t) {
                f32x16 s = qk(t, nm);
                if (has_first && t == t_first) {
#pragma unroll
                    for (int r = 0; r < 16; ++r) { const int c = (r & 3) + 8 * (r >> 2); s[r] = c > qq ? s[r] : NINF; }
                }
#pragma unroll
                for (int r = 0; r < 16; ++r) p[r] = __builtin_amdgcn_exp2f(s[r]);
                o = pv(t, p, o);
            }
            l = __shfl(o[10], l31);
            if (attempt == 1 || __all(l <= 1048576.f)) break;
            for (int t = t_first; t < t_last; ++t) {
                f32x16 s = qk(t, z16);
#pragma unroll
                for (int r = 0; r < 16; ++r) { const int c = (r & 3) + 8 * (r >> 2); const bool keep = !(has_first && t == t_first) || c > qq; m = fmaxf(m, keep ? s[r] : NINF); }
            }
            m = fmaxf(m, __shfl_xor(m, 32));
        }
        const float il = 1.f / l;
        const int T16 = 8 * bi + 2 * qt + (l31 >> 4);
        u32x4 om = u32x4{pk(o[0] * il, o[1] * il), pk(o[2] * il, o[3] * il), pk(o[4] * il, o[5] * il), pk(o[6] * il, o[7] * il)};
        __builtin_amdgcn_raw_buffer_store_b128(om, wsr, WS_OM + ((T16 * 16 + gp) * 64 + (2 * hd + hh) * 16 + (l31 & 15)) * 16, 0, 16);
        if (hh == 0) __builtin_amdgcn_raw_buffer_store_b32(pk(o[8] * il, o[9] * il), wsr, WS_OR + (((T16 * 16 + gp) * 2 + hd) * 16 + (l31 & 15)) * 4, 0, 16);
    }
    WAITVM(0);
    BAR();
    if (tid == 0) __hip_atomic_store((GAS u64*)(ws + WS_OFLAG) + bi * 16 + gp, nonce, RLX);
    STAMP(8);

    if (w == 0) { poll_flags((const GAS u64*)(ws + WS_YFLAG) + 72 * bc, 64, nonce, lane); poll_flags((const GAS u64*)(ws + WS_YFLAG) + 72 * bc + 64, 8, nonce, lane); }
    BAR();
    if (tid < 288) {
        const int t0 = 64 * br + 8 * oct;
        u32x4 y0 = __builtin_amdgcn_raw_buffer_load_b128(wsr, WS_Y + chB * 4096 + t0 * 2, 0, 16);
        u32x4 y1 = __builtin_amdgcn_raw_buffer_load_b128(wsr, WS_Y + (chB + 1) * 4096 + t0 * 2, 0, 16);
        const unsigned dv = *(const unsigned*)(ssd + chB);
        const float D0 = blo(dv), D1 = bhi(dv);
#pragma unroll
        for (int t = 0; t < 8; ++t) {
            const int slot = (t & 1) * 4 + (t >> 1);
            const float ya = (slot & 1) ? bhi(y0[slot >> 1]) : blo(y0[slot >> 1]);
            const float yb = (slot & 1) ? bhi(y1[slot >> 1]) : blo(y1[slot >> 1]);
            const unsigned qv = *(const unsigned*)(bq + (size_t)(t0 + t) * 576 + chB);
            const unsigned kd = pk(blo(kvp[t]) * D0, bhi(kvp[t]) * D1);
            const unsigned zz = pk(ya + blo(kd), yb + bhi(kd));
            *(unsigned*)(out + (size_t)(t0 + t) * 1152 + 576 + chB) = pk(blo(qv) * blo(zz), bhi(qv) * bhi(zz));
        }
    }
    STAMP(9);

    if (w == 0) poll_flags((const GAS u64*)(ws + WS_OFLAG) + (br >> 1) * 16, 16, nonce, lane);
    WAITVM(0);
    BAR();
    STAMP(10);
    {
        const int tt = w & 3, kh = w >> 2, T16 = 4 * br + tt;
        u32x4 of[9];
#pragma unroll
        for (int s = 0; s < 8; ++s) of[s] = __builtin_amdgcn_raw_buffer_load_b128(wsr, WS_OM + ((T16 * 16 + 8 * kh + s) * 64 + lane) * 16, 0, 16);
#pragma unroll
        for (int d = 0; d < 4; ++d) of[8][d] = __builtin_amdgcn_raw_buffer_load_b32(wsr, WS_OR + (((T16 * 16 + 8 * kh + 2 * g4 + (d >> 1)) * 2 + (d & 1)) * 16 + l15) * 4, 0, 16);
        f32x4 acc[5];
#pragma unroll
        for (int j = 0; j < 5; ++j) acc[j] = f32x4{0, 0, 0, 0};
        const int q4 = (lane >> 2) & 3, p4 = lane & 3, hd = g4 >> 1, hh = g4 & 1;
        const unsigned mrow = L0 + L_WO + (288 * kh + 20 * hd + 4 * hh + q4) * 144;
        const unsigned rrow = L0 + L_WO + (288 * kh + 72 * g4 + 16 + q4) * 144;
#pragma unroll
        for (int s = 0; s < 9; ++s) {
#pragma unroll
            for (int j = 0; j < 5; ++j) {
                const unsigned col = j < 4 ? j * 32 + p4 * 8 : 128 + (p4 & 1) * 8;
                u32x2 a, c;
                if (s < 8) { a = ldstr(mrow + s * 36 * 144 + col); c = ldstr(mrow + (s * 36 + 8) * 144 + col); }
                else { a = ldstr(rrow + col); c = ldstr(rrow + 36 * 144 + col); }
                acc[j] = mfma16(u32x4{a[0], a[1], c[0], c[1]}, of[s], acc[j]);
            }
        }
        const unsigned sc = L0 + tt * 5120;
        if (kh == 1) {
#pragma unroll
            for (int j = 0; j < 5; ++j) *(LAS f32x4*)(sc + j * 1024 + lane * 16) = acc[j];
        }
        BAR();
        if (kh == 0) {
            unsigned short* op = out + (size_t)(16 * T16 + l15) * 1152 + 72 * bc + 4 * g4;
#pragma unroll
            for (int j = 0; j < 5; ++j) {
                f32x4 v = acc[j] + *(LAS const f32x4*)(sc + j * 1024 + lane * 16);
                if (j < 4 || g4 < 2) *(u32x2*)(op + 16 * j) = u32x2{pk(v[0], v[1]), pk(v[2], v[3])};
            }
        }
    }
    if (b == 0 && w == 7) {
        for (int k = 0; k < 4; ++k) poll_flags((const GAS u64*)(ws + WS_KFLAG) + 64 * k, 64, nonce, lane);
        if (lane == 0) __hip_atomic_store((GAS u64*)ws, nonce, RLX);
    }
    STAMP(11);
}

extern "C" void kernel_launch(void* const* d_in, const int* in_sizes, int n_in, void* d_out, int out_size, void* d_ws, size_t ws_size, hipStream_t stream) {
    fused_kernel<<<256, 512, 0, stream>>>((const unsigned short*)d_in[0], (const unsigned short*)d_in[1], (const unsigned short*)d_in[2], (const unsigned short*)d_in[3],
                                          (const unsigned short*)d_in[4], (const unsigned short*)d_in[5], (const unsigned short*)d_in[6], (const unsigned short*)d_in[7],
                                          (const unsigned short*)d_in[8], (unsigned short*)d_out, (char*)d_ws);
}
```

```cpp
#define PFW 7
#include <hip/hip_runtime.h>
#include <stdint.h>

#define GAS __attribute__((address_space(1)))
#define LAS __attribute__((address_space(3)))
#define DEVI __device__ __forceinline__
typedef unsigned long long u64;
typedef __attribute__((ext_vector_type(8))) __bf16 bf16x8;
typedef __attribute__((ext_vector_type(2))) __bf16 bf16x2;
typedef __attribute__((ext_vector_type(2))) float f32x2;
typedef __attribute__((ext_vector_type(4))) float f32x4;
typedef __attribute__((ext_vector_type(16))) float f32x16;
typedef __attribute__((ext_vector_type(4))) unsigned u32x4;
typedef __attribute__((ext_vector_type(2))) unsigned u32x2;
typedef __attribute__((ext_vector_type(4))) short s16x4;

constexpr int WS_KFLAG = 256, WS_KVFLAG = 4096, WS_OFLAG = 8192, WS_YFLAG = 12288;
constexpr int WS_R0 = 32768;
constexpr int WS_KV = WS_R0 + 2359296;
constexpr int WS_OM = WS_KV + 4849664;
constexpr int WS_OR = WS_OM + 2097152;
constexpr int WS_Y = WS_OR + 262144;
constexpr int WS_END = WS_Y + 2359296;
constexpr int WS_TS = 16 << 20;
constexpr int L_B1K = 131072, L_B1V = 141312;
constexpr int KVB = 18944, HEADB = 9472;
constexpr int L_OWN = 2 * KVB, L_Q = 3 * KVB;
constexpr int L_KC = 66048, KCS = 4160, KCH = 4 * KCS;
constexpr int L_RB = 131072, RCS = 4576, RCH = 2 * RCS;
constexpr int L_WO = 66048;
constexpr int LDS_BYTES = 163840;
constexpr unsigned SPIN_CAP = 1u << 17;

#define STAMP(k) do { } while (0)

DEVI f32x4 mfma16(u32x4 a, u32x4 b, f32x4 c) { return __builtin_amdgcn_mfma_f32_16x16x32_bf16(__builtin_bit_cast(bf16x8, a), __builtin_bit_cast(bf16x8, b), c, 0, 0, 0); }
DEVI f32x16 mfma32(u32x4 a, u32x4 b, f32x16 c) { return __builtin_amdgcn_mfma_f32_32x32x16_bf16(__builtin_bit_cast(bf16x8, a), __builtin_bit_cast(bf16x8, b), c, 0, 0, 0); }
DEVI unsigned pk(float lo, float hi) { f32x2 v = {lo, hi}; return __builtin_bit_cast(unsigned, __builtin_convertvector(v, bf16x2)); }
DEVI float blo(unsigned v) { return __builtin_bit_cast(float, v << 16); }
DEVI float bhi(unsigned v) { return __builtin_bit_cast(float, v & 0xffff0000u); }
DEVI float rndbf(float x) { return blo(pk(x, x)); }
DEVI u32x4 lds128(unsigned a) { return *(LAS const u32x4*)a; }
DEVI u32x2 lds64(unsigned a) { return *(LAS const u32x2*)a; }
DEVI unsigned lds32(unsigned a) { return *(LAS const unsigned*)a; }
DEVI void st128(unsigned a, u32x4 v) { *(LAS u32x4*)a = v; }
DEVI void st64(unsigned a, u32x2 v) { *(LAS u32x2*)a = v; }
DEVI void st32(unsigned a, unsigned v) { *(LAS unsigned*)a = v; }
DEVI void st16(unsigned a, unsigned short v) { *(LAS unsigned short*)a = v; }
DEVI u32x2 ldstr(unsigned a) { return __builtin_bit_cast(u32x2, __builtin_amdgcn_ds_read_tr16_b64_v4i16((LAS s16x4*)a)); }
DEVI void dma16(const void* g, unsigned lds) {
    unsigned sv;
    asm volatile("s_mov_b32 %0, m0\n\ts_mov_b32 m0, %2\n\ts_nop 0\n\tglobal_load_lds_dwordx4 %1, off\n\ts_mov_b32 m0, %0" : "=&s"(sv) : "v"(g), "s"(lds) : "memory");
}
DEVI u32x4 gld128(const void* p) { u32x4 v; asm volatile("global_load_dwordx4 %0, %1, off" : "=v"(v) : "v"(p) : "memory"); return v; }
#define WAITVM(n) asm volatile("s_waitcnt vmcnt(%0)" ::"i"(n) : "memory")
#define TIE(x) asm volatile("" : "+v"(x))
#define BAR() do { asm volatile("s_waitcnt lgkmcnt(0)\n\ts_barrier" ::: "memory"); } while (0)
#define RLX __ATOMIC_RELAXED, __HIP_MEMORY_SCOPE_AGENT

DEVI void poll_flags(const GAS u64* f, int n, u64 nonce, int lane) {
    for (unsigned spins = 0; spins < SPIN_CAP; ++spins) {
        u64 v = nonce;
        if (lane < n) v = __hip_atomic_load(f + lane, RLX);
        if (__all(v == nonce)) break;
        __builtin_amdgcn_s_sleep(2);
    }
    __builtin_amdgcn_fence(__ATOMIC_ACQUIRE, "wavefront");
    asm volatile("" ::: "memory");
}

__global__ __launch_bounds__(512, 2) void fused_kernel(const unsigned short* __restrict__ x, const unsigned short* __restrict__ wqkv, const unsigned short* __restrict__ wo,
                                                       const unsigned short* __restrict__ bq, const unsigned short* __restrict__ bk, const unsigned short* __restrict__ bv,
                                                       const unsigned short* __restrict__ shk, const unsigned short* __restrict__ ssk, const unsigned short* __restrict__ ssd,
                                                       unsigned short* __restrict__ out, char* __restrict__ ws) {
    __shared__ __attribute__((aligned(16))) char smem[LDS_BYTES];
    const unsigned L0 = (unsigned)(uintptr_t)(LAS char*)smem;
    const int tid = threadIdx.x, lane = tid & 63, w = __builtin_amdgcn_readfirstlane(tid >> 6);
    const int b = blockIdx.x, xcd = b & 7, jj = b >> 3;
    const int bi = 4 * (xcd >> 1) + (jj & 3), gp = 8 * (xcd & 1) + (jj >> 2);
    const int br = 4 * xcd + (jj & 3), bc = jj >> 2;
    const int l15 = lane & 15, g4 = lane >> 4;
    const __amdgpu_buffer_rsrc_t wsr = __builtin_amdgcn_make_buffer_rsrc(ws, 0, WS_END, 0x00020000);
    STAMP(0);
    unsigned pfd[3] = {0, 0, 0};
    {
        u64 pc; asm volatile("s_getpc_b64 %0" : "=s"(pc));
        if (w == PFW) {
#pragma unroll
            for (int k = 0; k < 3; ++k) asm volatile("global_load_dword %0, %1, off" : "=v"(pfd[k]) : "v"((const char*)pc + (k * 64 + lane) * 128) : "memory");
        }
    }

    u32x2 ep; asm volatile("global_load_dwordx2 %0, %1, off sc1" : "=v"(ep) : "v"(ws) : "memory");
    const int nch = (b & 3) == 0 ? 3 : 2;
    const int cu = tid >> 8, cq = tid & 255;
    const int ch01 = 2 * b + cu, ch2 = 512 + (b >> 2);
    u32x4 ka0, ka1, kb0, kb1;
    {
        const char* p = (const char*)ssk + ch01 * 4096 + cq * 16;
        ka0 = gld128(p); ka1 = gld128(p + (cq < 255 ? 16 : 0));
        const char* p2 = (const char*)ssk + ch2 * 4096 + cq * 16;
        kb0 = gld128(p2); kb1 = gld128(p2 + (cq < 255 ? 16 : 0));
    }
    const int cp = tid >> 3, oct = tid & 7;
    const int chB = 72 * bc + 2 * (cp < 36 ? cp : 35);
    u32x4 shv = gld128((const char*)shk + chB * 8);
    for (int u = w; u < 19; u += 8) {
        if (u < 10) {
            int q = u * 64 + lane; q = q < 612 ? q : 611;
            int row = q / 9, cc = q - row * 9; int tok = 64 * br - 4 + row; tok = tok < 0 ? 0 : tok;
            dma16((const char*)bk + tok * 1152 + bc * 144 + cc * 16, L0 + L_B1K + u * 1024);
        } else {
            int q = (u - 10) * 64 + lane; int row = q / 9, cc = q - row * 9;
            dma16((const char*)bv + (64 * br + row) * 1152 + bc * 144 + cc * 16, L0 + L_B1V + (u - 10) * 1024);
        }
    }
    const char* xsrc = (const char*)x + (size_t)(128 * bi + 16 * w + (lane >> 3)) * 1152 + (((lane & 7) ^ (lane >> 3)) * 16);
    const char* wsrc;
    {
        int swz = 2 * (g4 | ((w & 1) << 2));
        int lc = l15 ^ swz; lc = lc < 15 ? lc : 14;
        int sec = lc / 5, cc = lc - sec * 5;
        wsrc = (const char*)wqkv + (size_t)(8 * w + g4) * 3456 + sec * 1152 + 72 * gp - (gp & 1) * 8 + cc * 16;
    }
    auto a1_issue = [&](int s) {
        const unsigned sl = L0 + (s & 3) * 32768;
        dma16(xsrc + s * 128, sl + (16 * w) * 128);
        dma16(xsrc + s * 128 + 8 * 1152, sl + (16 * w + 8) * 128);
        dma16(wsrc + (size_t)s * 64 * 3456, sl + 16384 + (8 * w) * 256);
        dma16(wsrc + (size_t)s * 64 * 3456 + 4 * 3456, sl + 16384 + (8 * w + 4) * 256);
    };
    a1_issue(0); a1_issue(1); a1_issue(2); a1_issue(3);

    WAITVM(16);
    TIE(ka0); TIE(ka1); TIE(kb0); TIE(kb1); TIE(shv); TIE(ep); TIE(pfd[0]); TIE(pfd[1]); TIE(pfd[2]);
    const u64 nonce = (((u64)(unsigned)__builtin_amdgcn_readfirstlane(ep[1]) << 32) | (unsigned)__builtin_amdgcn_readfirstlane(ep[0])) + 1;
    BAR();
    STAMP(1);
    unsigned kvp[8];
    if (tid < 288) {
        float k0[11], k1[11];
#pragma unroll
        for (int q = 0; q < 11; ++q) { unsigned v = lds32(L0 + L_B1K + (8 * oct + 1 + q) * 144 + cp * 4); k0[q] = blo(v); k1[q] = bhi(v); }
        if (br == 0 && oct == 0) { k0[0] = k0[1] = k0[2] = 0.f; k1[0] = k1[1] = k1[2] = 0.f; }
        const float s00 = blo(shv[0]), s01 = bhi(shv[0]), s02 = blo(shv[1]), s03 = bhi(shv[1]);
        const float s10 = blo(shv[2]), s11 = bhi(shv[2]), s12 = blo(shv[3]), s13 = bhi(shv[3]);
#pragma unroll
        for (int t = 0; t < 8; ++t) {
            unsigned v = lds32(L0 + L_B1V + (8 * oct + t) * 144 + cp * 4);
            float a0 = k0[t] * s03; a0 = fmaf(k0[t + 1], s02, a0); a0 = fmaf(k0[t + 2], s01, a0); a0 = fmaf(k0[t + 3], s00, a0);
            float a1 = k1[t] * s13; a1 = fmaf(k1[t + 1], s12, a1); a1 = fmaf(k1[t + 2], s11, a1); a1 = fmaf(k1[t + 3], s10, a1);
            unsigned ks = pk(a0, a1);
            kvp[t] = pk(blo(ks) * blo(v), bhi(ks) * bhi(v));
        }
        u32x4 r0, r1;
#pragma unroll
        for (int d = 0; d < 4; ++d) {
            r0[d] = (kvp[7 - 2 * d] & 0xffffu) | (kvp[6 - 2 * d] << 16);
            r1[d] = (kvp[7 - 2 * d] >> 16) | (kvp[6 - 2 * d] & 0xffff0000u);
        }
        const int off = WS_R0 + chB * 4096 + (2040 - 64 * br - 8 * oct) * 2;
        __builtin_amdgcn_raw_buffer_store_b128(r0, wsr, off, 0, 16);
        __builtin_amdgcn_raw_buffer_store_b128(r1, wsr, off + 4096, 0, 16);
    } else {
#pragma unroll
        for (int t = 0; t < 8; ++t) kvp[t] = 0;
    }
    STAMP(2);

    const int tg = w & 3, nh = w >> 2;
    f32x4 aq[2], ak[2], av[2], ar[2];
#pragma unroll
    for (int t = 0; t < 2; ++t) { aq[t] = f32x4{0, 0, 0, 0}; ak[t] = aq[t]; av[t] = aq[t]; ar[t] = aq[t]; }
    {
        const int q4 = (lane >> 2) & 3, p4 = lane & 3, po = (gp & 1) * 8;
        const int swz = 2 * (q4 | ((g4 & 1) << 2));
        auto woff = [&](int sec, int piece) { int byte = sec * 80 + po + 8 * piece; return (unsigned)(16384 + (8 * g4 + q4) * 256 + (((byte >> 4) ^ swz) * 16) + (byte & 8)); };
        const unsigned wq = woff(0, 5 * nh + p4), wk = woff(1, 5 * nh + p4), wv = woff(2, 5 * nh + p4), wr = woff(p4 < 3 ? p4 : 2, 4);
        const unsigned xo = (32 * tg + l15) * 128;
        const unsigned xs0 = ((g4 ^ (lane & 7)) * 16), xs1 = (((4 + g4) ^ (lane & 7)) * 16);
#pragma unroll
        for (int s = 0; s < 9; ++s) {
            if (s == 0) WAITVM(12); else if (s < 7) WAITVM(8); else if (s == 7) WAITVM(4); else WAITVM(0);
            BAR();
            if (s >= 1 && s + 3 <= 8) a1_issue(s + 3);
            if (s == 3 && tid == 0) __hip_atomic_store((GAS u64*)(ws + WS_KFLAG) + bc * 32 + br, nonce, RLX);
            const unsigned sl = L0 + (s & 3) * 32768;
#pragma unroll
            for (int kk = 0; kk < 2; ++kk) {
                u32x4 xf[2];
                xf[0] = lds128(sl + xo + (kk ? xs1 : xs0));
                xf[1] = lds128(sl + xo + 2048 + (kk ? xs1 : xs0));
                u32x2 a, c;
                u32x4 fq, fk, fv;
                a = ldstr(sl + wq + kk * 8192); c = ldstr(sl + wq + kk * 8192 + 1024); fq = u32x4{a[0], a[1], c[0], c[1]};
                a = ldstr(sl + wk + kk * 8192); c = ldstr(sl + wk + kk * 8192 + 1024); fk = u32x4{a[0], a[1], c[0], c[1]};
                a = ldstr(sl + wv + kk * 8192); c = ldstr(sl + wv + kk * 8192 + 1024); fv = u32x4{a[0], a[1], c[0], c[1]};
#pragma unroll
                for (int t = 0; t < 2; ++t) { aq[t] = mfma16(fq, xf[t], aq[t]); ak[t] = mfma16(fk, xf[t], ak[t]); av[t] = mfma16(xf[t], fv, av[t]); }
                if (nh == 0) {
                    a = ldstr(sl + wr + kk * 8192); c = ldstr(sl + wr + kk * 8192 + 1024);
                    u32x4 fr = u32x4{a[0], a[1], c[0], c[1]};
#pragma unroll
                    for (int t = 0; t < 2; ++t) ar[t] = mfma16(fr, xf[t], ar[t]);
                }
            }
        }
    }
    STAMP(3);
    BAR();
    {
        const float qs = 0.34004634f;
#pragma unroll
        for (int t = 0; t < 2; ++t) {
            const unsigned ko = tg * 1024 + (g4 >> 1) * 512 + (16 * t + l15) * 16 + (g4 & 1) * 8;
            st64(L0 + L_OWN + nh * HEADB + ko, u32x2{pk(ak[t][0], ak[t][1]), pk(ak[t][2], ak[t][3])});
            st64(L0 + L_Q + nh * 4608 + ko, u32x2{pk(aq[t][0] * qs, aq[t][1] * qs), pk(aq[t][2] * qs, aq[t][3] * qs)});
            const int tt = 2 * tg + t;
            st64(L0 + L_OWN + nh * HEADB + 4608 + tt * 608 + (g4 & 1) * 304 + l15 * 16 + (g4 >> 1) * 8, u32x2{pk(av[t][0], av[t][1]), pk(av[t][2], av[t][3])});
            if (nh == 0) {
                const int tok = 32 * tg + 16 * t + l15;
                if (g4 == 0) {
                    st32(L0 + L_Q + 4096 + tok * 4, pk(ar[t][0] * qs, ar[t][1] * qs));
                    st32(L0 + L_Q + 4608 + 4096 + tok * 4, pk(ar[t][2] * qs, ar[t][3] * qs));
                } else if (g4 == 1) {
                    st32(L0 + L_OWN + 4096 + tok * 4, pk(ar[t][0], ar[t][1]));
                    st32(L0 + L_OWN + HEADB + 4096 + tok * 4, pk(ar[t][2], ar[t][3]));
                } else if (g4 == 2) {
                    const unsigned p01 = pk(ar[t][0], ar[t][1]), p23 = pk(ar[t][2], ar[t][3]);
                    const unsigned vb = L0 + L_OWN + 4608 + tt * 608 + ((l15 >> 2) & 1) * 304 + 16 * 16 + (4 * (l15 >> 3) + (l15 & 3)) * 2;
                    st16(vb, (unsigned short)p01); st16(vb + 16, (unsigned short)(p01 >> 16));
                    st16(vb + HEADB, (unsigned short)p23); st16(vb + HEADB + 16, (unsigned short)(p23 >> 16));
                }
            } else if (lane < 4) {
                st128(L0 + L_OWN + (lane >> 1) * HEADB + 4608 + tt * 608 + (lane & 1) * 304 + 18 * 16, u32x4{0x3f803f80u, 0x3f803f80u, 0x3f803f80u, 0x3f803f80u});
            }
        }
    }
    {
        auto kcopy = [&](unsigned kb, u32x4 d0, u32x4 d1) {
            st128(kb + (4 + cq) * 16, d0);
            st128(kb + KCS + (4 + cq) * 16, u32x4{d0[1], d0[2], d0[3], d1[0]});
            st128(kb + 2 * KCS + (4 + cq) * 16, u32x4{d0[2], d0[3], d1[0], d1[1]});
            st128(kb + 3 * KCS + (4 + cq) * 16, u32x4{d0[3], d1[0], d1[1], d1[2]});
            if (cq == 0) {
                st128(kb + 3 * 16, u32x4{0, 0, 0, 0});
                st128(kb + KCS + 3 * 16, u32x4{0, 0, 0, d0[0]});
                st128(kb + 2 * KCS + 3 * 16, u32x4{0, 0, d0[0], d0[1]});
                st128(kb + 3 * KCS + 3 * 16, u32x4{0, d0[0], d0[1], d0[2]});
            } else if (cq < 4) {
#pragma unroll
                for (int c = 0; c < 4; ++c) st128(kb + c * KCS + (cq - 1) * 16, u32x4{0, 0, 0, 0});
            }
        };
        kcopy(L0 + L_KC + cu * KCH, ka0, ka1);
        if (nch == 3 && tid < 256) kcopy(L0 + L_KC + 2 * KCH, kb0, kb1);
    }
    BAR();
    {
        const int gb = WS_KV + (bi * 16 + gp) * KVB;
#pragma unroll
        for (int k = 0; k < 3; ++k) {
            const int q = tid + 512 * k;
            if (q < 1184) __builtin_amdgcn_raw_buffer_store_b128(lds128(L0 + L_OWN + q * 16), wsr, gb + q * 16, 0, 16);
        }
    }
    if (w == 0) {
        poll_flags((const GAS u64*)(ws + WS_KFLAG) + ((2 * b) / 72) * 32, 32, nonce, lane);
        if (nch == 3) poll_flags((const GAS u64*)(ws + WS_KFLAG) + (ch2 / 72) * 32, 32, nonce, lane);
    }
    WAITVM(0);
    BAR();
    if (tid == 0) __hip_atomic_store((GAS u64*)(ws + WS_KVFLAG) + gp * 16 + bi, nonce, RLX);
    STAMP(4);
    {
        auto rcopy = [&](unsigned rb, int ch) {
            const int off = WS_R0 + ch * 4096 + cq * 16;
            u32x4 d = __builtin_amdgcn_raw_buffer_load_b128(wsr, off, 0, 16);
            unsigned nx = 0;
            if (cq < 255) nx = __builtin_amdgcn_raw_buffer_load_b32(wsr, off + 16, 0, 16);
            st128(rb + cq * 16, d);
            st128(rb + RCS + cq * 16, u32x4{__builtin_amdgcn_alignbit(d[1], d[0], 16), __builtin_amdgcn_alignbit(d[2], d[1], 16), __builtin_amdgcn_alignbit(d[3], d[2], 16), __builtin_amdgcn_alignbit(nx, d[3], 16)});
            if (cq < 30) { st128(rb + (256 + cq) * 16, u32x4{0, 0, 0, 0}); st128(rb + RCS + (256 + cq) * 16, u32x4{0, 0, 0, 0}); }
        };
        rcopy(L0 + L_RB + cu * RCH, ch01);
        if (nch == 3 && tid < 256) rcopy(L0 + L_RB + 2 * RCH, ch2);
    }
    BAR();
    STAMP(5);

    {
        const int slot = w < 4 ? (w >> 1) : 2;
        const int rho0 = w < 4 ? 4 * (w & 1) : 2 * (w - 4);
        const int nrho = w < 4 ? 4 : (nch == 3 ? 2 : 0);
        f32x4 acc[8];
#pragma unroll
        for (int n = 0; n < 8; ++n) acc[n] = f32x4{0, 0, 0, 0};
        const unsigned la = L0 + L_KC + slot * KCH + ((l15 + 1) & 3) * KCS + (g4 + ((l15 + 1) >> 2)) * 16;
        const unsigned lb = L0 + L_RB + slot * RCH + ((l15 & 1) ? 0 : RCS) + (252 - 28 * 8 - 4 * (l15 >> 1) + g4) * 16;
        for (int rr = 0; rr < nrho; ++rr) {
            const unsigned ro = (rho0 + rr) * 64;
            u32x4 A[8], B[8];
#pragma unroll
            for (int m = 0; m < 8; ++m) { A[m] = lds128(la + ro + m * 512); B[m] = lds128(lb + ro + (7 - m) * 512); }
#pragma unroll
            for (int nu = 0; nu < 8; ++nu)
#pragma unroll
                for (int mu = 0; mu + nu < 8; ++mu) acc[mu + nu] = mfma16(A[mu], B[nu], acc[mu + nu]);
        }
        BAR();
        const unsigned sc = L0 + L_KC + slot * KCH;
        if (w < 4) {
            const int h = w & 1;
            if (h) {
#pragma unroll
                for (int n = 0; n < 4; ++n) *(LAS f32x4*)(sc + 4096 + n * 1024 + lane * 16) = acc[n];
            } else {
#pragma unroll
                for (int n = 0; n < 4; ++n) *(LAS f32x4*)(sc + n * 1024 + lane * 16) = acc[4 + n];
            }
        } else if (nch == 3) {
            const int h = w - 4;
#pragma unroll
            for (int n = 0; n < 4; ++n) *(LAS f32x4*)(sc + h * 4096 + n * 1024 + lane * 16) = acc[n];
        }
        BAR();
        f32x4 y[4];
        if (w < 4) {
            const int h = w & 1;
            if (h) {
#pragma unroll
                for (int n = 0; n < 4; ++n) y[n] = acc[4 + n] + *(LAS const f32x4*)(sc + n * 1024 + lane * 16);
            } else {
#pragma unroll
                for (int n = 0; n < 4; ++n) y[n] = acc[n] + *(LAS const f32x4*)(sc + 4096 + n * 1024 + lane * 16);
            }
        } else if (nch == 3) {
            const int h = w - 4;
            f32x4 s4 = f32x4{0, 0, 0, 0};
#pragma unroll
            for (int q = 0; q < 4; ++q) s4 += *(LAS const f32x4*)(sc + q * 4096 + h * 1024 + lane * 16);
            y[0] = s4;
        }
        BAR();
        if (w >= 4 && nch == 3) {
            const int h = w - 4;
#pragma unroll
            for (int n = 0; n < 4; ++n) *(LAS f32x4*)(sc + h * 4096 + n * 1024 + lane * 16) = acc[4 + n];
        }
        BAR();
        if (w >= 4 && nch == 3) {
            const int h = w - 4;
            f32x4 s4 = f32x4{0, 0, 0, 0};
#pragma unroll
            for (int q = 0; q < 4; ++q) s4 += *(LAS const f32x4*)(sc + q * 4096 + h * 1024 + lane * 16);
            y[1] = s4;
        }
        BAR();
        const unsigned ys = L0 + L_RB + slot * RCH;
        const unsigned yo = (l15 >> 1) * 64 + g4 * 16 + (l15 & 1) * 8;
        const int chs = slot == 2 ? ch2 : 2 * b + slot;
        if (w < 4) {
            const int h = w & 1;
#pragma unroll
            for (int n = 0; n < 4; ++n) st64(ys + (4 * h + n) * 512 + yo, u32x2{pk(y[n][0], y[n][1]), pk(y[n][2], y[n][3])});
#pragma unroll
            for (int k = 0; k < 2; ++k)
                __builtin_amdgcn_raw_buffer_store_b128(lds128(ys + h * 2048 + k * 1024 + lane * 16), wsr, WS_Y + chs * 4096 + h * 2048 + k * 1024 + lane * 16, 0, 16);
        } else if (nch == 3) {
            const int h = w - 4;
            st64(ys + h * 512 + yo, u32x2{pk(y[0][0], y[0][1]), pk(y[0][2], y[0][3])});
            st64(ys + (4 + h) * 512 + yo, u32x2{pk(y[1][0], y[1][1]), pk(y[1][2], y[1][3])});
            if (lane < 32) __builtin_amdgcn_raw_buffer_store_b128(lds128(ys + h * 512 + lane * 16), wsr, WS_Y + chs * 4096 + h * 512 + lane * 16, 0, 16);
            else __builtin_amdgcn_raw_buffer_store_b128(lds128(ys + (4 + h) * 512 + (lane - 32) * 16), wsr, WS_Y + chs * 4096 + (4 + h) * 512 + (lane - 32) * 16, 0, 16);
        }
    }
    if (w == 0 && bi > 0) poll_flags((const GAS u64*)(ws + WS_KVFLAG) + gp * 16 + (bi > 1 ? bi - 2 : 0), bi > 1 ? 2 : 1, nonce, lane);
    WAITVM(0);
    BAR();
    if (tid < nch) __hip_atomic_store((GAS u64*)(ws + WS_YFLAG) + (tid == 2 ? ch2 : 2 * b + tid), nonce, RLX);
    STAMP(6);
    {
        for (int hb = 0; hb < 2; ++hb) {
            const int ib = bi - 2 + hb;
            if (ib >= 0) {
                const int gb = WS_KV + (ib * 16 + gp) * KVB;
#pragma unroll
                for (int k = 0; k < 3; ++k) {
                    const int q = tid + 512 * k;
                    if (q < 1184) st128(L0 + hb * KVB + q * 16, __builtin_amdgcn_raw_buffer_load_b128(wsr, gb + q * 16, 0, 16));
                }
            }
        }
    }
    for (int u = w; u < 81; u += 8) {
        const int q = u * 64 + lane, row = q / 9, cc = q - row * 9;
        dma16((const char*)wo + row * 1152 + bc * 144 + cc * 16, L0 + L_WO + u * 1024);
    }
    BAR();
    STAMP(7);

    {
        const int hd = w >> 2, qt = w & 3, l31 = lane & 31, hh = lane >> 5;
        const u32x4 qm = lds128(L0 + L_Q + hd * 4608 + qt * 1024 + hh * 512 + l31 * 16);
        const unsigned qrv = lds32(L0 + L_Q + hd * 4608 + 4096 + (32 * qt + l31) * 4);
        const u32x4 qr = u32x4{hh ? 0u : qrv, 0, 0, 0};
        const int vrow = l31 < 19 ? l31 : (l31 == 19 ? 18 : (l31 < 28 ? 0 : 4));
        const unsigned kbase = L0 + hd * HEADB + hh * 512 + l31 * 16, krbase = L0 + hd * HEADB + 4096 + l31 * 4;
        const unsigned vbase = L0 + hd * HEADB + 4608 + hh * 304 + vrow * 16;
        const int qq = l31 - 4 * hh;
        const int t_first = bi >= 2 ? qt : (bi == 1 ? 4 : 8), t_last = 8 + qt;
        const bool has_first = bi >= 2;
        auto qk = [&](int t, f32x16 c) {
            const unsigned bo = (t >> 2) * KVB;
            u32x4 km = lds128(kbase + bo + (t & 3) * 1024);
            unsigned kr = lds32(krbase + bo + (t & 3) * 128);
            c = mfma32(km, qm, c);
            return mfma32(u32x4{kr, 0, 0, 0}, qr, c);
        };
        auto pv = [&](int t, const f32x16& p, f32x16 o) {
            const unsigned bo = (t >> 2) * KVB + (t & 3) * 1216;
            u32x4 v0 = lds128(vbase + bo), v1 = lds128(vbase + bo + 608);
            u32x4 p0 = u32x4{pk(p[0], p[1]), pk(p[2], p[3]), pk(p[4], p[5]), pk(p[6], p[7])};
            u32x4 p1 = u32x4{pk(p[8], p[9]), pk(p[10], p[11]), pk(p[12], p[13]), pk(p[14], p[15])};
            o = mfma32(v0, p0, o);
            return mfma32(v1, p1, o);
        };
        const float NINF = -__builtin_inff();
        f32x16 z16;
#pragma unroll
        for (int r = 0; r < 16; ++r) z16[r] = 0.f;
        f32x16 sd = qk(t_last, z16);
        float m = NINF;
#pragma unroll
        for (int r = 0; r < 16; ++r) { const int c = (r & 3) + 8 * (r >> 2); sd[r] = c <= qq ? sd[r] : NINF; m = fmaxf(m, sd[r]); }
        m = fmaxf(m, __shfl_xor(m, 32));
        f32x16 o;
        float l;
        for (int attempt = 0; attempt < 2; ++attempt) {
            f32x16 nm, p;
#pragma unroll
            for (int r = 0; r < 16; ++r) { nm[r] = -m; p[r] = __builtin_amdgcn_exp2f(sd[r] - m); }
            o = pv(t_last, p, z16);
            for (int t = t_first; t < t_last; ++t) {
                f32x16 s = qk(t, nm);
                if (has_first && t == t_first) {
#pragma unroll
                    for (int r = 0; r < 16; ++r) { const int c = (r & 3) + 8 * (r >> 2); s[r] = c > qq ? s[r] : NINF; }
                }
#pragma unroll
                for (int r = 0; r < 16; ++r) p[r] = __builtin_amdgcn_exp2f(s[r]);
                o = pv(t, p, o);
            }
            l = __shfl(o[10], l31);
            if (attempt == 1 || __all(l <= 1048576.f)) break;
            for (int t = t_first; t < t_last; ++t) {
                f32x16 s = qk(t, z16);
#pragma unroll
                for (int r = 0; r < 16; ++r) { const int c = (r & 3) + 8 * (r >> 2); const bool keep = !(has_first && t == t_first) || c > qq; m = fmaxf(m, keep ? s[r] : NINF); }
            }
            m = fmaxf(m, __shfl_xor(m, 32));
        }
        const float il = 1.f / l;
        const int T16 = 8 * bi + 2 * qt + (l31 >> 4);
        u32x4 om = u32x4{pk(o[0] * il, o[1] * il), pk(o[2] * il, o[3] * il), pk(o[4] * il, o[5] * il), pk(o[6] * il, o[7] * il)};
        __builtin_amdgcn_raw_buffer_store_b128(om, wsr, WS_OM + ((T16 * 16 + gp) * 64 + (2 * hd + hh) * 16 + (l31 & 15)) * 16, 0, 16);
        if (hh == 0) __builtin_amdgcn_raw_buffer_store_b32(pk(o[8] * il, o[9] * il), wsr, WS_OR + (((T16 * 16 + gp) * 2 + hd) * 16 + (l31 & 15)) * 4, 0, 16);
    }
    WAITVM(0);
    BAR();
    if (tid == 0) __hip_atomic_store((GAS u64*)(ws + WS_OFLAG) + bi * 16 + gp, nonce, RLX);
    STAMP(8);

    if (w == 0) { poll_flags((const GAS u64*)(ws + WS_YFLAG) + 72 * bc, 64, nonce, lane); poll_flags((const GAS u64*)(ws + WS_YFLAG) + 72 * bc + 64, 8, nonce, lane); }
    BAR();
    if (tid < 288) {
        const int t0 = 64 * br + 8 * oct;
        u32x4 y0 = __builtin_amdgcn_raw_buffer_load_b128(wsr, WS_Y + chB * 4096 + t0 * 2, 0, 16);
        u32x4 y1 = __builtin_amdgcn_raw_buffer_load_b128(wsr, WS_Y + (chB + 1) * 4096 + t0 * 2, 0, 16);
        const unsigned dv = *(const unsigned*)(ssd + chB);
        const float D0 = blo(dv), D1 = bhi(dv);
#pragma unroll
        for (int t = 0; t < 8; ++t) {
            const int slot = (t & 1) * 4 + (t >> 1);
            const float ya = (slot & 1) ? bhi(y0[slot >> 1]) : blo(y0[slot >> 1]);
            const float yb = (slot & 1) ? bhi(y1[slot >> 1]) : blo(y1[slot >> 1]);
            const unsigned qv = *(const unsigned*)(bq + (size_t)(t0 + t) * 576 + chB);
            const unsigned kd = pk(blo(kvp[t]) * D0, bhi(kvp[t]) * D1);
            const unsigned zz = pk(ya + blo(kd), yb + bhi(kd));
            *(unsigned*)(out + (size_t)(t0 + t) * 1152 + 576 + chB) = pk(blo(qv) * blo(zz), bhi(qv) * bhi(zz));
        }
    }
    STAMP(9);

    if (w == 0) poll_flags((const GAS u64*)(ws + WS_OFLAG) + (br >> 1) * 16, 16, nonce, lane);
    WAITVM(0);
    BAR();
    STAMP(10);
    {
        const int tt = w & 3, kh = w >> 2, T16 = 4 * br + tt;
        u32x4 of[9];
#pragma unroll
        for (int s = 0; s < 8; ++s) of[s] = __builtin_amdgcn_raw_buffer_load_b128(wsr, WS_OM + ((T16 * 16 + 8 * kh + s) * 64 + lane) * 16, 0, 16);
#pragma unroll
        for (int d = 0; d < 4; ++d) of[8][d] = __builtin_amdgcn_raw_buffer_load_b32(wsr, WS_OR + (((T16 * 16 + 8 * kh + 2 * g4 + (d >> 1)) * 2 + (d & 1)) * 16 + l15) * 4, 0, 16);
        f32x4 acc[5];
#pragma unroll
        for (int j = 0; j < 5; ++j) acc[j] = f32x4{0, 0, 0, 0};
        const int q4 = (lane >> 2) & 3, p4 = lane & 3, hd = g4 >> 1, hh = g4 & 1;
        const unsigned mrow = L0 + L_WO + (288 * kh + 20 * hd + 4 * hh + q4) * 144;
        const unsigned rrow = L0 + L_WO + (288 * kh + 72 * g4 + 16 + q4) * 144;
#pragma unroll
        for (int s = 0; s < 9; ++s) {
#pragma unroll
            for (int j = 0; j < 5; ++j) {
                const unsigned col = j < 4 ? j * 32 + p4 * 8 : 128 + (p4 & 1) * 8;
                u32x2 a, c;
                if (s < 8) { a = ldstr(mrow + s * 36 * 144 + col); c = ldstr(mrow + (s * 36 + 8) * 144 + col); }
                else { a = ldstr(rrow + col); c = ldstr(rrow + 36 * 144 + col); }
                acc[j] = mfma16(u32x4{a[0], a[1], c[0], c[1]}, of[s], acc[j]);
            }
        }
        const unsigned sc = L0 + tt * 5120;
        if (kh == 1) {
#pragma unroll
            for (int j = 0; j < 5; ++j) *(LAS f32x4*)(sc + j * 1024 + lane * 16) = acc[j];
        }
        BAR();
        if (kh == 0) {
            unsigned short* op = out + (size_t)(16 * T16 + l15) * 1152 + 72 * bc + 4 * g4;
#pragma unroll
            for (int j = 0; j < 5; ++j) {
                f32x4 v = acc[j] + *(LAS const f32x4*)(sc + j * 1024 + lane * 16);
                if (j < 4 || g4 < 2) *(u32x2*)(op + 16 * j) = u32x2{pk(v[0], v[1]), pk(v[2], v[3])};
            }
        }
    }
    if (b == 0 && w == 7) {
        for (int k = 0; k < 4; ++k) poll_flags((const GAS u64*)(ws + WS_KFLAG) + 64 * k, 64, nonce, lane);
        if (lane == 0) __hip_atomic_store((GAS u64*)ws, nonce, RLX);
    }
    STAMP(11);
}

extern "C" void kernel_launch(void* const* d_in, const int* in_sizes, int n_in, void* d_out, int out_size, void* d_ws, size_t ws_size, hipStream_t stream) {
    fused_kernel<<<256, 512, 0, stream>>>((const unsigned short*)d_in[0], (const unsigned short*)d_in[1], (const unsigned short*)d_in[2], (const unsigned short*)d_in[3],
                                          (const unsigned short*)d_in[4], (const unsigned short*)d_in[5], (const unsigned short*)d_in[6], (const unsigned short*)d_in[7],
                                          (const unsigned short*)d_in[8], (unsigned short*)d_out, (char*)d_ws);
}
```

```cpp
#define PROBE 1
#include <hip/hip_runtime.h>
#include <stdint.h>

#define GAS __attribute__((address_space(1)))
#define LAS __attribute__((address_space(3)))
#define DEVI __device__ __forceinline__
typedef unsigned long long u64;
typedef __attribute__((ext_vector_type(8))) __bf16 bf16x8;
typedef __attribute__((ext_vector_type(2))) __bf16 bf16x2;
typedef __attribute__((ext_vector_type(2))) float f32x2;
typedef __attribute__((ext_vector_type(4))) float f32x4;
typedef __attribute__((ext_vector_type(16))) float f32x16;
typedef __attribute__((ext_vector_type(4))) unsigned u32x4;
typedef __attribute__((ext_vector_type(2))) unsigned u32x2;
typedef __attribute__((ext_vector_type(4))) short s16x4;

constexpr int WS_KFLAG = 256, WS_KVFLAG = 4096, WS_OFLAG = 8192, WS_YFLAG = 12288;
constexpr int WS_R0 = 32768;
constexpr int WS_KV = WS_R0 + 2359296;
constexpr int WS_OM = WS_KV + 4849664;
constexpr int WS_OR = WS_OM + 2097152;
constexpr int WS_Y = WS_OR + 262144;
constexpr int WS_END = WS_Y + 2359296;
constexpr int WS_TS = 16 << 20;
constexpr int L_B1K = 131072, L_B1V = 141312;
constexpr int KVB = 18944, HEADB = 9472;
constexpr int L_OWN = 2 * KVB, L_Q = 3 * KVB;
constexpr int L_KC = 66048, KCS = 4160, KCH = 4 * KCS;
constexpr int L_RB = 131072, RCS = 4576, RCH = 2 * RCS;
constexpr int L_WO = 66048;
constexpr int LDS_BYTES = 163840;
constexpr unsigned SPIN_CAP = 1u << 17;

#define STAMP(k) do { if ((k) == 0) probe0 = __builtin_amdgcn_s_memrealtime(); if ((k) == PROBE) probe1 = __builtin_amdgcn_s_memrealtime(); } while (0)

DEVI f32x4 mfma16(u32x4 a, u32x4 b, f32x4 c) { return __builtin_amdgcn_mfma_f32_16x16x32_bf16(__builtin_bit_cast(bf16x8, a), __builtin_bit_cast(bf16x8, b), c, 0, 0, 0); }
DEVI f32x16 mfma32(u32x4 a, u32x4 b, f32x16 c) { return __builtin_amdgcn_mfma_f32_32x32x16_bf16(__builtin_bit_cast(bf16x8, a), __builtin_bit_cast(bf16x8, b), c, 0, 0, 0); }
DEVI unsigned pk(float lo, float hi) { f32x2 v = {lo, hi}; return __builtin_bit_cast(unsigned, __builtin_convertvector(v, bf16x2)); }
DEVI float blo(unsigned v) { return __builtin_bit_cast(float, v << 16); }
DEVI float bhi(unsigned v) { return __builtin_bit_cast(float, v & 0xffff0000u); }
DEVI float rndbf(float x) { return blo(pk(x, x)); }
DEVI u32x4 lds128(unsigned a) { return *(LAS const u32x4*)a; }
DEVI u32x2 lds64(unsigned a) { return *(LAS const u32x2*)a; }
DEVI unsigned lds32(unsigned a) { return *(LAS const unsigned*)a; }
DEVI void st128(unsigned a, u32x4 v) { *(LAS u32x4*)a = v; }
DEVI void st64(unsigned a, u32x2 v) { *(LAS u32x2*)a = v; }
DEVI void st32(unsigned a, unsigned v) { *(LAS unsigned*)a = v; }
DEVI void st16(unsigned a, unsigned short v) { *(LAS unsigned short*)a = v; }
DEVI u32x2 ldstr(unsigned a) { return __builtin_bit_cast(u32x2, __builtin_amdgcn_ds_read_tr16_b64_v4i16((LAS s16x4*)a)); }
DEVI void dma16(const void* g, unsigned lds) {
    unsigned sv;
    asm volatile("s_mov_b32 %0, m0\n\ts_mov_b32 m0, %2\n\ts_nop 0\n\tglobal_load_lds_dwordx4 %1, off\n\ts_mov_b32 m0, %0" : "=&s"(sv) : "v"(g), "s"(lds) : "memory");
}
DEVI u32x4 gld128(const void* p) { u32x4 v; asm volatile("global_load_dwordx4 %0, %1, off" : "=v"(v) : "v"(p) : "memory"); return v; }
#define WAITVM(n) asm volatile("s_waitcnt vmcnt(%0)" ::"i"(n) : "memory")
#define TIE(x) asm volatile("" : "+v"(x))
#define BAR() do { asm volatile("s_waitcnt lgkmcnt(0)\n\ts_barrier" ::: "memory"); } while (0)
#define RLX __ATOMIC_RELAXED, __HIP_MEMORY_SCOPE_AGENT

DEVI void poll_flags(const GAS u64* f, int n, u64 nonce, int lane) {
    for (unsigned spins = 0; spins < SPIN_CAP; ++spins) {
        u64 v = nonce;
        if (lane < n) v = __hip_atomic_load(f + lane, RLX);
        if (__all(v == nonce)) break;
        __builtin_amdgcn_s_sleep(2);
    }
    __builtin_amdgcn_fence(__ATOMIC_ACQUIRE, "wavefront");
    asm volatile("" ::: "memory");
}

__global__ __launch_bounds__(512, 2) void fused_kernel(const unsigned short* __restrict__ x, const unsigned short* __restrict__ wqkv, const unsigned short* __restrict__ wo,
                                                       const unsigned short* __restrict__ bq, const unsigned short* __restrict__ bk, const unsigned short* __restrict__ bv,
                                                       const unsigned short* __restrict__ shk, const unsigned short* __restrict__ ssk, const unsigned short* __restrict__ ssd,
                                                       unsigned short* __restrict__ out, char* __restrict__ ws) {
    __shared__ __attribute__((aligned(16))) char smem[LDS_BYTES];
    const unsigned L0 = (unsigned)(uintptr_t)(LAS char*)smem;
    u64 probe0 = 0, probe1 = 0;
    const int tid = threadIdx.x, lane = tid & 63, w = __builtin_amdgcn_readfirstlane(tid >> 6);
    const int b = blockIdx.x, xcd = b & 7, jj = b >> 3;
    const int bi = 4 * (xcd >> 1) + (jj & 3), gp = 8 * (xcd & 1) + (jj >> 2);
    const int br = 4 * xcd + (jj & 3), bc = jj >> 2;
    const int l15 = lane & 15, g4 = lane >> 4;
    const __amdgpu_buffer_rsrc_t wsr = __builtin_amdgcn_make_buffer_rsrc(ws, 0, WS_END, 0x00020000);
    STAMP(0);

    u32x2 ep; asm volatile("global_load_dwordx2 %0, %1, off sc1" : "=v"(ep) : "v"(ws) : "memory");
    const int nch = (b & 3) == 0 ? 3 : 2;
    const int cu = tid >> 8, cq = tid & 255;
    const int ch01 = 2 * b + cu, ch2 = 512 + (b >> 2);
    u32x4 ka0, ka1, kb0, kb1;
    {
        const char* p = (const char*)ssk + ch01 * 4096 + cq * 16;
        ka0 = gld128(p); ka1 = gld128(p + (cq < 255 ? 16 : 0));
        const char* p2 = (const char*)ssk + ch2 * 4096 + cq * 16;
        kb0 = gld128(p2); kb1 = gld128(p2 + (cq < 255 ? 16 : 0));
    }
    const int cp = tid >> 3, oct = tid & 7;
    const int chB = 72 * bc + 2 * (cp < 36 ? cp : 35);
    u32x4 shv = gld128((const char*)shk + chB * 8);
    for (int u = w; u < 19; u += 8) {
        if (u < 10) {
            int q = u * 64 + lane; q = q < 612 ? q : 611;
            int row = q / 9, cc = q - row * 9; int tok = 64 * br - 4 + row; tok = tok < 0 ? 0 : tok;
            dma16((const char*)bk + tok * 1152 + bc * 144 + cc * 16, L0 + L_B1K + u * 1024);
        } else {
            int q = (u - 10) * 64 + lane; int row = q / 9, cc = q - row * 9;
            dma16((const char*)bv + (64 * br + row) * 1152 + bc * 144 + cc * 16, L0 + L_B1V + (u - 10) * 1024);
        }
    }
    const char* xsrc = (const char*)x + (size_t)(128 * bi + 16 * w + (lane >> 3)) * 1152 + (((lane & 7) ^ (lane >> 3)) * 16);
    const char* wsrc;
    {
        int swz = 2 * (g4 | ((w & 1) << 2));
        int lc = l15 ^ swz; lc = lc < 15 ? lc : 14;
        int sec = lc / 5, cc = lc - sec * 5;
        wsrc = (const char*)wqkv + (size_t)(8 * w + g4) * 3456 + sec * 1152 + 72 * gp - (gp & 1) * 8 + cc * 16;
    }
    auto a1_issue = [&](int s) {
        const unsigned sl = L0 + (s & 3) * 32768;
        dma16(xsrc + s * 128, sl + (16 * w) * 128);
        dma16(xsrc + s * 128 + 8 * 1152, sl + (16 * w + 8) * 128);
        dma16(wsrc + (size_t)s * 64 * 3456, sl + 16384 + (8 * w) * 256);
        dma16(wsrc + (size_t)s * 64 * 3456 + 4 * 3456, sl + 16384 + (8 * w + 4) * 256);
    };
    a1_issue(0); a1_issue(1); a1_issue(2); a1_issue(3);

    WAITVM(16);
    TIE(ka0); TIE(ka1); TIE(kb0); TIE(kb1); TIE(shv); TIE(ep);
    const u64 nonce = (((u64)(unsigned)__builtin_amdgcn_readfirstlane(ep[1]) << 32) | (unsigned)__builtin_amdgcn_readfirstlane(ep[0])) + 1;
    BAR();
    STAMP(1);
    unsigned kvp[8];
    if (tid < 288) {
        float k0[11], k1[11];
#pragma unroll
        for (int q = 0; q < 11; ++q) { unsigned v = lds32(L0 + L_B1K + (8 * oct + 1 + q) * 144 + cp * 4); k0[q] = blo(v); k1[q] = bhi(v); }
        if (br == 0 && oct == 0) { k0[0] = k0[1] = k0[2] = 0.f; k1[0] = k1[1] = k1[2] = 0.f; }
        const float s00 = blo(shv[0]), s01 = bhi(shv[0]), s02 = blo(shv[1]), s03 = bhi(shv[1]);
        const float s10 = blo(shv[2]), s11 = bhi(shv[2]), s12 = blo(shv[3]), s13 = bhi(shv[3]);
#pragma unroll
        for (int t = 0; t < 8; ++t) {
            unsigned v = lds32(L0 + L_B1V + (8 * oct + t) * 144 + cp * 4);
            float a0 = k0[t] * s03; a0 = fmaf(k0[t + 1], s02, a0); a0 = fmaf(k0[t + 2], s01, a0); a0 = fmaf(k0[t + 3], s00, a0);
            float a1 = k1[t] * s13; a1 = fmaf(k1[t + 1], s12, a1); a1 = fmaf(k1[t + 2], s11, a1); a1 = fmaf(k1[t + 3], s10, a1);
            unsigned ks = pk(a0, a1);
            kvp[t] = pk(blo(ks) * blo(v), bhi(ks) * bhi(v));
        }
        u32x4 r0, r1;
#pragma unroll
        for (int d = 0; d < 4; ++d) {
            r0[d] = (kvp[7 - 2 * d] & 0xffffu) | (kvp[6 - 2 * d] << 16);
            r1[d] = (kvp[7 - 2 * d] >> 16) | (kvp[6 - 2 * d] & 0xffff0000u);
        }
        const int off = WS_R0 + chB * 4096 + (2040 - 64 * br - 8 * oct) * 2;
        __builtin_amdgcn_raw_buffer_store_b128(r0, wsr, off, 0, 16);
        __builtin_amdgcn_raw_buffer_store_b128(r1, wsr, off + 4096, 0, 16);
    } else {
#pragma unroll
        for (int t = 0; t < 8; ++t) kvp[t] = 0;
    }
    STAMP(2);

    const int tg = w & 3, nh = w >> 2;
    f32x4 aq[2], ak[2], av[2], ar[2];
#pragma unroll
    for (int t = 0; t < 2; ++t) { aq[t] = f32x4{0, 0, 0, 0}; ak[t] = aq[t]; av[t] = aq[t]; ar[t] = aq[t]; }
    {
        const int q4 = (lane >> 2) & 3, p4 = lane & 3, po = (gp & 1) * 8;
        const int swz = 2 * (q4 | ((g4 & 1) << 2));
        auto woff = [&](int sec, int piece) { int byte = sec * 80 + po + 8 * piece; return (unsigned)(16384 + (8 * g4 + q4) * 256 + (((byte >> 4) ^ swz) * 16) + (byte & 8)); };
        const unsigned wq = woff(0, 5 * nh + p4), wk = woff(1, 5 * nh + p4), wv = woff(2, 5 * nh + p4), wr = woff(p4 < 3 ? p4 : 2, 4);
        const unsigned xo = (32 * tg + l15) * 128;
        const unsigned xs0 = ((g4 ^ (lane & 7)) * 16), xs1 = (((4 + g4) ^ (lane & 7)) * 16);
#pragma unroll
        for (int s = 0; s < 9; ++s) {
            if (s == 0) WAITVM(12); else if (s < 7) WAITVM(8); else if (s == 7) WAITVM(4); else WAITVM(0);
            BAR();
            if (s >= 1 && s + 3 <= 8) a1_issue(s + 3);
            if (s == 3 && tid == 0) __hip_atomic_store((GAS u64*)(ws + WS_KFLAG) + bc * 32 + br, nonce, RLX);
            const unsigned sl = L0 + (s & 3) * 32768;
#pragma unroll
            for (int kk = 0; kk < 2; ++kk) {
                u32x4 xf[2];
                xf[0] = lds128(sl + xo + (kk ? xs1 : xs0));
                xf[1] = lds128(sl + xo + 2048 + (kk ? xs1 : xs0));
                u32x2 a, c;
                u32x4 fq, fk, fv;
                a = ldstr(sl + wq + kk * 8192); c = ldstr(sl + wq + kk * 8192 + 1024); fq = u32x4{a[0], a[1], c[0], c[1]};
                a = ldstr(sl + wk + kk * 8192); c = ldstr(sl + wk + kk * 8192 + 1024); fk = u32x4{a[0], a[1], c[0], c[1]};
                a = ldstr(sl + wv + kk * 8192); c = ldstr(sl + wv + kk * 8192 + 1024); fv = u32x4{a[0], a[1], c[0], c[1]};
#pragma unroll
                for (int t = 0; t < 2; ++t) { aq[t] = mfma16(fq, xf[t], aq[t]); ak[t] = mfma16(fk, xf[t], ak[t]); av[t] = mfma16(xf[t], fv, av[t]); }
                if (nh == 0) {
                    a = ldstr(sl + wr + kk * 8192); c = ldstr(sl + wr + kk * 8192 + 1024);
                    u32x4 fr = u32x4{a[0], a[1], c[0], c[1]};
#pragma unroll
                    for (int t = 0; t < 2; ++t) ar[t] = mfma16(fr, xf[t], ar[t]);
                }
            }
        }
    }
    STAMP(3);
    BAR();
    {
        const float qs = 0.34004634f;
#pragma unroll
        for (int t = 0; t < 2; ++t) {
            const unsigned ko = tg * 1024 + (g4 >> 1) * 512 + (16 * t + l15) * 16 + (g4 & 1) * 8;
            st64(L0 + L_OWN + nh * HEADB + ko, u32x2{pk(ak[t][0], ak[t][1]), pk(ak[t][2], ak[t][3])});
            st64(L0 + L_Q + nh * 4608 + ko, u32x2{pk(aq[t][0] * qs, aq[t][1] * qs), pk(aq[t][2] * qs, aq[t][3] * qs)});
            const int tt = 2 * tg + t;
            st64(L0 + L_OWN + nh * HEADB + 4608 + tt * 608 + (g4 & 1) * 304 + l15 * 16 + (g4 >> 1) * 8, u32x2{pk(av[t][0], av[t][1]), pk(av[t][2], av[t][3])});
            if (nh == 0) {
                const int tok = 32 * tg + 16 * t + l15;
                if (g4 == 0) {
                    st32(L0 + L_Q + 4096 + tok * 4, pk(ar[t][0] * qs, ar[t][1] * qs));
                    st32(L0 + L_Q + 4608 + 4096 + tok * 4, pk(ar[t][2] * qs, ar[t][3] * qs));
                } else if (g4 == 1) {
                    st32(L0 + L_OWN + 4096 + tok * 4, pk(ar[t][0], ar[t][1]));
                    st32(L0 + L_OWN + HEADB + 4096 + tok * 4, pk(ar[t][2], ar[t][3]));
                } else if (g4 == 2) {
                    const unsigned p01 = pk(ar[t][0], ar[t][1]), p23 = pk(ar[t][2], ar[t][3]);
                    const unsigned vb = L0 + L_OWN + 4608 + tt * 608 + ((l15 >> 2) & 1) * 304 + 16 * 16 + (4 * (l15 >> 3) + (l15 & 3)) * 2;
                    st16(vb, (unsigned short)p01); st16(vb + 16, (unsigned short)(p01 >> 16));
                    st16(vb + HEADB, (unsigned short)p23); st16(vb + HEADB + 16, (unsigned short)(p23 >> 16));
                }
            } else if (lane < 4) {
                st128(L0 + L_OWN + (lane >> 1) * HEADB + 4608 + tt * 608 + (lane & 1) * 304 + 18 * 16, u32x4{0x3f803f80u, 0x3f803f80u, 0x3f803f80u, 0x3f803f80u});
            }
        }
    }
    {
        auto kcopy = [&](unsigned kb, u32x4 d0, u32x4 d1) {
            st128(kb + (4 + cq) * 16, d0);
            st128(kb + KCS + (4 + cq) * 16, u32x4{d0[1], d0[2], d0[3], d1[0]});
            st128(kb + 2 * KCS + (4 + cq) * 16, u32x4{d0[2], d0[3], d1[0], d1[1]});
            st128(kb + 3 * KCS + (4 + cq) * 16, u32x4{d0[3], d1[0], d1[1], d1[2]});
            if (cq == 0) {
                st128(kb + 3 * 16, u32x4{0, 0, 0, 0});
                st128(kb + KCS + 3 * 16, u32x4{0, 0, 0, d0[0]});
                st128(kb + 2 * KCS + 3 * 16, u32x4{0, 0, d0[0], d0[1]});
                st128(kb + 3 * KCS + 3 * 16, u32x4{0, d0[0], d0[1], d0[2]});
            } else if (cq < 4) {
#pragma unroll
                for (int c = 0; c < 4; ++c) st128(kb + c * KCS + (cq - 1) * 16, u32x4{0, 0, 0, 0});
            }
        };
        kcopy(L0 + L_KC + cu * KCH, ka0, ka1);
        if (nch == 3 && tid < 256) kcopy(L0 + L_KC + 2 * KCH, kb0, kb1);
    }
    BAR();
    {
        const int gb = WS_KV + (bi * 16 + gp) * KVB;
#pragma unroll
        for (int k = 0; k < 3; ++k) {
            const int q = tid + 512 * k;
            if (q < 1184) __builtin_amdgcn_raw_buffer_store_b128(lds128(L0 + L_OWN + q * 16), wsr, gb + q * 16, 0, 16);
        }
    }
    if (w == 0) {
        poll_flags((const GAS u64*)(ws + WS_KFLAG) + ((2 * b) / 72) * 32, 32, nonce, lane);
        if (nch == 3) poll_flags((const GAS u64*)(ws + WS_KFLAG) + (ch2 / 72) * 32, 32, nonce, lane);
    }
    WAITVM(0);
    BAR();
    if (tid == 0) __hip_atomic_store((GAS u64*)(ws + WS_KVFLAG) + gp * 16 + bi, nonce, RLX);
    STAMP(4);
    {
        auto rcopy = [&](unsigned rb, int ch) {
            const int off = WS_R0 + ch * 4096 + cq * 16;
            u32x4 d = __builtin_amdgcn_raw_buffer_load_b128(wsr, off, 0, 16);
            unsigned nx = 0;
            if (cq < 255) nx = __builtin_amdgcn_raw_buffer_load_b32(wsr, off + 16, 0, 16);
            st128(rb + cq * 16, d);
            st128(rb + RCS + cq * 16, u32x4{__builtin_amdgcn_alignbit(d[1], d[0], 16), __builtin_amdgcn_alignbit(d[2], d[1], 16), __builtin_amdgcn_alignbit(d[3], d[2], 16), __builtin_amdgcn_alignbit(nx, d[3], 16)});
            if (cq < 30) { st128(rb + (256 + cq) * 16, u32x4{0, 0, 0, 0}); st128(rb + RCS + (256 + cq) * 16, u32x4{0, 0, 0, 0}); }
        };
        rcopy(L0 + L_RB + cu * RCH, ch01);
        if (nch == 3 && tid < 256) rcopy(L0 + L_RB + 2 * RCH, ch2);
    }
    BAR();
    STAMP(5);

    {
        const int slot = w < 4 ? (w >> 1) : 2;
        const int rho0 = w < 4 ? 4 * (w & 1) : 2 * (w - 4);
        const int nrho = w < 4 ? 4 : (nch == 3 ? 2 : 0);
        f32x4 acc[8];
#pragma unroll
        for (int n = 0; n < 8; ++n) acc[n] = f32x4{0, 0, 0, 0};
        const unsigned la = L0 + L_KC + slot * KCH + ((l15 + 1) & 3) * KCS + (g4 + ((l15 + 1) >> 2)) * 16;
        const unsigned lb = L0 + L_RB + slot * RCH + ((l15 & 1) ? 0 : RCS) + (252 - 28 * 8 - 4 * (l15 >> 1) + g4) * 16;
        for (int rr = 0; rr < nrho; ++rr) {
            const unsigned ro = (rho0 + rr) * 64;
            u32x4 A[8], B[8];
#pragma unroll
            for (int m = 0; m < 8; ++m) { A[m] = lds128(la + ro + m * 512); B[m] = lds128(lb + ro + (7 - m) * 512); }
#pragma unroll
            for (int nu = 0; nu < 8; ++nu)
#pragma unroll
                for (int mu = 0; mu + nu < 8; ++mu) acc[mu + nu] = mfma16(A[mu], B[nu], acc[mu + nu]);
        }
        BAR();
        const unsigned sc = L0 + L_KC + slot * KCH;
        if (w < 4) {
            const int h = w & 1;
            if (h) {
#pragma unroll
                for (int n = 0; n < 4; ++n) *(LAS f32x4*)(sc + 4096 + n * 1024 + lane * 16) = acc[n];
            } else {
#pragma unroll
                for (int n = 0; n < 4; ++n) *(LAS f32x4*)(sc + n * 1024 + lane * 16) = acc[4 + n];
            }
        } else if (nch == 3) {
            const int h = w - 4;
#pragma unroll
            for (int n = 0; n < 4; ++n) *(LAS f32x4*)(sc + h * 4096 + n * 1024 + lane * 16) = acc[n];
        }
        BAR();
        f32x4 y[4];
        if (w < 4) {
            const int h = w & 1;
            if (h) {
#pragma unroll
                for (int n = 0; n < 4; ++n) y[n] = acc[4 + n] + *(LAS const f32x4*)(sc + n * 1024 + lane * 16);
            } else {
#pragma unroll
                for (int n = 0; n < 4; ++n) y[n] = acc[n] + *(LAS const f32x4*)(sc + 4096 + n * 1024 + lane * 16);
            }
        } else if (nch == 3) {
            const int h = w - 4;
            f32x4 s4 = f32x4{0, 0, 0, 0};
#pragma unroll
            for (int q = 0; q < 4; ++q) s4 += *(LAS const f32x4*)(sc + q * 4096 + h * 1024 + lane * 16);
            y[0] = s4;
        }
        BAR();
        if (w >= 4 && nch == 3) {
            const int h = w - 4;
#pragma unroll
            for (int n = 0; n < 4; ++n) *(LAS f32x4*)(sc + h * 4096 + n * 1024 + lane * 16) = acc[4 + n];
        }
        BAR();
        if (w >= 4 && nch == 3) {
            const int h = w - 4;
            f32x4 s4 = f32x4{0, 0, 0, 0};
#pragma unroll
            for (int q = 0; q < 4; ++q) s4 += *(LAS const f32x4*)(sc + q * 4096 + h * 1024 + lane * 16);
            y[1] = s4;
        }
        BAR();
        const unsigned ys = L0 + L_RB + slot * RCH;
        const unsigned yo = (l15 >> 1) * 64 + g4 * 16 + (l15 & 1) * 8;
        const int chs = slot == 2 ? ch2 : 2 * b + slot;
        if (w < 4) {
            const int h = w & 1;
#pragma unroll
            for (int n = 0; n < 4; ++n) st64(ys + (4 * h + n) * 512 + yo, u32x2{pk(y[n][0], y[n][1]), pk(y[n][2], y[n][3])});
#pragma unroll
            for (int k = 0; k < 2; ++k)
                __builtin_amdgcn_raw_buffer_store_b128(lds128(ys + h * 2048 + k * 1024 + lane * 16), wsr, WS_Y + chs * 4096 + h * 2048 + k * 1024 + lane * 16, 0, 16);
        } else if (nch == 3) {
            const int h = w - 4;
            st64(ys + h * 512 + yo, u32x2{pk(y[0][0], y[0][1]), pk(y[0][2], y[0][3])});
            st64(ys + (4 + h) * 512 + yo, u32x2{pk(y[1][0], y[1][1]), pk(y[1][2], y[1][3])});
            if (lane < 32) __builtin_amdgcn_raw_buffer_store_b128(lds128(ys + h * 512 + lane * 16), wsr, WS_Y + chs * 4096 + h * 512 + lane * 16, 0, 16);
            else __builtin_amdgcn_raw_buffer_store_b128(lds128(ys + (4 + h) * 512 + (lane - 32) * 16), wsr, WS_Y + chs * 4096 + (4 + h) * 512 + (lane - 32) * 16, 0, 16);
        }
    }
    if (w == 0 && bi > 0) poll_flags((const GAS u64*)(ws + WS_KVFLAG) + gp * 16 + (bi > 1 ? bi - 2 : 0), bi > 1 ? 2 : 1, nonce, lane);
    WAITVM(0);
    BAR();
    if (tid < nch) __hip_atomic_store((GAS u64*)(ws + WS_YFLAG) + (tid == 2 ? ch2 : 2 * b + tid), nonce, RLX);
    STAMP(6);
    {
        for (int hb = 0; hb < 2; ++hb) {
            const int ib = bi - 2 + hb;
            if (ib >= 0) {
                const int gb = WS_KV + (ib * 16 + gp) * KVB;
#pragma unroll
                for (int k = 0; k < 3; ++k) {
                    const int q = tid + 512 * k;
                    if (q < 1184) st128(L0 + hb * KVB + q * 16, __builtin_amdgcn_raw_buffer_load_b128(wsr, gb + q * 16, 0, 16));
                }
            }
        }
    }
    for (int u = w; u < 81; u += 8) {
        const int q = u * 64 + lane, row = q / 9, cc = q - row * 9;
        dma16((const char*)wo + row * 1152 + bc * 144 + cc * 16, L0 + L_WO + u * 1024);
    }
    BAR();
    STAMP(7);

    {
        const int hd = w >> 2, qt = w & 3, l31 = lane & 31, hh = lane >> 5;
        const u32x4 qm = lds128(L0 + L_Q + hd * 4608 + qt * 1024 + hh * 512 + l31 * 16);
        const unsigned qrv = lds32(L0 + L_Q + hd * 4608 + 4096 + (32 * qt + l31) * 4);
        const u32x4 qr = u32x4{hh ? 0u : qrv, 0, 0, 0};
        const int vrow = l31 < 19 ? l31 : (l31 == 19 ? 18 : (l31 < 28 ? 0 : 4));
        const unsigned kbase = L0 + hd * HEADB + hh * 512 + l31 * 16, krbase = L0 + hd * HEADB + 4096 + l31 * 4;
        const unsigned vbase = L0 + hd * HEADB + 4608 + hh * 304 + vrow * 16;
        const int qq = l31 - 4 * hh;
        const int t_first = bi >= 2 ? qt : (bi == 1 ? 4 : 8), t_last = 8 + qt;
        const bool has_first = bi >= 2;
        auto qk = [&](int t, f32x16 c) {
            const unsigned bo = (t >> 2) * KVB;
            u32x4 km = lds128(kbase + bo + (t & 3) * 1024);
            unsigned kr = lds32(krbase + bo + (t & 3) * 128);
            c = mfma32(km, qm, c);
            return mfma32(u32x4{kr, 0, 0, 0}, qr, c);
        };
        auto pv = [&](int t, const f32x16& p, f32x16 o) {
            const unsigned bo = (t >> 2) * KVB + (t & 3) * 1216;
            u32x4 v0 = lds128(vbase + bo), v1 = lds128(vbase + bo + 608);
            u32x4 p0 = u32x4{pk(p[0], p[1]), pk(p[2], p[3]), pk(p[4], p[5]), pk(p[6], p[7])};
            u32x4 p1 = u32x4{pk(p[8], p[9]), pk(p[10], p[11]), pk(p[12], p[13]), pk(p[14], p[15])};
            o = mfma32(v0, p0, o);
            return mfma32(v1, p1, o);
        };
        const float NINF = -__builtin_inff();
        f32x16 z16;
#pragma unroll
        for (int r = 0; r < 16; ++r) z16[r] = 0.f;
        f32x16 sd = qk(t_last, z16);
        float m = NINF;
#pragma unroll
        for (int r = 0; r < 16; ++r) { const int c = (r & 3) + 8 * (r >> 2); sd[r] = c <= qq ? sd[r] : NINF; m = fmaxf(m, sd[r]); }
        m = fmaxf(m, __shfl_xor(m, 32));
        f32x16 o;
        float l;
        for (int attempt = 0; attempt < 2; ++attempt) {
            f32x16 nm, p;
#pragma unroll
            for (int r = 0; r < 16; ++r) { nm[r] = -m; p[r] = __builtin_amdgcn_exp2f(sd[r] - m); }
            o = pv(t_last, p, z16);
            for (int t = t_first; t < t_last; ++t) {
                f32x16 s = qk(t, nm);
                if (has_first && t == t_first) {
#pragma unroll
                    for (int r = 0; r < 16; ++r) { const int c = (r & 3) + 8 * (r >> 2); s[r] = c > qq ? s[r] : NINF; }
                }
#pragma unroll
                for (int r = 0; r < 16; ++r) p[r] = __builtin_amdgcn_exp2f(s[r]);
                o = pv(t, p, o);
            }
            l = __shfl(o[10], l31);
            if (attempt == 1 || __all(l <= 1048576.f)) break;
            for (int t = t_first; t < t_last; ++t) {
                f32x16 s = qk(t, z16);
#pragma unroll
                for (int r = 0; r < 16; ++r) { const int c = (r & 3) + 8 * (r >> 2); const bool keep = !(has_first && t == t_first) || c > qq; m = fmaxf(m, keep ? s[r] : NINF); }
            }
            m = fmaxf(m, __shfl_xor(m, 32));
        }
        const float il = 1.f / l;
        const int T16 = 8 * bi + 2 * qt + (l31 >> 4);
        u32x4 om = u32x4{pk(o[0] * il, o[1] * il), pk(o[2] * il, o[3] * il), pk(o[4] * il, o[5] * il), pk(o[6] * il, o[7] * il)};
        __builtin_amdgcn_raw_buffer_store_b128(om, wsr, WS_OM + ((T16 * 16 + gp) * 64 + (2 * hd + hh) * 16 + (l31 & 15)) * 16, 0, 16);
        if (hh == 0) __builtin_amdgcn_raw_buffer_store_b32(pk(o[8] * il, o[9] * il), wsr, WS_OR + (((T16 * 16 + gp) * 2 + hd) * 16 + (l31 & 15)) * 4, 0, 16);
    }
    WAITVM(0);
    BAR();
    if (tid == 0) __hip_atomic_store((GAS u64*)(ws + WS_OFLAG) + bi * 16 + gp, nonce, RLX);
    STAMP(8);

    if (w == 0) { poll_flags((const GAS u64*)(ws + WS_YFLAG) + 72 * bc, 64, nonce, lane); poll_flags((const GAS u64*)(ws + WS_YFLAG) + 72 * bc + 64, 8, nonce, lane); }
    BAR();
    if (tid < 288) {
        const int t0 = 64 * br + 8 * oct;
        u32x4 y0 = __builtin_amdgcn_raw_buffer_load_b128(wsr, WS_Y + chB * 4096 + t0 * 2, 0, 16);
        u32x4 y1 = __builtin_amdgcn_raw_buffer_load_b128(wsr, WS_Y + (chB + 1) * 4096 + t0 * 2, 0, 16);
        const unsigned dv = *(const unsigned*)(ssd + chB);
        const float D0 = blo(dv), D1 = bhi(dv);
#pragma unroll
        for (int t = 0; t < 8; ++t) {
            const int slot = (t & 1) * 4 + (t >> 1);
            const float ya = (slot & 1) ? bhi(y0[slot >> 1]) : blo(y0[slot >> 1]);
            const float yb = (slot & 1) ? bhi(y1[slot >> 1]) : blo(y1[slot >> 1]);
            const unsigned qv = *(const unsigned*)(bq + (size_t)(t0 + t) * 576 + chB);
            const unsigned kd = pk(blo(kvp[t]) * D0, bhi(kvp[t]) * D1);
            const unsigned zz = pk(ya + blo(kd), yb + bhi(kd));
            *(unsigned*)(out + (size_t)(t0 + t) * 1152 + 576 + chB) = pk(blo(qv) * blo(zz), bhi(qv) * bhi(zz));
        }
    }
    STAMP(9);

    if (w == 0) poll_flags((const GAS u64*)(ws + WS_OFLAG) + (br >> 1) * 16, 16, nonce, lane);
    WAITVM(0);
    BAR();
    STAMP(10);
    {
        const int tt = w & 3, kh = w >> 2, T16 = 4 * br + tt;
        u32x4 of[9];
#pragma unroll
        for (int s = 0; s < 8; ++s) of[s] = __builtin_amdgcn_raw_buffer_load_b128(wsr, WS_OM + ((T16 * 16 + 8 * kh + s) * 64 + lane) * 16, 0, 16);
#pragma unroll
        for (int d = 0; d < 4; ++d) of[8][d] = __builtin_amdgcn_raw_buffer_load_b32(wsr, WS_OR + (((T16 * 16 + 8 * kh + 2 * g4 + (d >> 1)) * 2 + (d & 1)) * 16 + l15) * 4, 0, 16);
        f32x4 acc[5];
#pragma unroll
        for (int j = 0; j < 5; ++j) acc[j] = f32x4{0, 0, 0, 0};
        const int q4 = (lane >> 2) & 3, p4 = lane & 3, hd = g4 >> 1, hh = g4 & 1;
        const unsigned mrow = L0 + L_WO + (288 * kh + 20 * hd + 4 * hh + q4) * 144;
        const unsigned rrow = L0 + L_WO + (288 * kh + 72 * g4 + 16 + q4) * 144;
#pragma unroll
        for (int s = 0; s < 9; ++s) {
#pragma unroll
            for (int j = 0; j < 5; ++j) {
                const unsigned col = j < 4 ? j * 32 + p4 * 8 : 128 + (p4 & 1) * 8;
                u32x2 a, c;
                if (s < 8) { a = ldstr(mrow + s * 36 * 144 + col); c = ldstr(mrow + (s * 36 + 8) * 144 + col); }
                else { a = ldstr(rrow + col); c = ldstr(rrow + 36 * 144 + col); }
                acc[j] = mfma16(u32x4{a[0], a[1], c[0], c[1]}, of[s], acc[j]);
            }
        }
        const unsigned sc = L0 + tt * 5120;
        if (kh == 1) {
#pragma unroll
            for (int j = 0; j < 5; ++j) *(LAS f32x4*)(sc + j * 1024 + lane * 16) = acc[j];
        }
        BAR();
        if (kh == 0) {
            unsigned short* op = out + (size_t)(16 * T16 + l15) * 1152 + 72 * bc + 4 * g4;
#pragma unroll
            for (int j = 0; j < 5; ++j) {
                f32x4 v = acc[j] + *(LAS const f32x4*)(sc + j * 1024 + lane * 16);
                if (j < 4 || g4 < 2) *(u32x2*)(op + 16 * j) = u32x2{pk(v[0], v[1]), pk(v[2], v[3])};
            }
        }
    }
    if (b == 0 && w == 7) {
        for (int k = 0; k < 4; ++k) poll_flags((const GAS u64*)(ws + WS_KFLAG) + 64 * k, 64, nonce, lane);
        if (lane == 0) __hip_atomic_store((GAS u64*)ws, nonce, RLX);
    }
    STAMP(11);
    { const u64 until = __builtin_amdgcn_s_memrealtime() + 10 * (probe1 - probe0); while (__builtin_amdgcn_s_memrealtime() < until) __builtin_amdgcn_s_sleep(4); }
}

extern "C" void kernel_launch(void* const* d_in, const int* in_sizes, int n_in, void* d_out, int out_size, void* d_ws, size_t ws_size, hipStream_t stream) {
    fused_kernel<<<256, 512, 0, stream>>>((const unsigned short*)d_in[0], (const unsigned short*)d_in[1], (const unsigned short*)d_in[2], (const unsigned short*)d_in[3],
                                          (const unsigned short*)d_in[4], (const unsigned short*)d_in[5], (const unsigned short*)d_in[6], (const unsigned short*)d_in[7],
                                          (const unsigned short*)d_in[8], (unsigned short*)d_out, (char*)d_ws);
}
```

```cpp
#include <hip/hip_runtime.h>
#include <stdint.h>

#define GAS __attribute__((address_space(1)))
#define LAS __attribute__((address_space(3)))
#define DEVI __device__ __forceinline__
typedef unsigned long long u64;
typedef __attribute__((ext_vector_type(8))) __bf16 bf16x8;
typedef __attribute__((ext_vector_type(2))) __bf16 bf16x2;
typedef __attribute__((ext_vector_type(2))) float f32x2;
typedef __attribute__((ext_vector_type(4))) float f32x4;
typedef __attribute__((ext_vector_type(16))) float f32x16;
typedef __attribute__((ext_vector_type(4))) unsigned u32x4;
typedef __attribute__((ext_vector_type(2))) unsigned u32x2;
typedef __attribute__((ext_vector_type(4))) short s16x4;

constexpr int WS_KFLAG = 256, WS_KVFLAG = 4096, WS_OFLAG = 8192, WS_YFLAG = 12288;
constexpr int WS_R0 = 32768;
constexpr int WS_KV = WS_R0 + 2359296;
constexpr int WS_OM = WS_KV + 4849664;
constexpr int WS_OR = WS_OM + 2097152;
constexpr int WS_Y = WS_OR + 262144;
constexpr int WS_END = WS_Y + 2359296;
constexpr int WS_TS = 16 << 20;
constexpr int L_B1K = 131072, L_B1V = 141312;
constexpr int KVB = 18944, HEADB = 9472;
constexpr int L_OWN = 2 * KVB, L_Q = 3 * KVB;
constexpr int L_KC = 66048, KCS = 4160, KCH = 4 * KCS;
constexpr int L_RB = 131072, RCS = 4576, RCH = 2 * RCS;
constexpr int L_WO = 66048;
constexpr int LDS_BYTES = 163840;
constexpr unsigned SPIN_CAP = 1u << 17;

#define STAMP(k) do { } while (0)

DEVI f32x4 mfma16(u32x4 a, u32x4 b, f32x4 c) { return __builtin_amdgcn_mfma_f32_16x16x32_bf16(__builtin_bit_cast(bf16x8, a), __builtin_bit_cast(bf16x8, b), c, 0, 0, 0); }
DEVI f32x16 mfma32(u32x4 a, u32x4 b, f32x16 c) { return __builtin_amdgcn_mfma_f32_32x32x16_bf16(__builtin_bit_cast(bf16x8, a), __builtin_bit_cast(bf16x8, b), c, 0, 0, 0); }
DEVI unsigned pk(float lo, float hi) { f32x2 v = {lo, hi}; return __builtin_bit_cast(unsigned, __builtin_convertvector(v, bf16x2)); }
DEVI float blo(unsigned v) { return __builtin_bit_cast(float, v << 16); }
DEVI float bhi(unsigned v) { return __builtin_bit_cast(float, v & 0xffff0000u); }
DEVI float rndbf(float x) { return blo(pk(x, x)); }
DEVI u32x4 lds128(unsigned a) { return *(LAS const u32x4*)a; }
DEVI u32x2 lds64(unsigned a) { return *(LAS const u32x2*)a; }
DEVI unsigned lds32(unsigned a) { return *(LAS const unsigned*)a; }
DEVI void st128(unsigned a, u32x4 v) { *(LAS u32x4*)a = v; }
DEVI void st64(unsigned a, u32x2 v) { *(LAS u32x2*)a = v; }
DEVI void st32(unsigned a, unsigned v) { *(LAS unsigned*)a = v; }
DEVI void st16(unsigned a, unsigned short v) { *(LAS unsigned short*)a = v; }
DEVI u32x2 ldstr(unsigned a) { return __builtin_bit_cast(u32x2, __builtin_amdgcn_ds_read_tr16_b64_v4i16((LAS s16x4*)a)); }
DEVI void dma16(const void* g, unsigned lds) {
    unsigned sv;
    asm volatile("s_mov_b32 %0, m0\n\ts_mov_b32 m0, %2\n\ts_nop 0\n\tglobal_load_lds_dwordx4 %1, off\n\ts_mov_b32 m0, %0" : "=&s"(sv) : "v"(g), "s"(lds) : "memory");
}
DEVI u32x4 gld128(const void* p) { u32x4 v; asm volatile("global_load_dwordx4 %0, %1, off" : "=v"(v) : "v"(p) : "memory"); return v; }
#define WAITVM(n) asm volatile("s_waitcnt vmcnt(%0)" ::"i"(n) : "memory")
#define TIE(x) asm volatile("" : "+v"(x))
#define BAR() do { asm volatile("s_waitcnt lgkmcnt(0)\n\ts_barrier" ::: "memory"); } while (0)
#define RLX __ATOMIC_RELAXED, __HIP_MEMORY_SCOPE_AGENT

DEVI void poll_flags(const GAS u64* f, int n, u64 nonce, int lane) {
    for (unsigned spins = 0; spins < SPIN_CAP; ++spins) {
        u64 v = nonce;
        if (lane < n) v = __hip_atomic_load(f + lane, RLX);
        if (__all(v == nonce)) break;
        __builtin_amdgcn_s_sleep(2);
    }
    __builtin_amdgcn_fence(__ATOMIC_ACQUIRE, "wavefront");
    asm volatile("" ::: "memory");
}

__global__ __launch_bounds__(512, 2) void fused_kernel(const unsigned short* __restrict__ x, const unsigned short* __restrict__ wqkv, const unsigned short* __restrict__ wo,
                                                       const unsigned short* __restrict__ bq, const unsigned short* __restrict__ bk, const unsigned short* __restrict__ bv,
                                                       const unsigned short* __restrict__ shk, const unsigned short* __restrict__ ssk, const unsigned short* __restrict__ ssd,
                                                       unsigned short* __restrict__ out, char* __restrict__ ws) {
    __shared__ __attribute__((aligned(16))) char smem[LDS_BYTES];
    const unsigned L0 = (unsigned)(uintptr_t)(LAS char*)smem;
    const int tid = threadIdx.x, lane = tid & 63, w = __builtin_amdgcn_readfirstlane(tid >> 6);
    const int b = blockIdx.x, xcd = b & 7, jj = b >> 3;
    const int bi = 4 * (xcd >> 1) + (jj & 3), gp = 8 * (xcd & 1) + (jj >> 2);
    const int br = 4 * xcd + (jj & 3), bc = jj >> 2;
    const int l15 = lane & 15, g4 = lane >> 4;
    const __amdgpu_buffer_rsrc_t wsr = __builtin_amdgcn_make_buffer_rsrc(ws, 0, WS_END, 0x00020000);
    STAMP(0);

    u32x2 ep; asm volatile("global_load_dwordx2 %0, %1, off sc1" : "=v"(ep) : "v"(ws) : "memory");
    const int nch = (b & 3) == 0 ? 3 : 2;
    const int cu = tid >> 8, cq = tid & 255;
    const int ch01 = 2 * b + cu, ch2 = 512 + (b >> 2);
    u32x4 ka0, ka1, kb0, kb1;
    {
        const char* p = (const char*)ssk + ch01 * 4096 + cq * 16;
        ka0 = gld128(p); ka1 = gld128(p + (cq < 255 ? 16 : 0));
        const char* p2 = (const char*)ssk + ch2 * 4096 + cq * 16;
        kb0 = gld128(p2); kb1 = gld128(p2 + (cq < 255 ? 16 : 0));
    }
    const int cp = tid >> 3, oct = tid & 7;
    const int chB = 72 * bc + 2 * (cp < 36 ? cp : 35);
    u32x4 shv = gld128((const char*)shk + chB * 8);
    for (int u = w; u < 19; u += 8) {
        if (u < 10) {
            int q = u * 64 + lane; q = q < 612 ? q : 611;
            int row = q / 9, cc = q - row * 9; int tok = 64 * br - 4 + row; tok = tok < 0 ? 0 : tok;
            dma16((const char*)bk + tok * 1152 + bc * 144 + cc * 16, L0 + L_B1K + u * 1024);
        } else {
            int q = (u - 10) * 64 + lane; int row = q / 9, cc = q - row * 9;
            dma16((const char*)bv + (64 * br + row) * 1152 + bc * 144 + cc * 16, L0 + L_B1V + (u - 10) * 1024);
        }
    }
    const char* xsrc = (const char*)x + (size_t)(128 * bi + 16 * w + (lane >> 3)) * 1152 + (((lane & 7) ^ (lane >> 3)) * 16);
    const char* wsrc;
    {
        int swz = 2 * (g4 | ((w & 1) << 2));
        int lc = l15 ^ swz; lc = lc < 15 ? lc : 14;
        int sec = lc / 5, cc = lc - sec * 5;
        wsrc = (const char*)wqkv + (size_t)(8 * w + g4) * 3456 + sec * 1152 + 72 * gp - (gp & 1) * 8 + cc * 16;
    }
    auto a1_issue = [&](int s) {
        const unsigned sl = L0 + (s & 3) * 32768;
        dma16(xsrc + s * 128, sl + (16 * w) * 128);
        dma16(xsrc + s * 128 + 8 * 1152, sl + (16 * w + 8) * 128);
        dma16(wsrc + (size_t)s * 64 * 3456, sl + 16384 + (8 * w) * 256);
        dma16(wsrc + (size_t)s * 64 * 3456 + 4 * 3456, sl + 16384 + (8 * w + 4) * 256);
    };
    a1_issue(0); a1_issue(1); a1_issue(2); a1_issue(3);

    WAITVM(16);
    TIE(ka0); TIE(ka1); TIE(kb0); TIE(kb1); TIE(shv); TIE(ep);
    const u64 nonce = (((u64)(unsigned)__builtin_amdgcn_readfirstlane(ep[1]) << 32) | (unsigned)__builtin_amdgcn_readfirstlane(ep[0])) + 1;
    BAR();
    STAMP(1);
    unsigned kvp[8];
    if (tid < 288) {
        float k0[11], k1[11];
#pragma unroll
        for (int q = 0; q < 11; ++q) { unsigned v = lds32(L0 + L_B1K + (8 * oct + 1 + q) * 144 + cp * 4); k0[q] = blo(v); k1[q] = bhi(v); }
        if (br == 0 && oct == 0) { k0[0] = k0[1] = k0[2] = 0.f; k1[0] = k1[1] = k1[2] = 0.f; }
        const float s00 = blo(shv[0]), s01 = bhi(shv[0]), s02 = blo(shv[1]), s03 = bhi(shv[1]);
        const float s10 = blo(shv[2]), s11 = bhi(shv[2]), s12 = blo(shv[3]), s13 = bhi(shv[3]);
#pragma unroll
        for (int t = 0; t < 8; ++t) {
            unsigned v = lds32(L0 + L_B1V + (8 * oct + t) * 144 + cp * 4);
            float a0 = k0[t] * s03; a0 = fmaf(k0[t + 1], s02, a0); a0 = fmaf(k0[t + 2], s01, a0); a0 = fmaf(k0[t + 3], s00, a0);
            float a1 = k1[t] * s13; a1 = fmaf(k1[t + 1], s12, a1); a1 = fmaf(k1[t + 2], s11, a1); a1 = fmaf(k1[t + 3], s10, a1);
            unsigned ks = pk(a0, a1);
            kvp[t] = pk(blo(ks) * blo(v), bhi(ks) * bhi(v));
        }
        u32x4 r0, r1;
#pragma unroll
        for (int d = 0; d < 4; ++d) {
            r0[d] = (kvp[7 - 2 * d] & 0xffffu) | (kvp[6 - 2 * d] << 16);
            r1[d] = (kvp[7 - 2 * d] >> 16) | (kvp[6 - 2 * d] & 0xffff0000u);
        }
        const int off = WS_R0 + chB * 4096 + (2040 - 64 * br - 8 * oct) * 2;
        __builtin_amdgcn_raw_buffer_store_b128(r0, wsr, off, 0, 16);
        __builtin_amdgcn_raw_buffer_store_b128(r1, wsr, off + 4096, 0, 16);
    } else {
#pragma unroll
        for (int t = 0; t < 8; ++t) kvp[t] = 0;
    }
    STAMP(2);

    const int tg = w & 3, nh = w >> 2;
    f32x4 aq[2], ak[2], av[2], ar[2];
#pragma unroll
    for (int t = 0; t < 2; ++t) { aq[t] = f32x4{0, 0, 0, 0}; ak[t] = aq[t]; av[t] = aq[t]; ar[t] = aq[t]; }
    {
        const int q4 = (lane >> 2) & 3, p4 = lane & 3, po = (gp & 1) * 8;
        const int swz = 2 * (q4 | ((g4 & 1) << 2));
        auto woff = [&](int sec, int piece) { int byte = sec * 80 + po + 8 * piece; return (unsigned)(16384 + (8 * g4 + q4) * 256 + (((byte >> 4) ^ swz) * 16) + (byte & 8)); };
        const unsigned wq = woff(0, 5 * nh + p4), wk = woff(1, 5 * nh + p4), wv = woff(2, 5 * nh + p4), wr = woff(p4 < 3 ? p4 : 2, 4);
        const unsigned xo = (32 * tg + l15) * 128;
        const unsigned xs0 = ((g4 ^ (lane & 7)) * 16), xs1 = (((4 + g4) ^ (lane & 7)) * 16);
#pragma unroll
        for (int s = 0; s < 9; ++s) {
            if (s == 0) WAITVM(12); else if (s < 7) WAITVM(8); else if (s == 7) WAITVM(4); else WAITVM(0);
            BAR();
            if (s >= 1 && s + 3 <= 8) a1_issue(s + 3);
            if (s == 3 && tid == 0) __hip_atomic_store((GAS u64*)(ws + WS_KFLAG) + bc * 32 + br, nonce, RLX);
            const unsigned sl = L0 + (s & 3) * 32768;
#pragma unroll
            for (int kk = 0; kk < 2; ++kk) {
                u32x4 xf[2];
                xf[0] = lds128(sl + xo + (kk ? xs1 : xs0));
                xf[1] = lds128(sl + xo + 2048 + (kk ? xs1 : xs0));
                u32x2 a, c;
                u32x4 fq, fk, fv;
                a = ldstr(sl + wq + kk * 8192); c = ldstr(sl + wq + kk * 8192 + 1024); fq = u32x4{a[0], a[1], c[0], c[1]};
                a = ldstr(sl + wk + kk * 8192); c = ldstr(sl + wk + kk * 8192 + 1024); fk = u32x4{a[0], a[1], c[0], c[1]};
                a = ldstr(sl + wv + kk * 8192); c = ldstr(sl + wv + kk * 8192 + 1024); fv = u32x4{a[0], a[1], c[0], c[1]};
#pragma unroll
                for (int t = 0; t < 2; ++t) { aq[t] = mfma16(fq, xf[t], aq[t]); ak[t] = mfma16(fk, xf[t], ak[t]); av[t] = mfma16(xf[t], fv, av[t]); }
                if (nh == 0) {
                    a = ldstr(sl + wr + kk * 8192); c = ldstr(sl + wr + kk * 8192 + 1024);
                    u32x4 fr = u32x4{a[0], a[1], c[0], c[1]};
#pragma unroll
                    for (int t = 0; t < 2; ++t) ar[t] = mfma16(fr, xf[t], ar[t]);
                }
            }
        }
    }
    STAMP(3);
    BAR();
    {
        const float qs = 0.34004634f;
#pragma unroll
        for (int t = 0; t < 2; ++t) {
            const unsigned ko = tg * 1024 + (g4 >> 1) * 512 + (16 * t + l15) * 16 + (g4 & 1) * 8;
            st64(L0 + L_OWN + nh * HEADB + ko, u32x2{pk(ak[t][0], ak[t][1]), pk(ak[t][2], ak[t][3])});
            st64(L0 + L_Q + nh * 4608 + ko, u32x2{pk(aq[t][0] * qs, aq[t][1] * qs), pk(aq[t][2] * qs, aq[t][3] * qs)});
            const int tt = 2 * tg + t;
            st64(L0 + L_OWN + nh * HEADB + 4608 + tt * 608 + (g4 & 1) * 304 + l15 * 16 + (g4 >> 1) * 8, u32x2{pk(av[t][0], av[t][1]), pk(av[t][2], av[t][3])});
            if (nh == 0) {
                const int tok = 32 * tg + 16 * t + l15;
                if (g4 == 0) {
                    st32(L0 + L_Q + 4096 + tok * 4, pk(ar[t][0] * qs, ar[t][1] * qs));
                    st32(L0 + L_Q + 4608 + 4096 + tok * 4, pk(ar[t][2] * qs, ar[t][3] * qs));
                } else if (g4 == 1) {
                    st32(L0 + L_OWN + 4096 + tok * 4, pk(ar[t][0], ar[t][1]));
                    st32(L0 + L_OWN + HEADB + 4096 + tok * 4, pk(ar[t][2], ar[t][3]));
                } else if (g4 == 2) {
                    const unsigned p01 = pk(ar[t][0], ar[t][1]), p23 = pk(ar[t][2], ar[t][3]);
                    const unsigned vb = L0 + L_OWN + 4608 + tt * 608 + ((l15 >> 2) & 1) * 304 + 16 * 16 + (4 * (l15 >> 3) + (l15 & 3)) * 2;
                    st16(vb, (unsigned short)p01); st16(vb + 16, (unsigned short)(p01 >> 16));
                    st16(vb + HEADB, (unsigned short)p23); st16(vb + HEADB + 16, (unsigned short)(p23 >> 16));
                }
            } else if (lane < 4) {
                st128(L0 + L_OWN + (lane >> 1) * HEADB + 4608 + tt * 608 + (lane & 1) * 304 + 18 * 16, u32x4{0x3f803f80u, 0x3f803f80u, 0x3f803f80u, 0x3f803f80u});
            }
        }
    }
    {
        auto kcopy = [&](unsigned kb, u32x4 d0, u32x4 d1) {
            st128(kb + (4 + cq) * 16, d0);
            st128(kb + KCS + (4 + cq) * 16, u32x4{d0[1], d0[2], d0[3], d1[0]});
            st128(kb + 2 * KCS + (4 + cq) * 16, u32x4{d0[2], d0[3], d1[0], d1[1]});
            st128(kb + 3 * KCS + (4 + cq) * 16, u32x4{d0[3], d1[0], d1[1], d1[2]});
            if (cq == 0) {
                st128(kb + 3 * 16, u32x4{0, 0, 0, 0});
                st128(kb + KCS + 3 * 16, u32x4{0, 0, 0, d0[0]});
                st128(kb + 2 * KCS + 3 * 16, u32x4{0, 0, d0[0], d0[1]});
                st128(kb + 3 * KCS + 3 * 16, u32x4{0, d0[0], d0[1], d0[2]});
            } else if (cq < 4) {
#pragma unroll
                for (int c = 0; c < 4; ++c) st128(kb + c * KCS + (cq - 1) * 16, u32x4{0, 0, 0, 0});
            }
        };
        kcopy(L0 + L_KC + cu * KCH, ka0, ka1);
        if (nch == 3 && tid < 256) kcopy(L0 + L_KC + 2 * KCH, kb0, kb1);
    }
    BAR();
    {
        const int gb = WS_KV + (bi * 16 + gp) * KVB;
#pragma unroll
        for (int k = 0; k < 3; ++k) {
            const int q = tid + 512 * k;
            if (q < 1184) __builtin_amdgcn_raw_buffer_store_b128(lds128(L0 + L_OWN + q * 16), wsr, gb + q * 16, 0, 16);
        }
    }
    if (w == 0) {
        poll_flags((const GAS u64*)(ws + WS_KFLAG) + ((2 * b) / 72) * 32, 32, nonce, lane);
        if (nch == 3) poll_flags((const GAS u64*)(ws + WS_KFLAG) + (ch2 / 72) * 32, 32, nonce, lane);
    }
    WAITVM(0);
    BAR();
    if (tid == 0) __hip_atomic_store((GAS u64*)(ws + WS_KVFLAG) + gp * 16 + bi, nonce, RLX);
    STAMP(4);
    {
        auto rcopy = [&](unsigned rb, int ch) {
            const int off = WS_R0 + ch * 4096 + cq * 16;
            u32x4 d = __builtin_amdgcn_raw_buffer_load_b128(wsr, off, 0, 16);
            unsigned nx = 0;
            if (cq < 255) nx = __builtin_amdgcn_raw_buffer_load_b32(wsr, off + 16, 0, 16);
            st128(rb + cq * 16, d);
            st128(rb + RCS + cq * 16, u32x4{__builtin_amdgcn_alignbit(d[1], d[0], 16), __builtin_amdgcn_alignbit(d[2], d[1], 16), __builtin_amdgcn_alignbit(d[3], d[2], 16), __builtin_amdgcn_alignbit(nx, d[3], 16)});
            if (cq < 30) { st128(rb + (256 + cq) * 16, u32x4{0, 0, 0, 0}); st128(rb + RCS + (256 + cq) * 16, u32x4{0, 0, 0, 0}); }
        };
        rcopy(L0 + L_RB + cu * RCH, ch01);
        if (nch == 3 && tid < 256) rcopy(L0 + L_RB + 2 * RCH, ch2);
    }
    BAR();
    STAMP(5);

    {
        const int slot = w < 4 ? (w >> 1) : 2;
        const int rho0 = w < 4 ? 4 * (w & 1) : 2 * (w - 4);
        const int nrho = w < 4 ? 4 : (nch == 3 ? 2 : 0);
        f32x4 acc[8];
#pragma unroll
        for (int n = 0; n < 8; ++n) acc[n] = f32x4{0, 0, 0, 0};
        const unsigned la = L0 + L_KC + slot * KCH + ((l15 + 1) & 3) * KCS + (g4 + ((l15 + 1) >> 2)) * 16;
        const unsigned lb = L0 + L_RB + slot * RCH + ((l15 & 1) ? 0 : RCS) + (252 - 28 * 8 - 4 * (l15 >> 1) + g4) * 16;
        for (int rr = 0; rr < nrho; ++rr) {
            const unsigned ro = (rho0 + rr) * 64;
            u32x4 A[8], B[8];
#pragma unroll
            for (int m = 0; m < 8; ++m) { A[m] = lds128(la + ro + m * 512); B[m] = lds128(lb + ro + (7 - m) * 512); }
#pragma unroll
            for (int nu = 0; nu < 8; ++nu)
#pragma unroll
                for (int mu = 0; mu + nu < 8; ++mu) acc[mu + nu] = mfma16(A[mu], B[nu], acc[mu + nu]);
        }
        BAR();
        const unsigned sc = L0 + L_KC + slot * KCH;
        if (w < 4) {
            const int h = w & 1;
            if (h) {
#pragma unroll
                for (int n = 0; n < 4; ++n) *(LAS f32x4*)(sc + 4096 + n * 1024 + lane * 16) = acc[n];
            } else {
#pragma unroll
                for (int n = 0; n < 4; ++n) *(LAS f32x4*)(sc + n * 1024 + lane * 16) = acc[4 + n];
            }
        } else if (nch == 3) {
            const int h = w - 4;
#pragma unroll
            for (int n = 0; n < 4; ++n) *(LAS f32x4*)(sc + h * 4096 + n * 1024 + lane * 16) = acc[n];
        }
        BAR();
        f32x4 y[4];
        if (w < 4) {
            const int h = w & 1;
            if (h) {
#pragma unroll
                for (int n = 0; n < 4; ++n) y[n] = acc[4 + n] + *(LAS const f32x4*)(sc + n * 1024 + lane * 16);
            } else {
#pragma unroll
                for (int n = 0; n < 4; ++n) y[n] = acc[n] + *(LAS const f32x4*)(sc + 4096 + n * 1024 + lane * 16);
            }
        } else if (nch == 3) {
            const int h = w - 4;
            f32x4 s4 = f32x4{0, 0, 0, 0};
#pragma unroll
            for (int q = 0; q < 4; ++q) s4 += *(LAS const f32x4*)(sc + q * 4096 + h * 1024 + lane * 16);
            y[0] = s4;
        }
        BAR();
        if (w >= 4 && nch == 3) {
            const int h = w - 4;
#pragma unroll
            for (int n = 0; n < 4; ++n) *(LAS f32x4*)(sc + h * 4096 + n * 1024 + lane * 16) = acc[4 + n];
        }
        BAR();
        if (w >= 4 && nch == 3) {
            const int h = w - 4;
            f32x4 s4 = f32x4{0, 0, 0, 0};
#pragma unroll
            for (int q = 0; q < 4; ++q) s4 += *(LAS const f32x4*)(sc + q * 4096 + h * 1024 + lane * 16);
            y[1] = s4;
        }
        BAR();
        const unsigned ys = L0 + L_RB + slot * RCH;
        const unsigned yo = (l15 >> 1) * 64 + g4 * 16 + (l15 & 1) * 8;
        const int chs = slot == 2 ? ch2 : 2 * b + slot;
        if (w < 4) {
            const int h = w & 1;
#pragma unroll
            for (int n = 0; n < 4; ++n) st64(ys + (4 * h + n) * 512 + yo, u32x2{pk(y[n][0], y[n][1]), pk(y[n][2], y[n][3])});
#pragma unroll
            for (int k = 0; k < 2; ++k)
                __builtin_amdgcn_raw_buffer_store_b128(lds128(ys + h * 2048 + k * 1024 + lane * 16), wsr, WS_Y + chs * 4096 + h * 2048 + k * 1024 + lane * 16, 0, 16);
        } else if (nch == 3) {
            const int h = w - 4;
            st64(ys + h * 512 + yo, u32x2{pk(y[0][0], y[0][1]), pk(y[0][2], y[0][3])});
            st64(ys + (4 + h) * 512 + yo, u32x2{pk(y[1][0], y[1][1]), pk(y[1][2], y[1][3])});
            if (lane < 32) __builtin_amdgcn_raw_buffer_store_b128(lds128(ys + h * 512 + lane * 16), wsr, WS_Y + chs * 4096 + h * 512 + lane * 16, 0, 16);
            else __builtin_amdgcn_raw_buffer_store_b128(lds128(ys + (4 + h) * 512 + (lane - 32) * 16), wsr, WS_Y + chs * 4096 + (4 + h) * 512 + (lane - 32) * 16, 0, 16);
        }
    }
    if (w == 0 && bi > 0) poll_flags((const GAS u64*)(ws + WS_KVFLAG) + gp * 16 + (bi > 1 ? bi - 2 : 0), bi > 1 ? 2 : 1, nonce, lane);
    WAITVM(0);
    BAR();
    if (tid < nch) __hip_atomic_store((GAS u64*)(ws + WS_YFLAG) + (tid == 2 ? ch2 : 2 * b + tid), nonce, RLX);
    STAMP(6);
    {
        for (int hb = 0; hb < 2; ++hb) {
            const int ib = bi - 2 + hb;
            if (ib >= 0) {
                const int gb = WS_KV + (ib * 16 + gp) * KVB;
#pragma unroll
                for (int k = 0; k < 3; ++k) {
                    const int q = tid + 512 * k;
                    if (q < 1184) st128(L0 + hb * KVB + q * 16, __builtin_amdgcn_raw_buffer_load_b128(wsr, gb + q * 16, 0, 16));
                }
            }
        }
    }
    for (int u = w; u < 81; u += 8) {
        const int q = u * 64 + lane, row = q / 9, cc = q - row * 9;
        dma16((const char*)wo + row * 1152 + bc * 144 + cc * 16, L0 + L_WO + u * 1024);
    }
    BAR();
    STAMP(7);

    {
        const int hd = w >> 2, qt = w & 3, l31 = lane & 31, hh = lane >> 5;
        const u32x4 qm = lds128(L0 + L_Q + hd * 4608 + qt * 1024 + hh * 512 + l31 * 16);
        const unsigned qrv = lds32(L0 + L_Q + hd * 4608 + 4096 + (32 * qt + l31) * 4);
        const u32x4 qr = u32x4{hh ? 0u : qrv, 0, 0, 0};
        const int vrow = l31 < 19 ? l31 : (l31 == 19 ? 18 : (l31 < 28 ? 0 : 4));
        const unsigned kbase = L0 + hd * HEADB + hh * 512 + l31 * 16, krbase = L0 + hd * HEADB + 4096 + l31 * 4;
        const unsigned vbase = L0 + hd * HEADB + 4608 + hh * 304 + vrow * 16;
        const int qq = l31 - 4 * hh;
        const int t_first = bi >= 2 ? qt : (bi == 1 ? 4 : 8), t_last = 8 + qt;
        const bool has_first = bi >= 2;
        auto qk = [&](int t, f32x16 c) {
            const unsigned bo = (t >> 2) * KVB;
            u32x4 km = lds128(kbase + bo + (t & 3) * 1024);
            unsigned kr = lds32(krbase + bo + (t & 3) * 128);
            c = mfma32(km, qm, c);
            return mfma32(u32x4{kr, 0, 0, 0}, qr, c);
        };
        auto pv = [&](int t, const f32x16& p, f32x16 o) {
            const unsigned bo = (t >> 2) * KVB + (t & 3) * 1216;
            u32x4 v0 = lds128(vbase + bo), v1 = lds128(vbase + bo + 608);
            u32x4 p0 = u32x4{pk(p[0], p[1]), pk(p[2], p[3]), pk(p[4], p[5]), pk(p[6], p[7])};
            u32x4 p1 = u32x4{pk(p[8], p[9]), pk(p[10], p[11]), pk(p[12], p[13]), pk(p[14], p[15])};
            o = mfma32(v0, p0, o);
            return mfma32(v1, p1, o);
        };
        const float NINF = -__builtin_inff();
        f32x16 z16;
#pragma unroll
        for (int r = 0; r < 16; ++r) z16[r] = 0.f;
        f32x16 sd = qk(t_last, z16);
        float m = NINF;
#pragma unroll
        for (int r = 0; r < 16; ++r) { const int c = (r & 3) + 8 * (r >> 2); sd[r] = c <= qq ? sd[r] : NINF; m = fmaxf(m, sd[r]); }
        m = fmaxf(m, __shfl_xor(m, 32));
        f32x16 o;
        float l;
        for (int attempt = 0; attempt < 2; ++attempt) {
            f32x16 nm, p;
#pragma unroll
            for (int r = 0; r < 16; ++r) { nm[r] = -m; p[r] = __builtin_amdgcn_exp2f(sd[r] - m); }
            o = pv(t_last, p, z16);
            for (int t = t_first; t < t_last; ++t) {
                f32x16 s = qk(t, nm);
                if (has_first && t == t_first) {
#pragma unroll
                    for (int r = 0; r < 16; ++r) { const int c = (r & 3) + 8 * (r >> 2); s[r] = c > qq ? s[r] : NINF; }
                }
#pragma unroll
                for (int r = 0; r < 16; ++r) p[r] = __builtin_amdgcn_exp2f(s[r]);
                o = pv(t, p, o);
            }
            l = __shfl(o[10], l31);
            if (attempt == 1 || __all(l <= 1048576.f)) break;
            for (int t = t_first; t < t_last; ++t) {
                f32x16 s = qk(t, z16);
#pragma unroll
                for (int r = 0; r < 16; ++r) { const int c = (r & 3) + 8 * (r >> 2); const bool keep = !(has_first && t == t_first) || c > qq; m = fmaxf(m, keep ? s[r] : NINF); }
            }
            m = fmaxf(m, __shfl_xor(m, 32));
        }
        const float il = 1.f / l;
        const int T16 = 8 * bi + 2 * qt + (l31 >> 4);
        u32x4 om = u32x4{pk(o[0] * il, o[1] * il), pk(o[2] * il, o[3] * il), pk(o[4] * il, o[5] * il), pk(o[6] * il, o[7] * il)};
        __builtin_amdgcn_raw_buffer_store_b128(om, wsr, WS_OM + ((T16 * 16 + gp) * 64 + (2 * hd + hh) * 16 + (l31 & 15)) * 16, 0, 16);
        if (hh == 0) __builtin_amdgcn_raw_buffer_store_b32(pk(o[8] * il, o[9] * il), wsr, WS_OR + (((T16 * 16 + gp) * 2 + hd) * 16 + (l31 & 15)) * 4, 0, 16);
    }
    WAITVM(0);
    BAR();
    if (tid == 0) __hip_atomic_store((GAS u64*)(ws + WS_OFLAG) + bi * 16 + gp, nonce, RLX);
    STAMP(8);

    if (w == 0) { poll_flags((const GAS u64*)(ws + WS_YFLAG) + 72 * bc, 64, nonce, lane); poll_flags((const GAS u64*)(ws + WS_YFLAG) + 72 * bc + 64, 8, nonce, lane); }
    BAR();
    if (tid < 288) {
        const int t0 = 64 * br + 8 * oct;
        u32x4 y0 = __builtin_amdgcn_raw_buffer_load_b128(wsr, WS_Y + chB * 4096 + t0 * 2, 0, 16);
        u32x4 y1 = __builtin_amdgcn_raw_buffer_load_b128(wsr, WS_Y + (chB + 1) * 4096 + t0 * 2, 0, 16);
        const unsigned dv = *(const unsigned*)(ssd + chB);
        const float D0 = blo(dv), D1 = bhi(dv);
#pragma unroll
        for (int t = 0; t < 8; ++t) {
            const int slot = (t & 1) * 4 + (t >> 1);
            const float ya = (slot & 1) ? bhi(y0[slot >> 1]) : blo(y0[slot >> 1]);
            const float yb = (slot & 1) ? bhi(y1[slot >> 1]) : blo(y1[slot >> 1]);
            const unsigned qv = *(const unsigned*)(bq + (size_t)(t0 + t) * 576 + chB);
            const unsigned kd = pk(blo(kvp[t]) * D0, bhi(kvp[t]) * D1);
            const unsigned zz = pk(ya + blo(kd), yb + bhi(kd));
            *(unsigned*)(out + (size_t)(t0 + t) * 1152 + 576 + chB) = pk(blo(qv) * blo(zz), bhi(qv) * bhi(zz));
        }
    }
    STAMP(9);

    if (w == 0) poll_flags((const GAS u64*)(ws + WS_OFLAG) + (br >> 1) * 16, 16, nonce, lane);
    WAITVM(0);
    BAR();
    STAMP(10);
    {
        const int tt = w & 3, kh = w >> 2, T16 = 4 * br + tt;
        u32x4 of[9];
#pragma unroll
        for (int s = 0; s < 8; ++s) of[s] = __builtin_amdgcn_raw_buffer_load_b128(wsr, WS_OM + ((T16 * 16 + 8 * kh + s) * 64 + lane) * 16, 0, 16);
#pragma unroll
        for (int d = 0; d < 4; ++d) of[8][d] = __builtin_amdgcn_raw_buffer_load_b32(wsr, WS_OR + (((T16 * 16 + 8 * kh + 2 * g4 + (d >> 1)) * 2 + (d & 1)) * 16 + l15) * 4, 0, 16);
        f32x4 acc[5];
#pragma unroll
        for (int j = 0; j < 5; ++j) acc[j] = f32x4{0, 0, 0, 0};
        const int q4 = (lane >> 2) & 3, p4 = lane & 3, hd = g4 >> 1, hh = g4 & 1;
        const unsigned mrow = L0 + L_WO + (288 * kh + 20 * hd + 4 * hh + q4) * 144;
        const unsigned rrow = L0 + L_WO + (288 * kh + 72 * g4 + 16 + q4) * 144;
#pragma unroll
        for (int s = 0; s < 9; ++s) {
#pragma unroll
            for (int j = 0; j < 5; ++j) {
                const unsigned col = j < 4 ? j * 32 + p4 * 8 : 128 + (p4 & 1) * 8;
                u32x2 a, c;
                if (s < 8) { a = ldstr(mrow + s * 36 * 144 + col); c = ldstr(mrow + (s * 36 + 8) * 144 + col); }
                else { a = ldstr(rrow + col); c = ldstr(rrow + 36 * 144 + col); }
                acc[j] = mfma16(u32x4{a[0], a[1], c[0], c[1]}, of[s], acc[j]);
            }
        }
        const unsigned sc = L0 + tt * 5120;
        if (kh == 1) {
#pragma unroll
            for (int j = 0; j < 5; ++j) *(LAS f32x4*)(sc + j * 1024 + lane * 16) = acc[j];
        }
        BAR();
        if (kh == 0) {
            unsigned short* op = out + (size_t)(16 * T16 + l15) * 1152 + 72 * bc + 4 * g4;
#pragma unroll
            for (int j = 0; j < 5; ++j) {
                f32x4 v = acc[j] + *(LAS const f32x4*)(sc + j * 1024 + lane * 16);
                if (j < 4 || g4 < 2) *(u32x2*)(op + 16 * j) = u32x2{pk(v[0], v[1]), pk(v[2], v[3])};
            }
        }
    }
    if (b == 0 && w == 7) {
        for (int k = 0; k < 4; ++k) poll_flags((const GAS u64*)(ws + WS_KFLAG) + 64 * k, 64, nonce, lane);
        if (lane == 0) __hip_atomic_store((GAS u64*)ws, nonce, RLX);
    }
    STAMP(11);
}

extern "C" void kernel_launch(void* const* d_in, const int* in_sizes, int n_in, void* d_out, int out_size, void* d_ws, size_t ws_size, hipStream_t stream) {
    fused_kernel<<<256, 512, 0, stream>>>((const unsigned short*)d_in[0], (const unsigned short*)d_in[1], (const unsigned short*)d_in[2], (const unsigned short*)d_in[3],
                                          (const unsigned short*)d_in[4], (const unsigned short*)d_in[5], (const unsigned short*)d_in[6], (const unsigned short*)d_in[7],
                                          (const unsigned short*)d_in[8], (unsigned short*)d_out, (char*)d_ws);
}
```

```cpp
#include <hip/hip_runtime.h>
#include <stdint.h>

#define GAS __attribute__((address_space(1)))
#define LAS __attribute__((address_space(3)))
#define DEVI __device__ __forceinline__
typedef unsigned long long u64;
typedef __attribute__((ext_vector_type(8))) __bf16 bf16x8;
typedef __attribute__((ext_vector_type(2))) __bf16 bf16x2;
typedef __attribute__((ext_vector_type(2))) float f32x2;
typedef __attribute__((ext_vector_type(4))) float f32x4;
typedef __attribute__((ext_vector_type(16))) float f32x16;
typedef __attribute__((ext_vector_type(4))) unsigned u32x4;
typedef __attribute__((ext_vector_type(2))) unsigned u32x2;
typedef __attribute__((ext_vector_type(4))) short s16x4;

constexpr int WS_KFLAG = 256, WS_KVFLAG = 4096, WS_OFLAG = 8192, WS_YFLAG = 12288;
constexpr int WS_R0 = 32768;
constexpr int WS_KV = WS_R0 + 2359296;
constexpr int WS_OM = WS_KV + 4849664;
constexpr int WS_OR = WS_OM + 2097152;
constexpr int WS_Y = WS_OR + 262144;
constexpr int WS_END = WS_Y + 2359296;
constexpr int WS_TS = 16 << 20;
constexpr int L_B1K = 131072, L_B1V = 141312;
constexpr int KVB = 18944, HEADB = 9472;
constexpr int L_OWN = 2 * KVB, L_Q = 3 * KVB;
constexpr int L_KC = 66048, KCS = 4160, KCH = 4 * KCS;
constexpr int L_RB = 131072, RCS = 4576, RCH = 2 * RCS;
constexpr int L_WO = 66048;
constexpr int LDS_BYTES = 163840;
constexpr unsigned SPIN_CAP = 1u << 17;

#define STAMP(k) do { } while (0)

DEVI f32x4 mfma16(u32x4 a, u32x4 b, f32x4 c) { return __builtin_amdgcn_mfma_f32_16x16x32_bf16(__builtin_bit_cast(bf16x8, a), __builtin_bit_cast(bf16x8, b), c, 0, 0, 0); }
DEVI f32x16 mfma32(u32x4 a, u32x4 b, f32x16 c) { return __builtin_amdgcn_mfma_f32_32x32x16_bf16(__builtin_bit_cast(bf16x8, a), __builtin_bit_cast(bf16x8, b), c, 0, 0, 0); }
DEVI unsigned pk(float lo, float hi) { f32x2 v = {lo, hi}; return __builtin_bit_cast(unsigned, __builtin_convertvector(v, bf16x2)); }
DEVI float blo(unsigned v) { return __builtin_bit_cast(float, v << 16); }
DEVI float bhi(unsigned v) { return __builtin_bit_cast(float, v & 0xffff0000u); }
DEVI float rndbf(float x) { return blo(pk(x, x)); }
DEVI u32x4 lds128(unsigned a) { return *(LAS const u32x4*)a; }
DEVI u32x2 lds64(unsigned a) { return *(LAS const u32x2*)a; }
DEVI unsigned lds32(unsigned a) { return *(LAS const unsigned*)a; }
DEVI void st128(unsigned a, u32x4 v) { *(LAS u32x4*)a = v; }
DEVI void st64(unsigned a, u32x2 v) { *(LAS u32x2*)a = v; }
DEVI void st32(unsigned a, unsigned v) { *(LAS unsigned*)a = v; }
DEVI void st16(unsigned a, unsigned short v) { *(LAS unsigned short*)a = v; }
DEVI u32x2 ldstr(unsigned a) { return __builtin_bit_cast(u32x2, __builtin_amdgcn_ds_read_tr16_b64_v4i16((LAS s16x4*)a)); }
DEVI void dma16(const void* g, unsigned lds) {
    unsigned sv;
    asm volatile("s_mov_b32 %0, m0\n\ts_mov_b32 m0, %2\n\ts_nop 0\n\tglobal_load_lds_dwordx4 %1, off\n\ts_mov_b32 m0, %0" : "=&s"(sv) : "v"(g), "s"(lds) : "memory");
}
DEVI u32x4 gld128(const void* p) { u32x4 v; asm volatile("global_load_dwordx4 %0, %1, off" : "=v"(v) : "v"(p) : "memory"); return v; }
#define WAITVM(n) asm volatile("s_waitcnt vmcnt(%0)" ::"i"(n) : "memory")
#define TIE(x) asm volatile("" : "+v"(x))
#define BAR() do { asm volatile("s_waitcnt lgkmcnt(0)\n\ts_barrier" ::: "memory"); } while (0)
#define RLX __ATOMIC_RELAXED, __HIP_MEMORY_SCOPE_AGENT

DEVI void poll_flags(const GAS u64* f, int n, u64 nonce, int lane) {
    for (unsigned spins = 0; spins < SPIN_CAP; ++spins) {
        u64 v = nonce;
        if (lane < n) v = __hip_atomic_load(f + lane, RLX);
        if (__all(v == nonce)) break;
        __builtin_amdgcn_s_sleep(2);
    }
    __builtin_amdgcn_fence(__ATOMIC_ACQUIRE, "wavefront");
    asm volatile("" ::: "memory");
}

__global__ __launch_bounds__(512, 2) void fused_kernel(const unsigned short* __restrict__ x, const unsigned short* __restrict__ wqkv, const unsigned short* __restrict__ wo,
                                                       const unsigned short* __restrict__ bq, const unsigned short* __restrict__ bk, const unsigned short* __restrict__ bv,
                                                       const unsigned short* __restrict__ shk, const unsigned short* __restrict__ ssk, const unsigned short* __restrict__ ssd,
                                                       unsigned short* __restrict__ out, char* __restrict__ ws) {
    __shared__ __attribute__((aligned(16))) char smem[LDS_BYTES];
    const unsigned L0 = (unsigned)(uintptr_t)(LAS char*)smem;
    const int tid = threadIdx.x, lane = tid & 63, w = __builtin_amdgcn_readfirstlane(tid >> 6);
    const int b = blockIdx.x, xcd = b & 7, jj = b >> 3;
    const int bi = 4 * (xcd >> 1) + (jj & 3), gp = 8 * (xcd & 1) + (jj >> 2);
    const int br = 4 * xcd + (jj & 3), bc = jj >> 2;
    const int l15 = lane & 15, g4 = lane >> 4;
    const __amdgpu_buffer_rsrc_t wsr = __builtin_amdgcn_make_buffer_rsrc(ws, 0, WS_END, 0x00020000);
    const u64 pt0 = __builtin_amdgcn_s_memrealtime();
    if (tid == 0) __hip_atomic_store((GAS u64*)(ws + WS_TS) + b, pt0, RLX);

    u32x2 ep; asm volatile("global_load_dwordx2 %0, %1, off sc1" : "=v"(ep) : "v"(ws) : "memory");
    const int nch = (b & 3) == 0 ? 3 : 2;
    const int cu = tid >> 8, cq = tid & 255;
    const int ch01 = 2 * b + cu, ch2 = 512 + (b >> 2);
    u32x4 ka0, ka1, kb0, kb1;
    {
        const char* p = (const char*)ssk + ch01 * 4096 + cq * 16;
        ka0 = gld128(p); ka1 = gld128(p + (cq < 255 ? 16 : 0));
        const char* p2 = (const char*)ssk + ch2 * 4096 + cq * 16;
        kb0 = gld128(p2); kb1 = gld128(p2 + (cq < 255 ? 16 : 0));
    }
    const int cp = tid >> 3, oct = tid & 7;
    const int chB = 72 * bc + 2 * (cp < 36 ? cp : 35);
    u32x4 shv = gld128((const char*)shk + chB * 8);
    for (int u = w; u < 19; u += 8) {
        if (u < 10) {
            int q = u * 64 + lane; q = q < 612 ? q : 611;
            int row = q / 9, cc = q - row * 9; int tok = 64 * br - 4 + row; tok = tok < 0 ? 0 : tok;
            dma16((const char*)bk + tok * 1152 + bc * 144 + cc * 16, L0 + L_B1K + u * 1024);
        } else {
            int q = (u - 10) * 64 + lane; int row = q / 9, cc = q - row * 9;
            dma16((const char*)bv + (64 * br + row) * 1152 + bc * 144 + cc * 16, L0 + L_B1V + (u - 10) * 1024);
        }
    }
    const char* xsrc = (const char*)x + (size_t)(128 * bi + 16 * w + (lane >> 3)) * 1152 + (((lane & 7) ^ (lane >> 3)) * 16);
    const char* wsrc;
    {
        int swz = 2 * (g4 | ((w & 1) << 2));
        int lc = l15 ^ swz; lc = lc < 15 ? lc : 14;
        int sec = lc / 5, cc = lc - sec * 5;
        wsrc = (const char*)wqkv + (size_t)(8 * w + g4) * 3456 + sec * 1152 + 72 * gp - (gp & 1) * 8 + cc * 16;
    }
    auto a1_issue = [&](int s) {
        const unsigned sl = L0 + (s & 3) * 32768;
        dma16(xsrc + s * 128, sl + (16 * w) * 128);
        dma16(xsrc + s * 128 + 8 * 1152, sl + (16 * w + 8) * 128);
        dma16(wsrc + (size_t)s * 64 * 3456, sl + 16384 + (8 * w) * 256);
        dma16(wsrc + (size_t)s * 64 * 3456 + 4 * 3456, sl + 16384 + (8 * w + 4) * 256);
    };
    a1_issue(0); a1_issue(1); a1_issue(2); a1_issue(3);

    WAITVM(16);
    TIE(ka0); TIE(ka1); TIE(kb0); TIE(kb1); TIE(shv); TIE(ep);
    const u64 nonce = (((u64)(unsigned)__builtin_amdgcn_readfirstlane(ep[1]) << 32) | (unsigned)__builtin_amdgcn_readfirstlane(ep[0])) + 1;
    BAR();
    STAMP(1);
    unsigned kvp[8];
    if (tid < 288) {
        float k0[11], k1[11];
#pragma unroll
        for (int q = 0; q < 11; ++q) { unsigned v = lds32(L0 + L_B1K + (8 * oct + 1 + q) * 144 + cp * 4); k0[q] = blo(v); k1[q] = bhi(v); }
        if (br == 0 && oct == 0) { k0[0] = k0[1] = k0[2] = 0.f; k1[0] = k1[1] = k1[2] = 0.f; }
        const float s00 = blo(shv[0]), s01 = bhi(shv[0]), s02 = blo(shv[1]), s03 = bhi(shv[1]);
        const float s10 = blo(shv[2]), s11 = bhi(shv[2]), s12 = blo(shv[3]), s13 = bhi(shv[3]);
#pragma unroll
        for (int t = 0; t < 8; ++t) {
            unsigned v = lds32(L0 + L_B1V + (8 * oct + t) * 144 + cp * 4);
            float a0 = k0[t] * s03; a0 = fmaf(k0[t + 1], s02, a0); a0 = fmaf(k0[t + 2], s01, a0); a0 = fmaf(k0[t + 3], s00, a0);
            float a1 = k1[t] * s13; a1 = fmaf(k1[t + 1], s12, a1); a1 = fmaf(k1[t + 2], s11, a1); a1 = fmaf(k1[t + 3], s10, a1);
            unsigned ks = pk(a0, a1);
            kvp[t] = pk(blo(ks) * blo(v), bhi(ks) * bhi(v));
        }
        u32x4 r0, r1;
#pragma unroll
        for (int d = 0; d < 4; ++d) {
            r0[d] = (kvp[7 - 2 * d] & 0xffffu) | (kvp[6 - 2 * d] << 16);
            r1[d] = (kvp[7 - 2 * d] >> 16) | (kvp[6 - 2 * d] & 0xffff0000u);
        }
        const int off = WS_R0 + chB * 4096 + (2040 - 64 * br - 8 * oct) * 2;
        __builtin_amdgcn_raw_buffer_store_b128(r0, wsr, off, 0, 16);
        __builtin_amdgcn_raw_buffer_store_b128(r1, wsr, off + 4096, 0, 16);
    } else {
#pragma unroll
        for (int t = 0; t < 8; ++t) kvp[t] = 0;
    }
    STAMP(2);

    const int tg = w & 3, nh = w >> 2;
    f32x4 aq[2], ak[2], av[2], ar[2];
#pragma unroll
    for (int t = 0; t < 2; ++t) { aq[t] = f32x4{0, 0, 0, 0}; ak[t] = aq[t]; av[t] = aq[t]; ar[t] = aq[t]; }
    {
        const int q4 = (lane >> 2) & 3, p4 = lane & 3, po = (gp & 1) * 8;
        const int swz = 2 * (q4 | ((g4 & 1) << 2));
        auto woff = [&](int sec, int piece) { int byte = sec * 80 + po + 8 * piece; return (unsigned)(16384 + (8 * g4 + q4) * 256 + (((byte >> 4) ^ swz) * 16) + (byte & 8)); };
        const unsigned wq = woff(0, 5 * nh + p4), wk = woff(1, 5 * nh + p4), wv = woff(2, 5 * nh + p4), wr = woff(p4 < 3 ? p4 : 2, 4);
        const unsigned xo = (32 * tg + l15) * 128;
        const unsigned xs0 = ((g4 ^ (lane & 7)) * 16), xs1 = (((4 + g4) ^ (lane & 7)) * 16);
#pragma unroll
        for (int s = 0; s < 9; ++s) {
            if (s == 0) WAITVM(12); else if (s < 7) WAITVM(8); else if (s == 7) WAITVM(4); else WAITVM(0);
            BAR();
            if (s >= 1 && s + 3 <= 8) a1_issue(s + 3);
            if (s == 3 && tid == 0) __hip_atomic_store((GAS u64*)(ws + WS_KFLAG) + bc * 32 + br, nonce, RLX);
            const unsigned sl = L0 + (s & 3) * 32768;
#pragma unroll
            for (int kk = 0; kk < 2; ++kk) {
                u32x4 xf[2];
                xf[0] = lds128(sl + xo + (kk ? xs1 : xs0));
                xf[1] = lds128(sl + xo + 2048 + (kk ? xs1 : xs0));
                u32x2 a, c;
                u32x4 fq, fk, fv;
                a = ldstr(sl + wq + kk * 8192); c = ldstr(sl + wq + kk * 8192 + 1024); fq = u32x4{a[0], a[1], c[0], c[1]};
                a = ldstr(sl + wk + kk * 8192); c = ldstr(sl + wk + kk * 8192 + 1024); fk = u32x4{a[0], a[1], c[0], c[1]};
                a = ldstr(sl + wv + kk * 8192); c = ldstr(sl + wv + kk * 8192 + 1024); fv = u32x4{a[0], a[1], c[0], c[1]};
#pragma unroll
                for (int t = 0; t < 2; ++t) { aq[t] = mfma16(fq, xf[t], aq[t]); ak[t] = mfma16(fk, xf[t], ak[t]); av[t] = mfma16(xf[t], fv, av[t]); }
                if (nh == 0) {
                    a = ldstr(sl + wr + kk * 8192); c = ldstr(sl + wr + kk * 8192 + 1024);
                    u32x4 fr = u32x4{a[0], a[1], c[0], c[1]};
#pragma unroll
                    for (int t = 0; t < 2; ++t) ar[t] = mfma16(fr, xf[t], ar[t]);
                }
            }
        }
    }
    STAMP(3);
    BAR();
    {
        const float qs = 0.34004634f;
#pragma unroll
        for (int t = 0; t < 2; ++t) {
            const unsigned ko = tg * 1024 + (g4 >> 1) * 512 + (16 * t + l15) * 16 + (g4 & 1) * 8;
            st64(L0 + L_OWN + nh * HEADB + ko, u32x2{pk(ak[t][0], ak[t][1]), pk(ak[t][2], ak[t][3])});
            st64(L0 + L_Q + nh * 4608 + ko, u32x2{pk(aq[t][0] * qs, aq[t][1] * qs), pk(aq[t][2] * qs, aq[t][3] * qs)});
            const int tt = 2 * tg + t;
            st64(L0 + L_OWN + nh * HEADB + 4608 + tt * 608 + (g4 & 1) * 304 + l15 * 16 + (g4 >> 1) * 8, u32x2{pk(av[t][0], av[t][1]), pk(av[t][2], av[t][3])});
            if (nh == 0) {
                const int tok = 32 * tg + 16 * t + l15;
                if (g4 == 0) {
                    st32(L0 + L_Q + 4096 + tok * 4, pk(ar[t][0] * qs, ar[t][1] * qs));
                    st32(L0 + L_Q + 4608 + 4096 + tok * 4, pk(ar[t][2] * qs, ar[t][3] * qs));
                } else if (g4 == 1) {
                    st32(L0 + L_OWN + 4096 + tok * 4, pk(ar[t][0], ar[t][1]));
                    st32(L0 + L_OWN + HEADB + 4096 + tok * 4, pk(ar[t][2], ar[t][3]));
                } else if (g4 == 2) {
                    const unsigned p01 = pk(ar[t][0], ar[t][1]), p23 = pk(ar[t][2], ar[t][3]);
                    const unsigned vb = L0 + L_OWN + 4608 + tt * 608 + ((l15 >> 2) & 1) * 304 + 16 * 16 + (4 * (l15 >> 3) + (l15 & 3)) * 2;
                    st16(vb, (unsigned short)p01); st16(vb + 16, (unsigned short)(p01 >> 16));
                    st16(vb + HEADB, (unsigned short)p23); st16(vb + HEADB + 16, (unsigned short)(p23 >> 16));
                }
            } else if (lane < 4) {
                st128(L0 + L_OWN + (lane >> 1) * HEADB + 4608 + tt * 608 + (lane & 1) * 304 + 18 * 16, u32x4{0x3f803f80u, 0x3f803f80u, 0x3f803f80u, 0x3f803f80u});
            }
        }
    }
    {
        auto kcopy = [&](unsigned kb, u32x4 d0, u32x4 d1) {
            st128(kb + (4 + cq) * 16, d0);
            st128(kb + KCS + (4 + cq) * 16, u32x4{d0[1], d0[2], d0[3], d1[0]});
            st128(kb + 2 * KCS + (4 + cq) * 16, u32x4{d0[2], d0[3], d1[0], d1[1]});
            st128(kb + 3 * KCS + (4 + cq) * 16, u32x4{d0[3], d1[0], d1[1], d1[2]});
            if (cq == 0) {
                st128(kb + 3 * 16, u32x4{0, 0, 0, 0});
                st128(kb + KCS + 3 * 16, u32x4{0, 0, 0, d0[0]});
                st128(kb + 2 * KCS + 3 * 16, u32x4{0, 0, d0[0], d0[1]});
                st128(kb + 3 * KCS + 3 * 16, u32x4{0, d0[0], d0[1], d0[2]});
            } else if (cq < 4) {
#pragma unroll
                for (int c = 0; c < 4; ++c) st128(kb + c * KCS + (cq - 1) * 16, u32x4{0, 0, 0, 0});
            }
        };
        kcopy(L0 + L_KC + cu * KCH, ka0, ka1);
        if (nch == 3 && tid < 256) kcopy(L0 + L_KC + 2 * KCH, kb0, kb1);
    }
    BAR();
    {
        const int gb = WS_KV + (bi * 16 + gp) * KVB;
#pragma unroll
        for (int k = 0; k < 3; ++k) {
            const int q = tid + 512 * k;
            if (q < 1184) __builtin_amdgcn_raw_buffer_store_b128(lds128(L0 + L_OWN + q * 16), wsr, gb + q * 16, 0, 16);
        }
    }
    if (w == 0) {
        poll_flags((const GAS u64*)(ws + WS_KFLAG) + ((2 * b) / 72) * 32, 32, nonce, lane);
        if (nch == 3) poll_flags((const GAS u64*)(ws + WS_KFLAG) + (ch2 / 72) * 32, 32, nonce, lane);
    }
    WAITVM(0);
    BAR();
    if (tid == 0) __hip_atomic_store((GAS u64*)(ws + WS_KVFLAG) + gp * 16 + bi, nonce, RLX);
    STAMP(4);
    {
        auto rcopy = [&](unsigned rb, int ch) {
            const int off = WS_R0 + ch * 4096 + cq * 16;
            u32x4 d = __builtin_amdgcn_raw_buffer_load_b128(wsr, off, 0, 16);
            unsigned nx = 0;
            if (cq < 255) nx = __builtin_amdgcn_raw_buffer_load_b32(wsr, off + 16, 0, 16);
            st128(rb + cq * 16, d);
            st128(rb + RCS + cq * 16, u32x4{__builtin_amdgcn_alignbit(d[1], d[0], 16), __builtin_amdgcn_alignbit(d[2], d[1], 16), __builtin_amdgcn_alignbit(d[3], d[2], 16), __builtin_amdgcn_alignbit(nx, d[3], 16)});
            if (cq < 30) { st128(rb + (256 + cq) * 16, u32x4{0, 0, 0, 0}); st128(rb + RCS + (256 + cq) * 16, u32x4{0, 0, 0, 0}); }
        };
        rcopy(L0 + L_RB + cu * RCH, ch01);
        if (nch == 3 && tid < 256) rcopy(L0 + L_RB + 2 * RCH, ch2);
    }
    BAR();
    STAMP(5);

    {
        const int slot = w < 4 ? (w >> 1) : 2;
        const int rho0 = w < 4 ? 4 * (w & 1) : 2 * (w - 4);
        const int nrho = w < 4 ? 4 : (nch == 3 ? 2 : 0);
        f32x4 acc[8];
#pragma unroll
        for (int n = 0; n < 8; ++n) acc[n] = f32x4{0, 0, 0, 0};
        const unsigned la = L0 + L_KC + slot * KCH + ((l15 + 1) & 3) * KCS + (g4 + ((l15 + 1) >> 2)) * 16;
        const unsigned lb = L0 + L_RB + slot * RCH + ((l15 & 1) ? 0 : RCS) + (252 - 28 * 8 - 4 * (l15 >> 1) + g4) * 16;
        for (int rr = 0; rr < nrho; ++rr) {
            const unsigned ro = (rho0 + rr) * 64;
            u32x4 A[8], B[8];
#pragma unroll
            for (int m = 0; m < 8; ++m) { A[m] = lds128(la + ro + m * 512); B[m] = lds128(lb + ro + (7 - m) * 512); }
#pragma unroll
            for (int nu = 0; nu < 8; ++nu)
#pragma unroll
                for (int mu = 0; mu + nu < 8; ++mu) acc[mu + nu] = mfma16(A[mu], B[nu], acc[mu + nu]);
        }
        BAR();
        const unsigned sc = L0 + L_KC + slot * KCH;
        if (w < 4) {
            const int h = w & 1;
            if (h) {
#pragma unroll
                for (int n = 0; n < 4; ++n) *(LAS f32x4*)(sc + 4096 + n * 1024 + lane * 16) = acc[n];
            } else {
#pragma unroll
                for (int n = 0; n < 4; ++n) *(LAS f32x4*)(sc + n * 1024 + lane * 16) = acc[4 + n];
            }
        } else if (nch == 3) {
            const int h = w - 4;
#pragma unroll
            for (int n = 0; n < 4; ++n) *(LAS f32x4*)(sc + h * 4096 + n * 1024 + lane * 16) = acc[n];
        }
        BAR();
        f32x4 y[4];
        if (w < 4) {
            const int h = w & 1;
            if (h) {
#pragma unroll
                for (int n = 0; n < 4; ++n) y[n] = acc[4 + n] + *(LAS const f32x4*)(sc + n * 1024 + lane * 16);
            } else {
#pragma unroll
                for (int n = 0; n < 4; ++n) y[n] = acc[n] + *(LAS const f32x4*)(sc + 4096 + n * 1024 + lane * 16);
            }
        } else if (nch == 3) {
            const int h = w - 4;
            f32x4 s4 = f32x4{0, 0, 0, 0};
#pragma unroll
            for (int q = 0; q < 4; ++q) s4 += *(LAS const f32x4*)(sc + q * 4096 + h * 1024 + lane * 16);
            y[0] = s4;
        }
        BAR();
        if (w >= 4 && nch == 3) {
            const int h = w - 4;
#pragma unroll
            for (int n = 0; n < 4; ++n) *(LAS f32x4*)(sc + h * 4096 + n * 1024 + lane * 16) = acc[4 + n];
        }
        BAR();
        if (w >= 4 && nch == 3) {
            const int h = w - 4;
            f32x4 s4 = f32x4{0, 0, 0, 0};
#pragma unroll
            for (int q = 0; q < 4; ++q) s4 += *(LAS const f32x4*)(sc + q * 4096 + h * 1024 + lane * 16);
            y[1] = s4;
        }
        BAR();
        const unsigned ys = L0 + L_RB + slot * RCH;
        const unsigned yo = (l15 >> 1) * 64 + g4 * 16 + (l15 & 1) * 8;
        const int chs = slot == 2 ? ch2 : 2 * b + slot;
        if (w < 4) {
            const int h = w & 1;
#pragma unroll
            for (int n = 0; n < 4; ++n) st64(ys + (4 * h + n) * 512 + yo, u32x2{pk(y[n][0], y[n][1]), pk(y[n][2], y[n][3])});
#pragma unroll
            for (int k = 0; k < 2; ++k)
                __builtin_amdgcn_raw_buffer_store_b128(lds128(ys + h * 2048 + k * 1024 + lane * 16), wsr, WS_Y + chs * 4096 + h * 2048 + k * 1024 + lane * 16, 0, 16);
        } else if (nch == 3) {
            const int h = w - 4;
            st64(ys + h * 512 + yo, u32x2{pk(y[0][0], y[0][1]), pk(y[0][2], y[0][3])});
            st64(ys + (4 + h) * 512 + yo, u32x2{pk(y[1][0], y[1][1]), pk(y[1][2], y[1][3])});
            if (lane < 32) __builtin_amdgcn_raw_buffer_store_b128(lds128(ys + h * 512 + lane * 16), wsr, WS_Y + chs * 4096 + h * 512 + lane * 16, 0, 16);
            else __builtin_amdgcn_raw_buffer_store_b128(lds128(ys + (4 + h) * 512 + (lane - 32) * 16), wsr, WS_Y + chs * 4096 + (4 + h) * 512 + (lane - 32) * 16, 0, 16);
        }
    }
    if (w == 0 && bi > 0) poll_flags((const GAS u64*)(ws + WS_KVFLAG) + gp * 16 + (bi > 1 ? bi - 2 : 0), bi > 1 ? 2 : 1, nonce, lane);
    WAITVM(0);
    BAR();
    if (tid < nch) __hip_atomic_store((GAS u64*)(ws + WS_YFLAG) + (tid == 2 ? ch2 : 2 * b + tid), nonce, RLX);
    STAMP(6);
    {
        for (int hb = 0; hb < 2; ++hb) {
            const int ib = bi - 2 + hb;
            if (ib >= 0) {
                const int gb = WS_KV + (ib * 16 + gp) * KVB;
#pragma unroll
                for (int k = 0; k < 3; ++k) {
                    const int q = tid + 512 * k;
                    if (q < 1184) st128(L0 + hb * KVB + q * 16, __builtin_amdgcn_raw_buffer_load_b128(wsr, gb + q * 16, 0, 16));
                }
            }
        }
    }
    for (int u = w; u < 81; u += 8) {
        const int q = u * 64 + lane, row = q / 9, cc = q - row * 9;
        dma16((const char*)wo + row * 1152 + bc * 144 + cc * 16, L0 + L_WO + u * 1024);
    }
    BAR();
    STAMP(7);

    {
        const int hd = w >> 2, qt = w & 3, l31 = lane & 31, hh = lane >> 5;
        const u32x4 qm = lds128(L0 + L_Q + hd * 4608 + qt * 1024 + hh * 512 + l31 * 16);
        const unsigned qrv = lds32(L0 + L_Q + hd * 4608 + 4096 + (32 * qt + l31) * 4);
        const u32x4 qr = u32x4{hh ? 0u : qrv, 0, 0, 0};
        const int vrow = l31 < 19 ? l31 : (l31 == 19 ? 18 : (l31 < 28 ? 0 : 4));
        const unsigned kbase = L0 + hd * HEADB + hh * 512 + l31 * 16, krbase = L0 + hd * HEADB + 4096 + l31 * 4;
        const unsigned vbase = L0 + hd * HEADB + 4608 + hh * 304 + vrow * 16;
        const int qq = l31 - 4 * hh;
        const int t_first = bi >= 2 ? qt : (bi == 1 ? 4 : 8), t_last = 8 + qt;
        const bool has_first = bi >= 2;
        auto qk = [&](int t, f32x16 c) {
            const unsigned bo = (t >> 2) * KVB;
            u32x4 km = lds128(kbase + bo + (t & 3) * 1024);
            unsigned kr = lds32(krbase + bo + (t & 3) * 128);
            c = mfma32(km, qm, c);
            return mfma32(u32x4{kr, 0, 0, 0}, qr, c);
        };
        auto pv = [&](int t, const f32x16& p, f32x16 o) {
            const unsigned bo = (t >> 2) * KVB + (t & 3) * 1216;
            u32x4 v0 = lds128(vbase + bo), v1 = lds128(vbase + bo + 608);
            u32x4 p0 = u32x4{pk(p[0], p[1]), pk(p[2], p[3]), pk(p[4], p[5]), pk(p[6], p[7])};
            u32x4 p1 = u32x4{pk(p[8], p[9]), pk(p[10], p[11]), pk(p[12], p[13]), pk(p[14], p[15])};
            o = mfma32(v0, p0, o);
            return mfma32(v1, p1, o);
        };
        const float NINF = -__builtin_inff();
        f32x16 z16;
#pragma unroll
        for (int r = 0; r < 16; ++r) z16[r] = 0.f;
        f32x16 sd = qk(t_last, z16);
        float m = NINF;
#pragma unroll
        for (int r = 0; r < 16; ++r) { const int c = (r & 3) + 8 * (r >> 2); sd[r] = c <= qq ? sd[r] : NINF; m = fmaxf(m, sd[r]); }
        m = fmaxf(m, __shfl_xor(m, 32));
        f32x16 o;
        float l;
        for (int attempt = 0; attempt < 2; ++attempt) {
            f32x16 nm, p;
#pragma unroll
            for (int r = 0; r < 16; ++r) { nm[r] = -m; p[r] = __builtin_amdgcn_exp2f(sd[r] - m); }
            o = pv(t_last, p, z16);
            for (int t = t_first; t < t_last; ++t) {
                f32x16 s = qk(t, nm);
                if (has_first && t == t_first) {
#pragma unroll
                    for (int r = 0; r < 16; ++r) { const int c = (r & 3) + 8 * (r >> 2); s[r] = c > qq ? s[r] : NINF; }
                }
#pragma unroll
                for (int r = 0; r < 16; ++r) p[r] = __builtin_amdgcn_exp2f(s[r]);
                o = pv(t, p, o);
            }
            l = __shfl(o[10], l31);
            if (attempt == 1 || __all(l <= 1048576.f)) break;
            for (int t = t_first; t < t_last; ++t) {
                f32x16 s = qk(t, z16);
#pragma unroll
                for (int r = 0; r < 16; ++r) { const int c = (r & 3) + 8 * (r >> 2); const bool keep = !(has_first && t == t_first) || c > qq; m = fmaxf(m, keep ? s[r] : NINF); }
            }
            m = fmaxf(m, __shfl_xor(m, 32));
        }
        const float il = 1.f / l;
        const int T16 = 8 * bi + 2 * qt + (l31 >> 4);
        u32x4 om = u32x4{pk(o[0] * il, o[1] * il), pk(o[2] * il, o[3] * il), pk(o[4] * il, o[5] * il), pk(o[6] * il, o[7] * il)};
        __builtin_amdgcn_raw_buffer_store_b128(om, wsr, WS_OM + ((T16 * 16 + gp) * 64 + (2 * hd + hh) * 16 + (l31 & 15)) * 16, 0, 16);
        if (hh == 0) __builtin_amdgcn_raw_buffer_store_b32(pk(o[8] * il, o[9] * il), wsr, WS_OR + (((T16 * 16 + gp) * 2 + hd) * 16 + (l31 & 15)) * 4, 0, 16);
    }
    WAITVM(0);
    BAR();
    if (tid == 0) __hip_atomic_store((GAS u64*)(ws + WS_OFLAG) + bi * 16 + gp, nonce, RLX);
    STAMP(8);

    if (w == 0) { poll_flags((const GAS u64*)(ws + WS_YFLAG) + 72 * bc, 64, nonce, lane); poll_flags((const GAS u64*)(ws + WS_YFLAG) + 72 * bc + 64, 8, nonce, lane); }
    BAR();
    if (tid < 288) {
        const int t0 = 64 * br + 8 * oct;
        u32x4 y0 = __builtin_amdgcn_raw_buffer_load_b128(wsr, WS_Y + chB * 4096 + t0 * 2, 0, 16);
        u32x4 y1 = __builtin_amdgcn_raw_buffer_load_b128(wsr, WS_Y + (chB + 1) * 4096 + t0 * 2, 0, 16);
        const unsigned dv = *(const unsigned*)(ssd + chB);
        const float D0 = blo(dv), D1 = bhi(dv);
#pragma unroll
        for (int t = 0; t < 8; ++t) {
            const int slot = (t & 1) * 4 + (t >> 1);
            const float ya = (slot & 1) ? bhi(y0[slot >> 1]) : blo(y0[slot >> 1]);
            const float yb = (slot & 1) ? bhi(y1[slot >> 1]) : blo(y1[slot >> 1]);
            const unsigned qv = *(const unsigned*)(bq + (size_t)(t0 + t) * 576 + chB);
            const unsigned kd = pk(blo(kvp[t]) * D0, bhi(kvp[t]) * D1);
            const unsigned zz = pk(ya + blo(kd), yb + bhi(kd));
            *(unsigned*)(out + (size_t)(t0 + t) * 1152 + 576 + chB) = pk(blo(qv) * blo(zz), bhi(qv) * bhi(zz));
        }
    }
    STAMP(9);

    if (w == 0) poll_flags((const GAS u64*)(ws + WS_OFLAG) + (br >> 1) * 16, 16, nonce, lane);
    WAITVM(0);
    BAR();
    STAMP(10);
    {
        const int tt = w & 3, kh = w >> 2, T16 = 4 * br + tt;
        u32x4 of[9];
#pragma unroll
        for (int s = 0; s < 8; ++s) of[s] = __builtin_amdgcn_raw_buffer_load_b128(wsr, WS_OM + ((T16 * 16 + 8 * kh + s) * 64 + lane) * 16, 0, 16);
#pragma unroll
        for (int d = 0; d < 4; ++d) of[8][d] = __builtin_amdgcn_raw_buffer_load_b32(wsr, WS_OR + (((T16 * 16 + 8 * kh + 2 * g4 + (d >> 1)) * 2 + (d & 1)) * 16 + l15) * 4, 0, 16);
        f32x4 acc[5];
#pragma unroll
        for (int j = 0; j < 5; ++j) acc[j] = f32x4{0, 0, 0, 0};
        const int q4 = (lane >> 2) & 3, p4 = lane & 3, hd = g4 >> 1, hh = g4 & 1;
        const unsigned mrow = L0 + L_WO + (288 * kh + 20 * hd + 4 * hh + q4) * 144;
        const unsigned rrow = L0 + L_WO + (288 * kh + 72 * g4 + 16 + q4) * 144;
#pragma unroll
        for (int s = 0; s < 9; ++s) {
#pragma unroll
            for (int j = 0; j < 5; ++j) {
                const unsigned col = j < 4 ? j * 32 + p4 * 8 : 128 + (p4 & 1) * 8;
                u32x2 a, c;
                if (s < 8) { a = ldstr(mrow + s * 36 * 144 + col); c = ldstr(mrow + (s * 36 + 8) * 144 + col); }
                else { a = ldstr(rrow + col); c = ldstr(rrow + 36 * 144 + col); }
                acc[j] = mfma16(u32x4{a[0], a[1], c[0], c[1]}, of[s], acc[j]);
            }
        }
        const unsigned sc = L0 + tt * 5120;
        if (kh == 1) {
#pragma unroll
            for (int j = 0; j < 5; ++j) *(LAS f32x4*)(sc + j * 1024 + lane * 16) = acc[j];
        }
        BAR();
        if (kh == 0) {
            unsigned short* op = out + (size_t)(16 * T16 + l15) * 1152 + 72 * bc + 4 * g4;
#pragma unroll
            for (int j = 0; j < 5; ++j) {
                f32x4 v = acc[j] + *(LAS const f32x4*)(sc + j * 1024 + lane * 16);
                if (j < 4 || g4 < 2) *(u32x2*)(op + 16 * j) = u32x2{pk(v[0], v[1]), pk(v[2], v[3])};
            }
        }
    }
    if (b == 0 && w == 7) {
        for (int k = 0; k < 4; ++k) poll_flags((const GAS u64*)(ws + WS_KFLAG) + 64 * k, 64, nonce, lane);
        if (lane == 0) __hip_atomic_store((GAS u64*)ws, nonce, RLX);
    }
    { u64 m = ~0ull; for (int k = 0; k < 4; ++k) { u64 v = __hip_atomic_load((GAS u64*)(ws + WS_TS) + lane + 64 * k, RLX); m = v < m ? v : m; }
      for (int o = 32; o; o >>= 1) { u64 v = __shfl_xor(m, o); m = v < m ? v : m; }
      { const u64 until = __builtin_amdgcn_s_memrealtime() + 10 * ((pt0 - m) < 5000ull ? (pt0 - m) : 5000ull); while (__builtin_amdgcn_s_memrealtime() < until) __builtin_amdgcn_s_sleep(4); } }
}

extern "C" void kernel_launch(void* const* d_in, const int* in_sizes, int n_in, void* d_out, int out_size, void* d_ws, size_t ws_size, hipStream_t stream) {
    fused_kernel<<<256, 512, 0, stream>>>((const unsigned short*)d_in[0], (const unsigned short*)d_in[1], (const unsigned short*)d_in[2], (const unsigned short*)d_in[3],
                                          (const unsigned short*)d_in[4], (const unsigned short*)d_in[5], (const unsigned short*)d_in[6], (const unsigned short*)d_in[7],
                                          (const unsigned short*)d_in[8], (unsigned short*)d_out, (char*)d_ws);
}
```

```cpp
#include <hip/hip_runtime.h>
#include <stdint.h>

#define GAS __attribute__((address_space(1)))
#define LAS __attribute__((address_space(3)))
#define DEVI __device__ __forceinline__
typedef unsigned long long u64;
typedef __attribute__((ext_vector_type(8))) __bf16 bf16x8;
typedef __attribute__((ext_vector_type(2))) __bf16 bf16x2;
typedef __attribute__((ext_vector_type(2))) float f32x2;
typedef __attribute__((ext_vector_type(4))) float f32x4;
typedef __attribute__((ext_vector_type(16))) float f32x16;
typedef __attribute__((ext_vector_type(4))) unsigned u32x4;
typedef __attribute__((ext_vector_type(2))) unsigned u32x2;
typedef __attribute__((ext_vector_type(4))) short s16x4;

constexpr int WS_KFLAG = 256, WS_QFLAG = 4096, WS_OFLAG = 8192, WS_YFLAG = 12288;
constexpr int WS_R0 = 32768;
constexpr int WS_QKV = WS_R0 + 2359296;
constexpr int WS_OM = WS_QKV + 7077888;
constexpr int WS_OR = WS_OM + 2097152;
constexpr int WS_Y = WS_OR + 262144;
constexpr int WS_END = WS_Y + 2359296;
constexpr int WS_TS = 32 << 20;
constexpr int L_B1K = 131072, L_B1V = 141312;
constexpr int L_B1S = 151552;
constexpr int KCS = 4160, KCH = 4 * KCS;
constexpr int RCS = 4576, RCH = 2 * RCS;
constexpr int L_KB = 32768, L_RB = 131072;
constexpr int L_KA = 66048, L_RA = 0;
constexpr int L_SCR = 114688;
constexpr int BLK = 10752, PCS = 2112;
constexpr int L_KBLK = 0, L_VBLK = 66048, L_QBLK = 66048 + 3 * BLK;
constexpr int L_WO1 = 32768, L_WO2 = 109056;
constexpr int L_ONES = 161280;
constexpr int L_Z = L_QBLK;
constexpr int L_X = 0;
constexpr int L_SYNC = 163584;
constexpr int SY_LAND = L_SYNC, SY_CONS = L_SYNC + 16, SY_PW = L_SYNC + 32, SY_PR = L_SYNC + 48, SY_PREP = L_SYNC + 64, SY_QDR = L_SYNC + 68, SY_YDR = L_SYNC + 72,
              SY_ODR = L_SYNC + 76, SY_NONCE = L_SYNC + 80, SY_KVD = L_SYNC + 96, SY_QRD = L_SYNC + 112;
constexpr int LDS_BYTES = 163840;
constexpr unsigned SPIN_CAP = 1u << 17, LSPIN_CAP = 1u << 20;

#define STAMPW(k) do { } while (0)
#define STAMPC(k) do { } while (0)
#define STAMP(k) do { } while (0)

DEVI f32x4 mfma16(u32x4 a, u32x4 b, f32x4 c) { return __builtin_amdgcn_mfma_f32_16x16x32_bf16(__builtin_bit_cast(bf16x8, a), __builtin_bit_cast(bf16x8, b), c, 0, 0, 0); }
DEVI f32x16 mfma32(u32x4 a, u32x4 b, f32x16 c) { return __builtin_amdgcn_mfma_f32_32x32x16_bf16(__builtin_bit_cast(bf16x8, a), __builtin_bit_cast(bf16x8, b), c, 0, 0, 0); }
DEVI f32x16 mfma32k8(u32x2 a, u32x2 b, f32x16 c) { return __builtin_amdgcn_mfma_f32_32x32x8bf16_1k(__builtin_bit_cast(s16x4, a), __builtin_bit_cast(s16x4, b), c, 0, 0, 0); }
DEVI unsigned pk(float lo, float hi) { f32x2 v = {lo, hi}; return __builtin_bit_cast(unsigned, __builtin_convertvector(v, bf16x2)); }
DEVI float blo(unsigned v) { return __builtin_bit_cast(float, v << 16); }
DEVI float bhi(unsigned v) { return __builtin_bit_cast(float, v & 0xffff0000u); }
DEVI u32x4 lds128(unsigned a) { return *(LAS const u32x4*)(uintptr_t)a; }
DEVI u32x2 lds64(unsigned a) { return *(LAS const u32x2*)(uintptr_t)a; }
DEVI unsigned lds32(unsigned a) { return *(LAS const unsigned*)(uintptr_t)a; }
DEVI void st128(unsigned a, u32x4 v) { *(LAS u32x4*)(uintptr_t)a = v; }
DEVI void st64(unsigned a, u32x2 v) { *(LAS u32x2*)(uintptr_t)a = v; }
DEVI void st32(unsigned a, unsigned v) { *(LAS unsigned*)(uintptr_t)a = v; }
DEVI f32x4 ldsf4(unsigned a) { return *(LAS const f32x4*)(uintptr_t)a; }
DEVI void stf4(unsigned a, f32x4 v) { *(LAS f32x4*)(uintptr_t)a = v; }
DEVI u32x2 ldstr(unsigned a) { return __builtin_bit_cast(u32x2, __builtin_amdgcn_ds_read_tr16_b64_v4i16((LAS s16x4*)(uintptr_t)a)); }
DEVI u32x4 vlds128(unsigned a) { return *(volatile LAS const u32x4*)(uintptr_t)a; }
DEVI unsigned vlds32(unsigned a) { return *(volatile LAS const unsigned*)(uintptr_t)a; }
DEVI void vst32(unsigned a, unsigned v) { *(volatile LAS unsigned*)(uintptr_t)a = v; }
DEVI void dma16(const void* g, unsigned lds) {
    unsigned sv;
    asm volatile("s_mov_b32 %0, m0\n\ts_mov_b32 m0, %2\n\ts_nop 0\n\tglobal_load_lds_dwordx4 %1, off\n\ts_mov_b32 m0, %0" : "=&s"(sv) : "v"(g), "s"(lds) : "memory");
}
DEVI void dmaS(unsigned voff, const void* sbase, unsigned lds) {
    unsigned sv;
    asm volatile("s_mov_b32 %0, m0\n\ts_mov_b32 m0, %3\n\ts_nop 0\n\tglobal_load_lds_dwordx4 %1, %2\n\ts_mov_b32 m0, %0" : "=&s"(sv) : "v"(voff), "s"(sbase), "s"(lds) : "memory");
}
#define WAITVM(n) asm volatile("s_waitcnt vmcnt(%0)" ::"i"(n) : "memory")
#define BAR() do { asm volatile("s_waitcnt lgkmcnt(0)\n\ts_barrier" ::: "memory"); } while (0)
#define CFENCE() asm volatile("" ::: "memory")
#define SB0() __builtin_amdgcn_sched_barrier(0)
#define RLX __ATOMIC_RELAXED, __HIP_MEMORY_SCOPE_AGENT
DEVI unsigned rfl(unsigned v) { return __builtin_amdgcn_readfirstlane(v); }
DEVI unsigned umin4(u32x4 v) { const unsigned a = v[0] < v[1] ? v[0] : v[1], c = v[2] < v[3] ? v[2] : v[3]; return a < c ? a : c; }

DEVI void spin4(unsigned a, unsigned need) {
#pragma nounroll
    for (unsigned s = 0; s < LSPIN_CAP; ++s) {
        if (rfl(umin4(vlds128(a))) >= need) break;
        __builtin_amdgcn_s_sleep(1);
    }
    CFENCE();
}
DEVI void spin1(unsigned a, unsigned need) {
#pragma nounroll
    for (unsigned s = 0; s < LSPIN_CAP; ++s) {
        if (rfl(vlds32(a)) >= need) break;
        __builtin_amdgcn_s_sleep(1);
    }
    CFENCE();
}
DEVI void signal(unsigned a, unsigned v, int lane) { CFENCE(); if (lane == 0) vst32(a, v); CFENCE(); }
DEVI bool arrive(unsigned a, unsigned n, int lane) {
    CFENCE();
    unsigned old = 0;
    if (lane == 0) old = __hip_atomic_fetch_add((LAS unsigned*)(uintptr_t)a, 1u, __ATOMIC_RELAXED, __HIP_MEMORY_SCOPE_WORKGROUP);
    CFENCE();
    return lane == 0 && old == n - 1;
}
DEVI void poll_idx(const GAS u64* f, int idx, u64 nonce) {
#pragma nounroll
    for (unsigned spins = 0; spins < SPIN_CAP; ++spins) {
        u64 v = nonce;
        if (idx >= 0) v = __hip_atomic_load(f + idx, RLX);
        if (__all(v == nonce)) break;
        __builtin_amdgcn_s_sleep(2);
    }
    __builtin_amdgcn_fence(__ATOMIC_ACQUIRE, "wavefront");
    CFENCE();
}

__device__ constexpr int CMU[36] = {0,0,0,0,1,1,1,1,2,2,2,0,3,3,3,2,4,4,1,0,5,5,4,3,6,2,1,0,7,6,5,4,3,2,1,0};
__device__ constexpr int CNU[36] = {7,6,5,4,6,5,4,3,5,4,3,3,4,3,2,2,3,2,2,2,2,1,1,1,1,1,1,1,0,0,0,0,0,0,0,0};

__global__ __launch_bounds__(512, 2) void fused_kernel(const unsigned short* __restrict__ x, const unsigned short* __restrict__ wqkv, const unsigned short* __restrict__ wo,
                                                       const unsigned short* __restrict__ bq, const unsigned short* __restrict__ bk, const unsigned short* __restrict__ bv,
                                                       const unsigned short* __restrict__ shk, const unsigned short* __restrict__ ssk, const unsigned short* __restrict__ ssd,
                                                       unsigned short* __restrict__ out, char* __restrict__ ws) {
    __shared__ __attribute__((aligned(256))) char smem[LDS_BYTES];
    const unsigned L0 = (unsigned)(uintptr_t)(LAS char*)smem;
    const int tid = threadIdx.x, lane = tid & 63, w = __builtin_amdgcn_readfirstlane(tid >> 6);
    const int b = blockIdx.x, xcd = b & 7, jj = b >> 3;
    const bool busy = jj < 28;
    const int ri = 4 * (xcd >> 1) + (jj & 3), cj = 7 * (xcd & 1) + (jj >> 2);
    const int br = 4 * xcd + (jj & 3), bc = jj >> 2;
    const int ai = (31 - jj) >> 1, gp = 2 * xcd + (jj & 1);
    const int pairB = busy ? xcd * 28 + jj : 225 + 2 * (xcd * 4 + jj - 28);
    const int l15 = lane & 15, g4 = lane >> 4, l31 = lane & 31, hh = lane >> 5;
    const __amdgpu_buffer_rsrc_t wsr = __builtin_amdgcn_make_buffer_rsrc(ws, 0, WS_END, 0x00020000);
    STAMP(0);
    auto b1_stage = [&]() {
        for (int u = w; u < 20; u += 8) {
            const bool isk = u < 10;
            const int q = (isk ? u : u - 10) * 64 + lane;
            const int g = q / 74; int r = q - g * 74; r = r < 72 ? r : 71;
            int row = 8 * g + r / 9; const int cc = r - (r / 9) * 9;
            if (isk) {
                row = row < 68 ? row : 67;
                int tok = 64 * br - 4 + row; tok = tok < 0 ? 0 : tok;
                dma16((const char*)bk + tok * 1152 + bc * 144 + cc * 16, L0 + L_B1K + u * 1024);
            } else {
                row = row < 64 ? row : 63;
                dma16((const char*)bv + (64 * br + row) * 1152 + bc * 144 + cc * 16, L0 + L_B1V + (u - 10) * 1024);
            }
        }
        if (w == 7) dma16(lane < 36 ? (const char*)shk + bc * 576 + lane * 16 : (const char*)ssd + bc * 144 + (lane < 45 ? lane - 36 : 0) * 16, L0 + L_B1S);
    };
    unsigned kvp[8];
#pragma unroll
    for (int t = 0; t < 8; ++t) kvp[t] = 0;
    unsigned dv = 0;
    const int bl = w < 4 ? tid : 256 + l31;
    const int cp = bl >> 3, oct = bl & 7;
    auto b1_compute = [&]() {
        const u32x4 shv = lds128(L0 + L_B1S + cp * 16);
        dv = lds32(L0 + L_B1S + 576 + cp * 4);
        unsigned vv[8];
#pragma unroll
        for (int t = 0; t < 8; ++t) vv[t] = lds32(L0 + L_B1V + oct * 1184 + t * 144 + cp * 4);
        f32x2 k[11];
#pragma unroll
        for (int q = 0; q < 11; ++q) {
            const unsigned v = lds32(L0 + L_B1K + oct * 1184 + (1 + q) * 144 + ((1 + q) >> 3) * 32 + cp * 4);
            k[q] = f32x2{blo(v), bhi(v)};
        }
        if (br == 0 && oct == 0) { k[0] = f32x2{0.f, 0.f}; k[1] = k[0]; k[2] = k[0]; }
        const f32x2 s0 = {blo(shv[0]), blo(shv[2])}, s1 = {bhi(shv[0]), bhi(shv[2])}, s2 = {blo(shv[1]), blo(shv[3])}, s3 = {bhi(shv[1]), bhi(shv[3])};
#pragma unroll
        for (int t = 0; t < 8; ++t) {
            const unsigned v = vv[t];
            f32x2 a = k[t] * s3;
            a = __builtin_elementwise_fma(k[t + 1], s2, a);
            a = __builtin_elementwise_fma(k[t + 2], s1, a);
            a = __builtin_elementwise_fma(k[t + 3], s0, a);
            const unsigned ks = pk(a[0], a[1]);
            kvp[t] = pk(blo(ks) * blo(v), bhi(ks) * bhi(v));
        }
        u32x4 r0, r1;
#pragma unroll
        for (int d = 0; d < 4; ++d) {
            r0[d] = (kvp[7 - 2 * d] & 0xffffu) | (kvp[6 - 2 * d] << 16);
            r1[d] = (kvp[7 - 2 * d] >> 16) | (kvp[6 - 2 * d] & 0xffff0000u);
        }
        const int off = WS_R0 + (72 * bc + 2 * cp) * 4096 + (2040 - 64 * br - 8 * oct) * 2;
        __builtin_amdgcn_raw_buffer_store_b128(r0, wsr, off, 0, 16);
        __builtin_amdgcn_raw_buffer_store_b128(r1, wsr, off + 4096, 0, 16);
    };
    auto get_nonce = [&]() {
        const u32x2 v = *(volatile LAS const u32x2*)(uintptr_t)(L0 + SY_NONCE);
        return ((u64)rfl(v[1]) << 32) | rfl(v[0]);
    };
    auto gather = [&](int sw, u64 nonce) {
        spin4(L0 + SY_CONS, 9);
        {
            int idx = -1;
            if (lane < 14) {
                const int j = lane >> 1, sec = j < 3 ? 1 : (j < 6 ? 2 : 0), rr = j < 6 ? ai - 2 + (j < 3 ? j : j - 3) : ai;
                const int c0 = 576 * sec + 36 * gp - 4 * (gp & 1) + (lane & 1) * 39;
                if (rr >= 0) idx = rr * 16 + (c0 >> 7);
            }
            poll_idx((const GAS u64*)(ws + WS_QFLAG), idx, nonce);
        }
        STAMPW(24 + sw);
        u32x4 g[18];
        const int p0 = (36 * gp - 4 * (gp & 1)) >> 3;
#pragma unroll
        for (int k = 0; k < 18; ++k) {
            const int u = sw + 4 * k, j = u / 10, e = u - 10 * j;
            const int sec = j < 3 ? 1 : (j < 6 ? 2 : 0), rr = j < 6 ? ai - 2 + (j < 3 ? j : j - 3) : ai;
            if (u < 70 && rr >= 0) g[k] = __builtin_amdgcn_raw_buffer_load_b128(wsr, lane * 16, WS_QKV + ((rr * 216 + 72 * sec + p0) * 128) * 16 + e * 1024, 16);
        }
#pragma unroll
        for (int k = 0; k < 18; ++k) {
            const int u = sw + 4 * k, j = u / 10, e = u - 10 * j;
            const int rr = j < 6 ? ai - 2 + (j < 3 ? j : j - 3) : ai;
            const unsigned dst = L0 + (j < 3 ? L_KBLK + j * BLK : L_VBLK + (j - 3) * BLK) + (e >> 1) * PCS + (e & 1) * 1024 + lane * 16;
            if (u < 70 && rr >= 0) st128(dst, g[k]);
        }
    };

    if (w < 4) {
        b1_stage();
        if (w == 0 && (lane < 20 || (lane >= 24 && lane < 40))) vst32(L0 + L_SYNC + lane * 4, lane == 3 ? 0xffffffffu : 0u);
        if (w == 1) {
#pragma unroll
            for (int i = 0; i < 3; ++i) { const int q = i * 64 + lane; if (q < 140) st128(L0 + L_ONES + q * 16, u32x4{0x3f803f80u, 0x3f803f80u, 0x3f803f80u, 0x3f803f80u}); }
        }
        WAITVM(0);
        BAR();
        STAMP(1);
        b1_compute();
        STAMP(2);
        if (busy) {
            const int wt = w & 1, wc = w >> 1;
            const bool live = !(cj == 13 && wc == 1);
            if (live) {
                unsigned xo[4], wof[2];
#pragma unroll
                for (int j = 0; j < 4; ++j) xo[j] = L0 + (64 * wt + l31) * 128 + (((2 * j + hh) ^ ((l31 >> 1) & 7)) * 16);
                {
                    const int q = (lane >> 2) & 3, p = lane & 3;
#pragma unroll
                    for (int ct = 0; ct < 2; ++ct) wof[ct] = L0 + 16384 + (8 * hh + q) * 256 + (((8 * wc + 4 * ct + 2 * (g4 & 1) + (p >> 1)) ^ (q << 2)) * 16) + (p & 1) * 8;
                }
                f32x16 acc[2][2];
#pragma unroll
                for (int r = 0; r < 16; ++r) { acc[0][0][r] = 0.f; acc[0][1][r] = 0.f; acc[1][0][r] = 0.f; acc[1][1][r] = 0.f; }
                u32x4 xf[2][2];
                u32x2 wa[2][2], wc2[2][2];
                auto ldx = [&](int s, int j, int pb, int tt) { xf[pb][tt] = lds128(xo[j] + (s & 3) * 32768 + tt * 4096); };
                auto ldwa = [&](int s, int j, int pb, int ct) { wa[pb][ct] = ldstr(wof[ct] + (s & 3) * 32768 + j * 4096); };
                auto ldwc = [&](int s, int j, int pb, int ct) { wc2[pb][ct] = ldstr(wof[ct] + (s & 3) * 32768 + j * 4096 + 1024); };
                auto W = [&](int pb, int ct) { return u32x4{wa[pb][ct][0], wa[pb][ct][1], wc2[pb][ct][0], wc2[pb][ct][1]}; };
                spin4(L0 + SY_LAND, 1);
                STAMP(3); STAMPC(40);
                ldx(0, 0, 0, 0); ldwa(0, 0, 0, 0); ldwc(0, 0, 0, 0); ldx(0, 0, 0, 1); ldwa(0, 0, 0, 1); ldwc(0, 0, 0, 1);
#pragma unroll
                for (int s = 0; s < 9; ++s) {
                    u32x4 landv;
#pragma unroll
                    for (int j = 0; j < 4; ++j) {
                        const int pb = j & 1, nb = pb ^ 1;
                        const bool more = j < 3 || s < 8;
                        const int ns = j < 3 ? s : s + 1, nj = (j + 1) & 3;
                        SB0();
                        acc[0][0] = mfma32(W(pb, 0), xf[pb][0], acc[0][0]);
                        SB0();
                        if (j == 3 && s < 8) { if (rfl(umin4(landv)) < (unsigned)(s + 2)) spin4(L0 + SY_LAND, s + 2); CFENCE(); }
                        if (more) { ldx(ns, nj, nb, 0); ldwa(ns, nj, nb, 0); ldwc(ns, nj, nb, 0); }
                        SB0();
                        acc[0][1] = mfma32(W(pb, 0), xf[pb][1], acc[0][1]);
                        SB0();
                        if (more) { ldx(ns, nj, nb, 1); ldwa(ns, nj, nb, 1); }
                        SB0();
                        acc[1][0] = mfma32(W(pb, 1), xf[pb][0], acc[1][0]);
                        SB0();
                        if (more) { ldwc(ns, nj, nb, 1); }
                        if (j == 2) {
                            signal(L0 + SY_CONS + w * 4, s + 1, lane);
                        }
                        if (j == 1 && s < 8) landv = vlds128(L0 + SY_LAND);
                        if (j == 0 && s == 2) { WAITVM(0); signal(L0 + SY_KVD + w * 4, 1, lane); }
                        SB0();
                        acc[1][1] = mfma32(W(pb, 1), xf[pb][1], acc[1][1]);
                    }
                }
                STAMP(4); STAMPC(41);
#pragma unroll
                for (int ct = 0; ct < 2; ++ct) {
                    const int n0 = 128 * cj + 64 * wc + 32 * ct;
                    const float sc = n0 < 576 ? 0.34004634f : 1.0f;
#pragma unroll
                    for (int tt = 0; tt < 2; ++tt) {
#pragma unroll
                        for (int pi = 0; pi < 2; ++pi) {
                            const f32x16& a = acc[ct][tt];
                            const unsigned a0 = pk(a[8 * pi] * sc, a[8 * pi + 1] * sc), a1 = pk(a[8 * pi + 2] * sc, a[8 * pi + 3] * sc);
                            const unsigned b0 = pk(a[8 * pi + 4] * sc, a[8 * pi + 5] * sc), b1 = pk(a[8 * pi + 6] * sc, a[8 * pi + 7] * sc);
                            auto s0 = __builtin_amdgcn_permlane32_swap(a0, b0, false, false);
                            auto s1 = __builtin_amdgcn_permlane32_swap(a1, b1, false, false);
                            const int off = WS_QKV + ((ri * 216 + (n0 >> 3) + 2 * pi + hh) * 128 + 64 * wt + 32 * tt + l31) * 16;
                            __builtin_amdgcn_raw_buffer_store_b128(u32x4{s0[0], s1[0], s0[1], s1[1]}, wsr, off, 0, 16);
                        }
                    }
                }
            } else {
                signal(L0 + SY_CONS + w * 4, 9, lane);
                WAITVM(0); signal(L0 + SY_KVD + w * 4, 1, lane);
            }
            STAMP(5);
        } else {
            WAITVM(0); signal(L0 + SY_KVD + w * 4, 1, lane);
        }
        const u64 nonce = get_nonce();
        bool qdone = !busy;
        auto qpublish = [&]() {
            WAITVM(0);
            if (arrive(L0 + SY_QDR, 4, lane)) __hip_atomic_store((GAS u64*)(ws + WS_QFLAG) + ri * 16 + cj, nonce, RLX);
            STAMP(8);
        };
        for (int pass = busy ? 1 : 0; pass < 2; ++pass) {
            if (!qdone && rfl(vlds32(L0 + SY_PREP)) < (unsigned)(pass + 1)) { qpublish(); qdone = true; }
            spin1(L0 + SY_PREP, pass + 1);
            STAMP(6 + 3 * pass);
            const int slot = w >> 1, h = w & 1;
            const unsigned kb = L0 + (pass ? L_KB : L_KA) + slot * KCH, rb = L0 + (pass ? L_RB : L_RA) + slot * RCH;
            const int ch = 2 * (pairB - 1 + pass) + slot;
            const unsigned la = kb + ((l15 + 1) & 3) * KCS + (g4 + ((l15 + 1) >> 2)) * 16 + h * 256;
            const unsigned lb = rb + ((l15 & 1) ? 0 : RCS) + (28 - 4 * (l15 >> 1) + g4) * 16 + h * 256;
            f32x4 acc[8];
#pragma unroll
            for (int n = 0; n < 8; ++n) acc[n] = f32x4{0, 0, 0, 0};
            u32x4 A[2][8], B[2][8];
#pragma unroll
            for (int m = 0; m < 8; ++m) { A[0][m] = lds128(la + m * 512); B[0][m] = lds128(lb + (7 - m) * 512); }
#pragma unroll
            for (int rr = 0; rr < 4; ++rr) {
                const int cur = rr & 1, nx = cur ^ 1;
#pragma unroll
                for (int i = 0; i < 36; ++i) {
                    SB0();
                    acc[CMU[i] + CNU[i]] = mfma16(A[cur][CMU[i]], B[cur][CNU[i]], acc[CMU[i] + CNU[i]]);
                    SB0();
                    if (rr < 3 && (i & 1) == 0 && i < 32) {
                        const int m = i >> 2;
                        if (i & 2) B[nx][7 - m] = lds128(lb + (rr + 1) * 64 + m * 512);
                        else A[nx][m] = lds128(la + (rr + 1) * 64 + m * 512);
                    }
                }
                if (rr == 1 && !qdone) { qpublish(); qdone = true; }
            }
            if (pass == 0) signal(L0 + SY_CONS + w * 4, 9, lane);
            const unsigned mine = L0 + L_SCR + w * 4096 + lane * 16, theirs = L0 + L_SCR + (w ^ 1) * 4096 + lane * 16;
            if (pass == 1) spin1(L0 + SY_PR + (w ^ 1) * 4, busy ? 0 : 1);
            f32x4 y[4];
            if (h) {
#pragma unroll
                for (int n = 0; n < 4; ++n) stf4(mine + n * 1024, acc[n]);
            } else {
#pragma unroll
                for (int n = 0; n < 4; ++n) stf4(mine + n * 1024, acc[4 + n]);
            }
            signal(L0 + SY_PW + w * 4, pass + 1, lane);
            spin1(L0 + SY_PW + (w ^ 1) * 4, pass + 1);
            if (h) {
#pragma unroll
                for (int n = 0; n < 4; ++n) y[n] = acc[4 + n] + ldsf4(theirs + n * 1024);
            } else {
#pragma unroll
                for (int n = 0; n < 4; ++n) y[n] = acc[n] + ldsf4(theirs + n * 1024);
            }
            signal(L0 + SY_PR + w * 4, pass + 1, lane);
            const bool odd = lane & 1;
#pragma unroll
            for (int i = 0; i < 4; i += 2) {
                const unsigned a0 = pk(y[i][0], y[i][1]), a1 = pk(y[i][2], y[i][3]), c0 = pk(y[i + 1][0], y[i + 1][1]), c1 = pk(y[i + 1][2], y[i + 1][3]);
                const unsigned r0 = __builtin_amdgcn_mov_dpp(odd ? a0 : c0, 0xB1, 0xF, 0xF, true), r1 = __builtin_amdgcn_mov_dpp(odd ? a1 : c1, 0xB1, 0xF, 0xF, true);
                const u32x4 v = odd ? u32x4{r0, r1, c0, c1} : u32x4{a0, a1, r0, r1};
                __builtin_amdgcn_raw_buffer_store_b128(v, wsr, WS_Y + ch * 4096 + (4 * h + i + (odd ? 1 : 0)) * 512 + ((l15 >> 1) * 4 + g4) * 16, 0, 16);
            }
            STAMP(7 + 3 * pass);
        }
    } else if (w < 7) {
        const int d = w - 4;
        b1_stage();
        if (busy) {
            const unsigned xv0 = (lane >> 3) * 1152 + (((lane & 7) ^ (lane >> 4)) * 16), xv1 = (lane >> 3) * 1152 + (((lane & 7) ^ (4 + (lane >> 4))) * 16);
            int wch = (lane & 15) ^ ((lane >> 4) << 2);
            if (cj == 13) wch &= 7;
            const unsigned wv = (lane >> 4) * 3456 + wch * 16;
            const char* xb = (const char*)x + (size_t)(128 * ri) * 1152;
            const char* wb = (const char*)wqkv + cj * 256;
            auto issue = [&](int s) {
                const unsigned sl = L0 + (s & 3) * 32768;
#pragma unroll
                for (int i = 0; i < 11; ++i) {
                    int u = d + 3 * i; u = u < 32 ? u : d;
                    if (u < 16) dmaS((u & 1) ? xv1 : xv0, xb + s * 128 + u * 9216, sl + u * 1024);
                    else dmaS(wv, wb + (size_t)(s * 64 + (u - 16) * 4) * 3456, sl + u * 1024);
                }
            };
            const unsigned land = L0 + SY_LAND + d * 4;
            issue(0); issue(1);
            WAITVM(22);
            BAR();
            issue(2); issue(3);
            WAITVM(33); signal(land, 1, lane);
            WAITVM(22); signal(land, 2, lane);
            WAITVM(11); signal(land, 3, lane); spin4(L0 + SY_CONS, 1); issue(4);
            WAITVM(11); signal(land, 4, lane); spin4(L0 + SY_CONS, 2); issue(5);
            WAITVM(11); signal(land, 5, lane); spin4(L0 + SY_CONS, 3); issue(6);
            WAITVM(11); signal(land, 6, lane); spin4(L0 + SY_CONS, 4); issue(7);
            WAITVM(11); signal(land, 7, lane); spin4(L0 + SY_CONS, 5); issue(8);
            WAITVM(11); signal(land, 8, lane);
            WAITVM(0); signal(land, 9, lane);
        } else {
            WAITVM(0);
            BAR();
        }
        gather(d, get_nonce());
    } else {
        const u64 ep = __hip_atomic_load((const GAS u64*)ws, RLX);
        b1_stage();
        WAITVM(0);
        const u64 nonce = (((u64)rfl((unsigned)(ep >> 32)) << 32) | rfl((unsigned)ep)) + 1;
        if (lane == 0) *(volatile LAS u32x2*)(uintptr_t)(L0 + SY_NONCE) = u32x2{(unsigned)nonce, (unsigned)(nonce >> 32)};
        BAR();
        if (lane < 32) b1_compute();
        u32x4 kd0[2][4], kd1[2][4];
        auto kload = [&](int pair) {
#pragma unroll
            for (int c = 0; c < 2; ++c)
#pragma unroll
                for (int r = 0; r < 4; ++r) {
                    const int cq = r * 64 + lane;
                    const char* p = (const char*)ssk + (2 * pair + c) * 4096 + cq * 16;
                    kd0[c][r] = *(const u32x4*)p; kd1[c][r] = *(const u32x4*)(p + (cq < 255 ? 16 : 0));
                }
        };
        auto kcopy = [&](unsigned kb, int c) {
#pragma unroll
            for (int r = 0; r < 4; ++r) {
                const unsigned a = kb + (4 + r * 64 + lane) * 16;
                const u32x4 d0 = kd0[c][r], d1 = kd1[c][r];
                st128(a, d0);
                st128(a + KCS, u32x4{d0[1], d0[2], d0[3], d1[0]});
                st128(a + 2 * KCS, u32x4{d0[2], d0[3], d1[0], d1[1]});
                st128(a + 3 * KCS, u32x4{d0[3], d1[0], d1[1], d1[2]});
            }
            const u32x4 d0 = kd0[c][0];
            if (lane == 0) {
                st128(kb + 3 * 16, u32x4{0, 0, 0, 0});
                st128(kb + KCS + 3 * 16, u32x4{0, 0, 0, d0[0]});
                st128(kb + 2 * KCS + 3 * 16, u32x4{0, 0, d0[0], d0[1]});
                st128(kb + 3 * KCS + 3 * 16, u32x4{0, d0[0], d0[1], d0[2]});
            } else if (lane < 13) {
                const int e = lane - 1;
                st128(kb + (e / 3) * KCS + (e % 3) * 16, u32x4{0, 0, 0, 0});
            }
        };
        u32x4 rd[2][4]; unsigned rn[2][4];
        auto rload = [&](int pair) {
#pragma unroll
            for (int c = 0; c < 2; ++c)
#pragma unroll
                for (int r = 0; r < 4; ++r) {
                    const int cq = r * 64 + lane, off = WS_R0 + (2 * pair + c) * 4096 + cq * 16;
                    rd[c][r] = __builtin_amdgcn_raw_buffer_load_b128(wsr, off, 0, 16);
                    rn[c][r] = __builtin_amdgcn_raw_buffer_load_b32(wsr, off + (cq < 255 ? 16 : 0), 0, 16);
                }
        };
        auto rcopy = [&](unsigned rb0) {
#pragma unroll
            for (int c = 0; c < 2; ++c) {
                const unsigned rb = rb0 + c * RCH;
#pragma unroll
                for (int r = 0; r < 4; ++r) {
                    const int cq = r * 64 + lane;
                    const u32x4 dd = rd[c][r]; const unsigned nx = cq < 255 ? rn[c][r] : 0u;
                    st128(rb + cq * 16, dd);
                    st128(rb + RCS + cq * 16, u32x4{__builtin_amdgcn_alignbit(dd[1], dd[0], 16), __builtin_amdgcn_alignbit(dd[2], dd[1], 16), __builtin_amdgcn_alignbit(dd[3], dd[2], 16), __builtin_amdgcn_alignbit(nx, dd[3], 16)});
                }
                if (lane < 60) st128(rb + (lane >= 30 ? RCS : 0) + (256 + (lane >= 30 ? lane - 30 : lane)) * 16, u32x4{0, 0, 0, 0});
            }
        };
        WAITVM(0);
        spin4(L0 + SY_KVD, 1);
        if (lane == 0) __hip_atomic_store((GAS u64*)(ws + WS_KFLAG) + bc * 32 + br, nonce, RLX);
        STAMPW(11);
        if (!busy) { kload(pairB - 1); kcopy(L0 + L_KA, 0); kcopy(L0 + L_KA + KCH, 1); }
        kload(pairB);
        if (!busy) { kcopy(L0 + L_KB, 0); kcopy(L0 + L_KB + KCH, 1); }
        {
            const int cB = (2 * pairB) / 72, cA = (2 * pairB - 2) / 72;
            int idx = lane < 32 ? cB * 32 + lane : -1;
            if (!busy && lane >= 32) idx = cA * 32 + lane - 32;
            poll_idx((const GAS u64*)(ws + WS_KFLAG), idx, nonce);
        }
        STAMPW(12);
        if (!busy) { rload(pairB - 1); rcopy(L0 + L_RA); signal(L0 + SY_PREP, 1, lane); }
        rload(pairB);
        rcopy(L0 + L_RB);
        if (busy) {
            spin4(L0 + SY_CONS, 6); kcopy(L0 + L_KB, 0);
            spin4(L0 + SY_CONS, 7); kcopy(L0 + L_KB + KCH, 1);
        }
        signal(L0 + SY_PREP, 2, lane);
        STAMPW(13);
        gather(3, nonce);
    }
    STAMPW(16 + w);
    BAR();
    STAMP(14);
    const u64 nonce = get_nonce();
    auto wo_dma = [&](int k) {
        const int u = w + 8 * k;
        if (u < 82) {
            const int q = (u < 31 ? u : u - 31) * 64 + lane;
            int row = q / 9; const int cc = q - row * 9;
            row += u < 31 ? 0 : 216; row = row < 576 ? row : 575;
            dma16((const char*)wo + row * 1152 + bc * 144 + cc * 16, L0 + (u < 31 ? L_WO1 + u * 1024 : L_WO2 + (u - 31) * 1024));
        }
    };
    auto ypublish = [&]() {
        if (w < 4) {
            WAITVM(0);
            if (arrive(L0 + SY_YDR, 4, lane)) {
                __hip_atomic_store((GAS u64*)(ws + WS_YFLAG) + pairB, nonce, RLX);
                if (!busy) __hip_atomic_store((GAS u64*)(ws + WS_YFLAG) + pairB - 1, nonce, RLX);
            }
        }
    };
    const int id0 = tid, tk0 = id0 / 9, gr0 = id0 - 9 * tk0, id1 = 512 + lane, tk1 = id1 / 9, gr1 = id1 - 9 * tk1;
    u32x4 qg0, qg1 = u32x4{0, 0, 0, 0};
    auto q_prefetch = [&]() {
        qg0 = *(const u32x4*)((const char*)bq + (64 * br + tk0) * 1152 + bc * 144 + gr0 * 16);
        if (w == 5) qg1 = *(const u32x4*)((const char*)bq + (64 * br + tk1) * 1152 + bc * 144 + gr1 * 16);
    };
    {
        const int hd = w >> 2, qt = w & 3, cs = 4 * (gp & 1);
        u32x4 qm = lds128(L0 + L_QBLK + (2 * hd + hh) * PCS + (32 * qt + l31) * 16);
        u32x2 qr = lds64(L0 + L_QBLK + (2 * hd + 2) * PCS + (32 * qt + l31) * 16 + hh * 8);
        signal(L0 + SY_QRD + w * 4, 1, lane);
        {
            const int lo = cs + 18 * hd;
#pragma unroll
            for (int d = 0; d < 4; ++d) { const int c = 8 * (2 * hd + hh) + 2 * d; qm[d] = (c >= lo && c < lo + 18) ? qm[d] : 0u; }
#pragma unroll
            for (int d = 0; d < 2; ++d) { const int c = 8 * (2 * hd + 2) + 4 * hh + 2 * d; qr[d] = (c >= lo && c < lo + 18) ? qr[d] : 0u; }
        }
        const unsigned kA = L0 + L_KBLK + (2 * hd + hh) * PCS + l31 * 16, kB = L0 + L_KBLK + (2 * hd + 2) * PCS + l31 * 16 + hh * 8;
        unsigned vA, vsel;
        {
            const int q = (lane >> 2) & 3, p = lane & 3;
            const bool ones = (g4 & 1) && p > 0;
            const int c = (g4 & 1) ? cs + 16 : cs + (hd ? 20 : 0) + 4 * p;
            vA = ones ? L0 + L_ONES + 64 * ((hd ? 1 : 3) + hh) : L0 + L_VBLK + (c >> 3) * PCS + (4 * hh + q) * 16 + ((c >> 2) & 1) * 8;
            vsel = ones ? 0u : 0xffffffffu;
        }
        const int qq = l31 - 4 * hh;
        const int t_first = ai >= 2 ? qt : (ai == 1 ? 4 : 8), t_last = 8 + qt;
        const bool has_first = ai >= 2;
        auto toff = [&](int t) { return (unsigned)((t >> 2) * BLK + (t & 3) * 512); };
        auto qk = [&](int t, const f32x16& c) {
            const unsigned o = toff(t);
            const u32x4 km = lds128(kA + o);
            const u32x2 kr = lds64(kB + o);
            return mfma32k8(kr, qr, mfma32(km, qm, c));
        };
        auto pv = [&](int t, const f32x16& p, const f32x16& o) {
            const unsigned va = vA + (toff(t) & vsel);
            const u32x2 t0 = ldstr(va), t1 = ldstr(va + 128), t2 = ldstr(va + 256), t3 = ldstr(va + 384);
            const u32x4 p0 = u32x4{pk(p[0], p[1]), pk(p[2], p[3]), pk(p[4], p[5]), pk(p[6], p[7])};
            const u32x4 p1 = u32x4{pk(p[8], p[9]), pk(p[10], p[11]), pk(p[12], p[13]), pk(p[14], p[15])};
            return mfma32(u32x4{t2[0], t2[1], t3[0], t3[1]}, p1, mfma32(u32x4{t0[0], t0[1], t1[0], t1[1]}, p0, o));
        };
        const float NINF = -__builtin_inff();
        f32x16 z16;
#pragma unroll
        for (int r = 0; r < 16; ++r) z16[r] = 0.f;
        f32x16 sd = qk(t_last, z16);
        float m = NINF;
#pragma unroll
        for (int r = 0; r < 16; ++r) { const int c = (r & 3) + 8 * (r >> 2); sd[r] = c <= qq ? sd[r] : NINF; m = fmaxf(m, sd[r]); }
        m = fmaxf(m, __shfl_xor(m, 32));
        f32x16 o;
        float l;
        bool redo;
        if (ai >= 2) {
            f32x16 nm, p, s[2];
#pragma unroll
            for (int r = 0; r < 16; ++r) { nm[r] = -m; }
            s[0] = qk(qt, nm);
#pragma unroll
            for (int r = 0; r < 16; ++r) p[r] = __builtin_amdgcn_exp2f(sd[r] - m);
            o = pv(t_last, p, z16);
#pragma unroll
            for (int i = 0; i < 8; ++i) {
                if (i < 7) s[(i + 1) & 1] = qk(qt + i + 1, nm);
                f32x16 sc = s[i & 1];
                if (i == 0) {
#pragma unroll
                    for (int r = 0; r < 16; ++r) { const int c = (r & 3) + 8 * (r >> 2); sc[r] = c > qq ? sc[r] : NINF; }
                }
#pragma unroll
                for (int r = 0; r < 16; ++r) p[r] = __builtin_amdgcn_exp2f(sc[r]);
                o = pv(qt + i, p, o);
                if (i == 2) { ypublish(); q_prefetch(); }
                if (i >= 3) { wo_dma(2 * (i - 3)); wo_dma(2 * (i - 3) + 1); }
            }
            wo_dma(10);
            l = o[12];
            redo = !__all(l <= 1048576.f);
        } else {
            ypublish(); q_prefetch();
            for (int k = 0; k < 11; ++k) wo_dma(k);
            redo = true;
        }
        if (redo) {
            for (int attempt = ai >= 2 ? 1 : 0; attempt < 2; ++attempt) {
                if (attempt == 1) {
                    for (int t = t_first; t < t_last; ++t) {
                        f32x16 s = qk(t, z16);
#pragma unroll
                        for (int r = 0; r < 16; ++r) { const int c = (r & 3) + 8 * (r >> 2); const bool keep = !(has_first && t == t_first) || c > qq; m = fmaxf(m, keep ? s[r] : NINF); }
                    }
                    m = fmaxf(m, __shfl_xor(m, 32));
                }
                f32x16 nm, p;
#pragma unroll
                for (int r = 0; r < 16; ++r) { nm[r] = -m; p[r] = __builtin_amdgcn_exp2f(sd[r] - m); }
                o = pv(t_last, p, z16);
                for (int t = t_first; t < t_last; ++t) {
                    f32x16 s = qk(t, nm);
                    if (has_first && t == t_first) {
#pragma unroll
                        for (int r = 0; r < 16; ++r) { const int c = (r & 3) + 8 * (r >> 2); s[r] = c > qq ? s[r] : NINF; }
                    }
#pragma unroll
                    for (int r = 0; r < 16; ++r) p[r] = __builtin_amdgcn_exp2f(s[r]);
                    o = pv(t, p, o);
                }
                l = o[12];
                if (__all(l <= 1048576.f)) break;
            }
        }
        STAMP(15);
        const float il = 1.f / l;
        const int T16 = 8 * ai + 2 * qt + (l31 >> 4);
        const u32x4 om = u32x4{pk(o[0] * il, o[1] * il), pk(o[2] * il, o[3] * il), pk(o[4] * il, o[5] * il), pk(o[6] * il, o[7] * il)};
        __builtin_amdgcn_raw_buffer_store_b128(om, wsr, WS_OM + ((T16 * 16 + gp) * 64 + (2 * hd + hh) * 16 + (l31 & 15)) * 16, 0, 16);
        const unsigned ra = pk(o[8] * il, o[9] * il), rb2 = pk(o[10] * il, o[11] * il), rv = hd ? rb2 : ra;
        const u32x4 rq = u32x4{rv, (unsigned)__builtin_amdgcn_mov_dpp(rv, 0x55, 0xF, 0xF, true), (unsigned)__builtin_amdgcn_mov_dpp(rv, 0xAA, 0xF, 0xF, true), (unsigned)__builtin_amdgcn_mov_dpp(rv, 0xFF, 0xF, 0xF, true)};
        if (hh == 0 && (l31 & 3) == 0) __builtin_amdgcn_raw_buffer_store_b128(rq, wsr, WS_OR + ((((4 * ai + qt) * 16 + gp) * 2 + hd) * 32 + l31) * 4, 0, 16);
    }
    WAITVM(0);
    if (arrive(L0 + SY_ODR, 8, lane)) __hip_atomic_store((GAS u64*)(ws + WS_OFLAG) + ai * 16 + gp, nonce, RLX);
    STAMP(20);
    if (w < 4 || w == 7) {
        poll_idx((const GAS u64*)(ws + WS_YFLAG), lane < 36 ? 36 * bc + lane : -1, nonce);
        if (w < 4 || lane < 32) {
            const int chB = 72 * bc + 2 * cp, t0 = 64 * br + 8 * oct;
            const u32x4 y0 = __builtin_amdgcn_raw_buffer_load_b128(wsr, WS_Y + chB * 4096 + t0 * 2, 0, 16);
            const u32x4 y1 = __builtin_amdgcn_raw_buffer_load_b128(wsr, WS_Y + (chB + 1) * 4096 + t0 * 2, 0, 16);
            const float D0 = blo(dv), D1 = bhi(dv);
            unsigned zz[8];
#pragma unroll
            for (int t = 0; t < 8; ++t) {
                const int slot = (t & 1) * 4 + (t >> 1);
                const float ya = (slot & 1) ? bhi(y0[slot >> 1]) : blo(y0[slot >> 1]);
                const float yb = (slot & 1) ? bhi(y1[slot >> 1]) : blo(y1[slot >> 1]);
                const unsigned kd = pk(blo(kvp[t]) * D0, bhi(kvp[t]) * D1);
                zz[t] = pk(ya + blo(kd), yb + bhi(kd));
            }
            spin4(L0 + SY_QRD, 1); spin4(L0 + SY_QRD + 16, 1);
#pragma unroll
            for (int t = 0; t < 8; ++t) st32(L0 + L_Z + oct * 1184 + t * 144 + cp * 4, zz[t]);
        }
    } else if (w == 4) {
        poll_idx((const GAS u64*)(ws + WS_OFLAG), lane < 16 ? (br >> 1) * 16 + lane : -1, nonce);
    }
    STAMP(21);
    BAR();
    STAMP(22);
    {
        const int tt = w & 3, kh = w >> 2, T16 = 4 * br + tt;
        u32x4 of[9];
#pragma unroll
        for (int s = 0; s < 8; ++s) of[s] = __builtin_amdgcn_raw_buffer_load_b128(wsr, WS_OM + ((T16 * 16 + 8 * kh + s) * 64 + lane) * 16, 0, 16);
#pragma unroll
        for (int d = 0; d < 4; ++d) of[8][d] = __builtin_amdgcn_raw_buffer_load_b32(wsr, WS_OR + ((((T16 >> 1) * 16 + 8 * kh + 2 * g4 + (d >> 1)) * 2 + (d & 1)) * 32 + 16 * (T16 & 1) + l15) * 4, 0, 16);
        {
            auto gate = [&](const u32x4& qv, int tk, int gr) {
                const u32x4 z = lds128(L0 + L_Z + tk * 144 + (tk >> 3) * 32 + gr * 16);
                u32x4 r;
#pragma unroll
                for (int d = 0; d < 4; ++d) r[d] = pk(blo(qv[d]) * blo(z[d]), bhi(qv[d]) * bhi(z[d]));
                *(u32x4*)((char*)out + (size_t)(64 * br + tk) * 2304 + 1152 + bc * 144 + gr * 16) = r;
            };
            gate(qg0, tk0, gr0);
            if (w == 5) gate(qg1, tk1, gr1);
        }
        f32x4 acc[5];
#pragma unroll
        for (int j = 0; j < 5; ++j) acc[j] = f32x4{0, 0, 0, 0};
        const int q4 = (lane >> 2) & 3, p4 = lane & 3, hd = g4 >> 1, hq = g4 & 1;
        const unsigned mlane = L0 + (20 * hd + 4 * hq + q4) * 144 + p4 * 8;
        const int gr = 8 * kh + 2 * g4;
        const unsigned rlane = L0 + (gr < 6 ? L_WO1 + gr * 5184 : L_WO2 + (gr - 6) * 5184) + (16 + q4) * 144;
#pragma unroll
        for (int s = 0; s < 9; ++s) {
            const int gq = 8 * kh + s;
            const unsigned mrow = mlane + (gq < 6 ? L_WO1 + gq * 5184 : L_WO2 + (gq - 6) * 5184);
#pragma unroll
            for (int j = 0; j < 5; ++j) {
                u32x2 a, c;
                if (s < 8) {
                    const unsigned ad = j < 4 ? mrow + j * 32 : mrow - p4 * 8 + 128 + (p4 & 1) * 8;
                    a = ldstr(ad); c = ldstr(ad + 8 * 144);
                } else {
                    const unsigned ad = rlane + (j < 4 ? j * 32 + p4 * 8 : 128 + (p4 & 1) * 8);
                    a = ldstr(ad); c = ldstr(ad + 5184);
                }
                acc[j] = mfma16(u32x4{a[0], a[1], c[0], c[1]}, of[s], acc[j]);
            }
        }
        const unsigned sc = L0 + L_X + tt * 5120 + lane * 16;
        if (kh == 0) { stf4(sc + 2048, acc[2]); stf4(sc + 3072, acc[3]); }
        else { stf4(sc, acc[0]); stf4(sc + 1024, acc[1]); stf4(sc + 4096, acc[4]); }
        BAR();
        char* op = (char*)out + (size_t)(16 * T16 + l15) * 2304 + bc * 144;
        auto st2 = [&](const f32x4& va, const f32x4& vb, int j0) {
            const unsigned a0 = pk(va[0], va[1]), a1 = pk(va[2], va[3]), b0 = pk(vb[0], vb[1]), b1 = pk(vb[2], vb[3]);
            auto s0 = __builtin_amdgcn_permlane16_swap(a0, b0, false, false);
            auto s1 = __builtin_amdgcn_permlane16_swap(a1, b1, false, false);
            return u32x4{s0[0], s1[0], s0[1], s1[1]};
        };
        if (kh == 0) {
            const f32x4 v0 = acc[0] + ldsf4(sc), v1 = acc[1] + ldsf4(sc + 1024), v4 = acc[4] + ldsf4(sc + 4096);
            *(u32x4*)(op + (g4 & 1) * 32 + (g4 >> 1) * 16) = st2(v0, v1, 0);
            const u32x4 r4 = st2(v4, v4, 4);
            if (g4 == 0) *(u32x4*)(op + 128) = r4;
        } else {
            const f32x4 v2 = acc[2] + ldsf4(sc + 2048), v3 = acc[3] + ldsf4(sc + 3072);
            *(u32x4*)(op + 64 + (g4 & 1) * 32 + (g4 >> 1) * 16) = st2(v2, v3, 2);
        }
    }
    STAMP(23);
    if (b == 0 && w == 7) {
        for (int k = 0; k < 4; ++k) poll_idx((const GAS u64*)(ws + WS_KFLAG), 64 * k + lane, nonce);
        if (lane == 0) __hip_atomic_store((GAS u64*)ws, nonce, RLX);
    }
}

extern "C" void kernel_launch(void* const* d_in, const int* in_sizes, int n_in, void* d_out, int out_size, void* d_ws, size_t ws_size, hipStream_t stream) {
    fused_kernel<<<256, 512, 0, stream>>>((const unsigned short*)d_in[0], (const unsigned short*)d_in[1], (const unsigned short*)d_in[2], (const unsigned short*)d_in[3],
                                          (const unsigned short*)d_in[4], (const unsigned short*)d_in[5], (const unsigned short*)d_in[6], (const unsigned short*)d_in[7],
                                          (const unsigned short*)d_in[8], (unsigned short*)d_out, (char*)d_ws);
}
```

```cpp
#include <hip/hip_runtime.h>
#include <stdint.h>

#define GAS __attribute__((address_space(1)))
#define LAS __attribute__((address_space(3)))
#define DEVI __device__ __forceinline__
typedef unsigned long long u64;
typedef __attribute__((ext_vector_type(8))) __bf16 bf16x8;
typedef __attribute__((ext_vector_type(2))) __bf16 bf16x2;
typedef __attribute__((ext_vector_type(2))) float f32x2;
typedef __attribute__((ext_vector_type(4))) float f32x4;
typedef __attribute__((ext_vector_type(16))) float f32x16;
typedef __attribute__((ext_vector_type(4))) unsigned u32x4;
typedef __attribute__((ext_vector_type(2))) unsigned u32x2;
typedef __attribute__((ext_vector_type(4))) short s16x4;

constexpr int WS_KFLAG = 256, WS_QFLAG = 4096, WS_OFLAG = 8192, WS_YFLAG = 12288;
constexpr int WS_R0 = 32768;
constexpr int WS_QKV = WS_R0 + 2359296;
constexpr int WS_OM = WS_QKV + 7077888;
constexpr int WS_OR = WS_OM + 2097152;
constexpr int WS_Y = WS_OR + 262144;
constexpr int WS_END = WS_Y + 2359296;
constexpr int WS_TS = 32 << 20;
constexpr int L_B1K = 131072, L_B1V = 141312;
constexpr int L_B1S = 151552;
constexpr int KCS = 4160, KCH = 4 * KCS;
constexpr int RCS = 4576, RCH = 2 * RCS;
constexpr int L_KB = 32768, L_RB = 131072;
constexpr int L_KA = 66048, L_RA = 0;
constexpr int L_SCR = 114688;
constexpr int BLK = 10752, PCS = 2112;
constexpr int L_KBLK = 0, L_VBLK = 66048, L_QBLK = 66048 + 3 * BLK;
constexpr int L_WO1 = 32768, L_WO2 = 109056;
constexpr int L_ONES = 161280;
constexpr int L_Z = L_QBLK;
constexpr int L_X = 0;
constexpr int L_SYNC = 163584;
constexpr int SY_LAND = L_SYNC, SY_CONS = L_SYNC + 16, SY_PW = L_SYNC + 32, SY_PR = L_SYNC + 48, SY_PREP = L_SYNC + 64, SY_QDR = L_SYNC + 68, SY_YDR = L_SYNC + 72,
              SY_ODR = L_SYNC + 76, SY_NONCE = L_SYNC + 80, SY_KVD = L_SYNC + 96, SY_QRD = L_SYNC + 112, SY_QSEEN = L_SYNC + 144;
constexpr int LDS_BYTES = 163840;
#define POLLS 8
constexpr unsigned SPIN_CAP = 1u << 17, LSPIN_CAP = 1u << 20;

#define STAMPW(k) do { } while (0)
#define STAMPC(k) do { } while (0)
#define STAMP(k) do { } while (0)

DEVI f32x4 mfma16(u32x4 a, u32x4 b, f32x4 c) { return __builtin_amdgcn_mfma_f32_16x16x32_bf16(__builtin_bit_cast(bf16x8, a), __builtin_bit_cast(bf16x8, b), c, 0, 0, 0); }
DEVI f32x16 mfma32(u32x4 a, u32x4 b, f32x16 c) { return __builtin_amdgcn_mfma_f32_32x32x16_bf16(__builtin_bit_cast(bf16x8, a), __builtin_bit_cast(bf16x8, b), c, 0, 0, 0); }
DEVI f32x16 mfma32k8(u32x2 a, u32x2 b, f32x16 c) { return __builtin_amdgcn_mfma_f32_32x32x8bf16_1k(__builtin_bit_cast(s16x4, a), __builtin_bit_cast(s16x4, b), c, 0, 0, 0); }
DEVI unsigned pk(float lo, float hi) { f32x2 v = {lo, hi}; return __builtin_bit_cast(unsigned, __builtin_convertvector(v, bf16x2)); }
DEVI float blo(unsigned v) { return __builtin_bit_cast(float, v << 16); }
DEVI float bhi(unsigned v) { return __builtin_bit_cast(float, v & 0xffff0000u); }
DEVI u32x4 lds128(unsigned a) { return *(LAS const u32x4*)(uintptr_t)a; }
DEVI u32x2 lds64(unsigned a) { return *(LAS const u32x2*)(uintptr_t)a; }
DEVI unsigned lds32(unsigned a) { return *(LAS const unsigned*)(uintptr_t)a; }
DEVI void st128(unsigned a, u32x4 v) { *(LAS u32x4*)(uintptr_t)a = v; }
DEVI void st64(unsigned a, u32x2 v) { *(LAS u32x2*)(uintptr_t)a = v; }
DEVI void st32(unsigned a, unsigned v) { *(LAS unsigned*)(uintptr_t)a = v; }
DEVI f32x4 ldsf4(unsigned a) { return *(LAS const f32x4*)(uintptr_t)a; }
DEVI void stf4(unsigned a, f32x4 v) { *(LAS f32x4*)(uintptr_t)a = v; }
DEVI u32x2 ldstr(unsigned a) { return __builtin_bit_cast(u32x2, __builtin_amdgcn_ds_read_tr16_b64_v4i16((LAS s16x4*)(uintptr_t)a)); }
DEVI u32x4 vlds128(unsigned a) { return *(volatile LAS const u32x4*)(uintptr_t)a; }
DEVI unsigned vlds32(unsigned a) { return *(volatile LAS const unsigned*)(uintptr_t)a; }
DEVI void vst32(unsigned a, unsigned v) { *(volatile LAS unsigned*)(uintptr_t)a = v; }
DEVI void dma16(const void* g, unsigned lds) {
    unsigned sv;
    asm volatile("s_mov_b32 %0, m0\n\ts_mov_b32 m0, %2\n\ts_nop 0\n\tglobal_load_lds_dwordx4 %1, off\n\ts_mov_b32 m0, %0" : "=&s"(sv) : "v"(g), "s"(lds) : "memory");
}
DEVI void dmaS(unsigned voff, const void* sbase, unsigned lds) {
    unsigned sv;
    asm volatile("s_mov_b32 %0, m0\n\ts_mov_b32 m0, %3\n\ts_nop 0\n\tglobal_load_lds_dwordx4 %1, %2\n\ts_mov_b32 m0, %0" : "=&s"(sv) : "v"(voff), "s"(sbase), "s"(lds) : "memory");
}
#define WAITVM(n) asm volatile("s_waitcnt vmcnt(%0)" ::"i"(n) : "memory")
#define BAR() do { asm volatile("s_waitcnt lgkmcnt(0)\n\ts_barrier" ::: "memory"); } while (0)
#define CFENCE() asm volatile("" ::: "memory")
#define SB0() __builtin_amdgcn_sched_barrier(0)
#define RLX __ATOMIC_RELAXED, __HIP_MEMORY_SCOPE_AGENT
DEVI unsigned rfl(unsigned v) { return __builtin_amdgcn_readfirstlane(v); }
DEVI unsigned umin4(u32x4 v) { const unsigned a = v[0] < v[1] ? v[0] : v[1], c = v[2] < v[3] ? v[2] : v[3]; return a < c ? a : c; }

DEVI void spin4(unsigned a, unsigned need) {
#pragma nounroll
    for (unsigned s = 0; s < LSPIN_CAP; ++s) {
        if (rfl(umin4(vlds128(a))) >= need) break;
        __builtin_amdgcn_s_sleep(1);
    }
    CFENCE();
}
DEVI void spin1(unsigned a, unsigned need) {
#pragma nounroll
    for (unsigned s = 0; s < LSPIN_CAP; ++s) {
        if (rfl(vlds32(a)) >= need) break;
        __builtin_amdgcn_s_sleep(1);
    }
    CFENCE();
}
DEVI void signal(unsigned a, unsigned v, int lane) { CFENCE(); if (lane == 0) vst32(a, v); CFENCE(); }
DEVI bool arrive(unsigned a, unsigned n, int lane) {
    CFENCE();
    unsigned old = 0;
    if (lane == 0) old = __hip_atomic_fetch_add((LAS unsigned*)(uintptr_t)a, 1u, __ATOMIC_RELAXED, __HIP_MEMORY_SCOPE_WORKGROUP);
    CFENCE();
    return lane == 0 && old == n - 1;
}
template <int SL = 2>
DEVI void poll_idx(const GAS u64* f, int idx, u64 nonce) {
#pragma nounroll
    for (unsigned spins = 0; spins < SPIN_CAP; ++spins) {
        u64 v = nonce;
        if (idx >= 0) v = __hip_atomic_load(f + idx, RLX);
        if (__all(v == nonce)) break;
        __builtin_amdgcn_s_sleep(SL);
    }
    __builtin_amdgcn_fence(__ATOMIC_ACQUIRE, "wavefront");
    CFENCE();
}

__device__ constexpr int CMU[36] = {0,0,0,0,1,1,1,1,2,2,2,0,3,3,3,2,4,4,1,0,5,5,4,3,6,2,1,0,7,6,5,4,3,2,1,0};
__device__ constexpr int CNU[36] = {7,6,5,4,6,5,4,3,5,4,3,3,4,3,2,2,3,2,2,2,2,1,1,1,1,1,1,1,0,0,0,0,0,0,0,0};

__global__ __launch_bounds__(512, 2) void fused_kernel(const unsigned short* __restrict__ x, const unsigned short* __restrict__ wqkv, const unsigned short* __restrict__ wo,
                                                       const unsigned short* __restrict__ bq, const unsigned short* __restrict__ bk, const unsigned short* __restrict__ bv,
                                                       const unsigned short* __restrict__ shk, const unsigned short* __restrict__ ssk, const unsigned short* __restrict__ ssd,
                                                       unsigned short* __restrict__ out, char* __restrict__ ws) {
    __shared__ __attribute__((aligned(256))) char smem[LDS_BYTES];
    const unsigned L0 = (unsigned)(uintptr_t)(LAS char*)smem;
    const int tid = threadIdx.x, lane = tid & 63, w = __builtin_amdgcn_readfirstlane(tid >> 6);
    const int b = blockIdx.x, xcd = b & 7, jj = b >> 3;
    const bool busy = jj < 28;
    const int ri = 4 * (xcd >> 1) + (jj & 3), cj = 7 * (xcd & 1) + (jj >> 2);
    const int br = 4 * xcd + (jj & 3), bc = jj >> 2;
    const int ai = (31 - jj) >> 1, gp = 2 * xcd + (jj & 1);
    const int pairB = busy ? xcd * 28 + jj : 225 + 2 * (xcd * 4 + jj - 28);
    const int l15 = lane & 15, g4 = lane >> 4, l31 = lane & 31, hh = lane >> 5;
    const __amdgpu_buffer_rsrc_t wsr = __builtin_amdgcn_make_buffer_rsrc(ws, 0, WS_END, 0x00020000);
    STAMP(0);
    auto b1_stage = [&]() {
        for (int u = w; u < 20; u += 8) {
            const bool isk = u < 10;
            const int q = (isk ? u : u - 10) * 64 + lane;
            const int g = q / 74; int r = q - g * 74; r = r < 72 ? r : 71;
            int row = 8 * g + r / 9; const int cc = r - (r / 9) * 9;
            if (isk) {
                row = row < 68 ? row : 67;
                int tok = 64 * br - 4 + row; tok = tok < 0 ? 0 : tok;
                dma16((const char*)bk + tok * 1152 + bc * 144 + cc * 16, L0 + L_B1K + u * 1024);
            } else {
                row = row < 64 ? row : 63;
                dma16((const char*)bv + (64 * br + row) * 1152 + bc * 144 + cc * 16, L0 + L_B1V + (u - 10) * 1024);
            }
        }
        if (w == 7) dma16(lane < 36 ? (const char*)shk + bc * 576 + lane * 16 : (const char*)ssd + bc * 144 + (lane < 45 ? lane - 36 : 0) * 16, L0 + L_B1S);
    };
    unsigned kvp[8];
#pragma unroll
    for (int t = 0; t < 8; ++t) kvp[t] = 0;
    unsigned dv = 0;
    const int bl = w < 4 ? tid : 256 + l31;
    const int cp = bl >> 3, oct = bl & 7;
    auto b1_compute = [&]() {
        const u32x4 shv = lds128(L0 + L_B1S + cp * 16);
        dv = lds32(L0 + L_B1S + 576 + cp * 4);
        unsigned vv[8];
#pragma unroll
        for (int t = 0; t < 8; ++t) vv[t] = lds32(L0 + L_B1V + oct * 1184 + t * 144 + cp * 4);
        f32x2 k[11];
#pragma unroll
        for (int q = 0; q < 11; ++q) {
            const unsigned v = lds32(L0 + L_B1K + oct * 1184 + (1 + q) * 144 + ((1 + q) >> 3) * 32 + cp * 4);
            k[q] = f32x2{blo(v), bhi(v)};
        }
        if (br == 0 && oct == 0) { k[0] = f32x2{0.f, 0.f}; k[1] = k[0]; k[2] = k[0]; }
        const f32x2 s0 = {blo(shv[0]), blo(shv[2])}, s1 = {bhi(shv[0]), bhi(shv[2])}, s2 = {blo(shv[1]), blo(shv[3])}, s3 = {bhi(shv[1]), bhi(shv[3])};
#pragma unroll
        for (int t = 0; t < 8; ++t) {
            const unsigned v = vv[t];
            f32x2 a = k[t] * s3;
            a = __builtin_elementwise_fma(k[t + 1], s2, a);
            a = __builtin_elementwise_fma(k[t + 2], s1, a);
            a = __builtin_elementwise_fma(k[t + 3], s0, a);
            const unsigned ks = pk(a[0], a[1]);
            kvp[t] = pk(blo(ks) * blo(v), bhi(ks) * bhi(v));
        }
        u32x4 r0, r1;
#pragma unroll
        for (int d = 0; d < 4; ++d) {
            r0[d] = (kvp[7 - 2 * d] & 0xffffu) | (kvp[6 - 2 * d] << 16);
            r1[d] = (kvp[7 - 2 * d] >> 16) | (kvp[6 - 2 * d] & 0xffff0000u);
        }
        const int off = WS_R0 + (72 * bc + 2 * cp) * 4096 + (2040 - 64 * br - 8 * oct) * 2;
        __builtin_amdgcn_raw_buffer_store_b128(r0, wsr, off, 0, 16);
        __builtin_amdgcn_raw_buffer_store_b128(r1, wsr, off + 4096, 0, 16);
    };
    auto get_nonce = [&]() {
        const u32x2 v = *(volatile LAS const u32x2*)(uintptr_t)(L0 + SY_NONCE);
        return ((u64)rfl(v[1]) << 32) | rfl(v[0]);
    };
    auto gather = [&](int sw, u64 nonce) {
        spin4(L0 + SY_CONS, 9);
        if (sw == 0) {
            int idx = -1;
            if (lane < 14) {
                const int j = lane >> 1, sec = j < 3 ? 1 : (j < 6 ? 2 : 0), rr = j < 6 ? ai - 2 + (j < 3 ? j : j - 3) : ai;
                const int c0 = 576 * sec + 36 * gp - 4 * (gp & 1) + (lane & 1) * 39;
                if (rr >= 0) idx = rr * 16 + (c0 >> 7);
            }
            poll_idx<POLLS>((const GAS u64*)(ws + WS_QFLAG), idx, nonce);
            signal(L0 + SY_QSEEN, 1, lane);
        } else {
            spin1(L0 + SY_QSEEN, 1);
        }
        STAMPW(24 + sw);
        u32x4 g[18];
        const int p0 = (36 * gp - 4 * (gp & 1)) >> 3;
#pragma unroll
        for (int k = 0; k < 18; ++k) {
            const int u = sw + 4 * k, j = u / 10, e = u - 10 * j;
            const int sec = j < 3 ? 1 : (j < 6 ? 2 : 0), rr = j < 6 ? ai - 2 + (j < 3 ? j : j - 3) : ai;
            if (u < 70 && rr >= 0) g[k] = __builtin_amdgcn_raw_buffer_load_b128(wsr, lane * 16, WS_QKV + ((rr * 216 + 72 * sec + p0) * 128) * 16 + e * 1024, 16);
        }
#pragma unroll
        for (int k = 0; k < 18; ++k) {
            const int u = sw + 4 * k, j = u / 10, e = u - 10 * j;
            const int rr = j < 6 ? ai - 2 + (j < 3 ? j : j - 3) : ai;
            const unsigned dst = L0 + (j < 3 ? L_KBLK + j * BLK : L_VBLK + (j - 3) * BLK) + (e >> 1) * PCS + (e & 1) * 1024 + lane * 16;
            if (u < 70 && rr >= 0) st128(dst, g[k]);
        }
    };

    if (w < 4) {
        b1_stage();
        if (w == 0 && (lane < 20 || (lane >= 24 && lane < 44))) vst32(L0 + L_SYNC + lane * 4, lane == 3 ? 0xffffffffu : 0u);
        if (w == 1) {
#pragma unroll
            for (int i = 0; i < 3; ++i) { const int q = i * 64 + lane; if (q < 140) st128(L0 + L_ONES + q * 16, u32x4{0x3f803f80u, 0x3f803f80u, 0x3f803f80u, 0x3f803f80u}); }
        }
        WAITVM(0);
        BAR();
        STAMP(1);
        b1_compute();
        STAMP(2);
        if (busy) {
            const int wt = w & 1, wc = w >> 1;
            const bool live = !(cj == 13 && wc == 1);
            if (live) {
                unsigned xo[4], wof[2];
#pragma unroll
                for (int j = 0; j < 4; ++j) xo[j] = L0 + (64 * wt + l31) * 128 + (((2 * j + hh) ^ ((l31 >> 1) & 7)) * 16);
                {
                    const int q = (lane >> 2) & 3, p = lane & 3;
#pragma unroll
                    for (int ct = 0; ct < 2; ++ct) wof[ct] = L0 + 16384 + (8 * hh + q) * 256 + (((8 * wc + 4 * ct + 2 * (g4 & 1) + (p >> 1)) ^ (q << 2)) * 16) + (p & 1) * 8;
                }
                f32x16 acc[2][2];
#pragma unroll
                for (int r = 0; r < 16; ++r) { acc[0][0][r] = 0.f; acc[0][1][r] = 0.f; acc[1][0][r] = 0.f; acc[1][1][r] = 0.f; }
                u32x4 xf[2][2];
                u32x2 wa[2][2], wc2[2][2];
                auto ldx = [&](int s, int j, int pb, int tt) { xf[pb][tt] = lds128(xo[j] + (s & 3) * 32768 + tt * 4096); };
                auto ldwa = [&](int s, int j, int pb, int ct) { wa[pb][ct] = ldstr(wof[ct] + (s & 3) * 32768 + j * 4096); };
                auto ldwc = [&](int s, int j, int pb, int ct) { wc2[pb][ct] = ldstr(wof[ct] + (s & 3) * 32768 + j * 4096 + 1024); };
                auto W = [&](int pb, int ct) { return u32x4{wa[pb][ct][0], wa[pb][ct][1], wc2[pb][ct][0], wc2[pb][ct][1]}; };
                spin4(L0 + SY_LAND, 1);
                STAMP(3); STAMPC(40);
                ldx(0, 0, 0, 0); ldwa(0, 0, 0, 0); ldwc(0, 0, 0, 0); ldx(0, 0, 0, 1); ldwa(0, 0, 0, 1); ldwc(0, 0, 0, 1);
#pragma unroll
                for (int s = 0; s < 9; ++s) {
                    u32x4 landv;
#pragma unroll
                    for (int j = 0; j < 4; ++j) {
                        const int pb = j & 1, nb = pb ^ 1;
                        const bool more = j < 3 || s < 8;
                        const int ns = j < 3 ? s : s + 1, nj = (j + 1) & 3;
                        SB0();
                        acc[0][0] = mfma32(W(pb, 0), xf[pb][0], acc[0][0]);
                        SB0();
                        if (j == 3 && s < 8) { if (rfl(umin4(landv)) < (unsigned)(s + 2)) spin4(L0 + SY_LAND, s + 2); CFENCE(); }
                        if (more) { ldx(ns, nj, nb, 0); ldwa(ns, nj, nb, 0); ldwc(ns, nj, nb, 0); }
                        SB0();
                        acc[0][1] = mfma32(W(pb, 0), xf[pb][1], acc[0][1]);
                        SB0();
                        if (more) { ldx(ns, nj, nb, 1); ldwa(ns, nj, nb, 1); }
                        SB0();
                        acc[1][0] = mfma32(W(pb, 1), xf[pb][0], acc[1][0]);
                        SB0();
                        if (more) { ldwc(ns, nj, nb, 1); }
                        if (j == 2) {
                            signal(L0 + SY_CONS + w * 4, s + 1, lane);
                        }
                        if (j == 1 && s < 8) landv = vlds128(L0 + SY_LAND);
                        if (j == 0 && s == 2) { WAITVM(0); signal(L0 + SY_KVD + w * 4, 1, lane); }
                        SB0();
                        acc[1][1] = mfma32(W(pb, 1), xf[pb][1], acc[1][1]);
                    }
                }
                STAMP(4); STAMPC(41);
#pragma unroll
                for (int ct = 0; ct < 2; ++ct) {
                    const int n0 = 128 * cj + 64 * wc + 32 * ct;
                    const float sc = n0 < 576 ? 0.34004634f : 1.0f;
#pragma unroll
                    for (int tt = 0; tt < 2; ++tt) {
#pragma unroll
                        for (int pi = 0; pi < 2; ++pi) {
                            const f32x16& a = acc[ct][tt];
                            const unsigned a0 = pk(a[8 * pi] * sc, a[8 * pi + 1] * sc), a1 = pk(a[8 * pi + 2] * sc, a[8 * pi + 3] * sc);
                            const unsigned b0 = pk(a[8 * pi + 4] * sc, a[8 * pi + 5] * sc), b1 = pk(a[8 * pi + 6] * sc, a[8 * pi + 7] * sc);
                            auto s0 = __builtin_amdgcn_permlane32_swap(a0, b0, false, false);
                            auto s1 = __builtin_amdgcn_permlane32_swap(a1, b1, false, false);
                            const int off = WS_QKV + ((ri * 216 + (n0 >> 3) + 2 * pi + hh) * 128 + 64 * wt + 32 * tt + l31) * 16;
                            __builtin_amdgcn_raw_buffer_store_b128(u32x4{s0[0], s1[0], s0[1], s1[1]}, wsr, off, 0, 16);
                        }
                    }
                }
            } else {
                signal(L0 + SY_CONS + w * 4, 9, lane);
                WAITVM(0); signal(L0 + SY_KVD + w * 4, 1, lane);
            }
            STAMP(5);
        } else {
            WAITVM(0); signal(L0 + SY_KVD + w * 4, 1, lane);
        }
        const u64 nonce = get_nonce();
        bool qdone = !busy;
        auto qpublish = [&]() {
            WAITVM(0);
            if (arrive(L0 + SY_QDR, 4, lane)) __hip_atomic_store((GAS u64*)(ws + WS_QFLAG) + ri * 16 + cj, nonce, RLX);
            STAMP(8);
        };
        for (int pass = busy ? 1 : 0; pass < 2; ++pass) {
            if (!qdone && rfl(vlds32(L0 + SY_PREP)) < (unsigned)(pass + 1)) { qpublish(); qdone = true; }
            spin1(L0 + SY_PREP, pass + 1);
            STAMP(6 + 3 * pass);
            const int slot = w >> 1, h = w & 1;
            const unsigned kb = L0 + (pass ? L_KB : L_KA) + slot * KCH, rb = L0 + (pass ? L_RB : L_RA) + slot * RCH;
            const int ch = 2 * (pairB - 1 + pass) + slot;
            const unsigned la = kb + ((l15 + 1) & 3) * KCS + (g4 + ((l15 + 1) >> 2)) * 16 + h * 256;
            const unsigned lb = rb + ((l15 & 1) ? 0 : RCS) + (28 - 4 * (l15 >> 1) + g4) * 16 + h * 256;
            f32x4 acc[8];
#pragma unroll
            for (int n = 0; n < 8; ++n) acc[n] = f32x4{0, 0, 0, 0};
            u32x4 A[2][8], B[2][8];
#pragma unroll
            for (int m = 0; m < 8; ++m) { A[0][m] = lds128(la + m * 512); B[0][m] = lds128(lb + (7 - m) * 512); }
#pragma unroll
            for (int rr = 0; rr < 4; ++rr) {
                const int cur = rr & 1, nx = cur ^ 1;
#pragma unroll
                for (int i = 0; i < 36; ++i) {
                    SB0();
                    acc[CMU[i] + CNU[i]] = mfma16(A[cur][CMU[i]], B[cur][CNU[i]], acc[CMU[i] + CNU[i]]);
                    SB0();
                    if (rr < 3 && (i & 1) == 0 && i < 32) {
                        const int m = i >> 2;
                        if (i & 2) B[nx][7 - m] = lds128(lb + (rr + 1) * 64 + m * 512);
                        else A[nx][m] = lds128(la + (rr + 1) * 64 + m * 512);
                    }
                }
                if (rr == 1 && !qdone) { qpublish(); qdone = true; }
            }
            if (pass == 0) signal(L0 + SY_CONS + w * 4, 9, lane);
            const unsigned mine = L0 + L_SCR + w * 4096 + lane * 16, theirs = L0 + L_SCR + (w ^ 1) * 4096 + lane * 16;
            if (pass == 1) spin1(L0 + SY_PR + (w ^ 1) * 4, busy ? 0 : 1);
            f32x4 y[4];
            if (h) {
#pragma unroll
                for (int n = 0; n < 4; ++n) stf4(mine + n * 1024, acc[n]);
            } else {
#pragma unroll
                for (int n = 0; n < 4; ++n) stf4(mine + n * 1024, acc[4 + n]);
            }
            signal(L0 + SY_PW + w * 4, pass + 1, lane);
            spin1(L0 + SY_PW + (w ^ 1) * 4, pass + 1);
            if (h) {
#pragma unroll
                for (int n = 0; n < 4; ++n) y[n] = acc[4 + n] + ldsf4(theirs + n * 1024);
            } else {
#pragma unroll
                for (int n = 0; n < 4; ++n) y[n] = acc[n] + ldsf4(theirs + n * 1024);
            }
            signal(L0 + SY_PR + w * 4, pass + 1, lane);
            const bool odd = lane & 1;
#pragma unroll
            for (int i = 0; i < 4; i += 2) {
                const unsigned a0 = pk(y[i][0], y[i][1]), a1 = pk(y[i][2], y[i][3]), c0 = pk(y[i + 1][0], y[i + 1][1]), c1 = pk(y[i + 1][2], y[i + 1][3]);
                const unsigned r0 = __builtin_amdgcn_mov_dpp(odd ? a0 : c0, 0xB1, 0xF, 0xF, true), r1 = __builtin_amdgcn_mov_dpp(odd ? a1 : c1, 0xB1, 0xF, 0xF, true);
                const u32x4 v = odd ? u32x4{r0, r1, c0, c1} : u32x4{a0, a1, r0, r1};
                __builtin_amdgcn_raw_buffer_store_b128(v, wsr, WS_Y + ch * 4096 + (4 * h + i + (odd ? 1 : 0)) * 512 + ((l15 >> 1) * 4 + g4) * 16, 0, 16);
            }
            STAMP(7 + 3 * pass);
        }
    } else if (w < 7) {
        const int d = w - 4;
        b1_stage();
        if (busy) {
            const unsigned xv0 = (lane >> 3) * 1152 + (((lane & 7) ^ (lane >> 4)) * 16), xv1 = (lane >> 3) * 1152 + (((lane & 7) ^ (4 + (lane >> 4))) * 16);
            int wch = (lane & 15) ^ ((lane >> 4) << 2);
            if (cj == 13) wch &= 7;
            const unsigned wv = (lane >> 4) * 3456 + wch * 16;
            const char* xb = (const char*)x + (size_t)(128 * ri) * 1152;
            const char* wb = (const char*)wqkv + cj * 256;
            auto issue = [&](int s) {
                const unsigned sl = L0 + (s & 3) * 32768;
#pragma unroll
                for (int i = 0; i < 11; ++i) {
                    int u = d + 3 * i; u = u < 32 ? u : d;
                    if (u < 16) dmaS((u & 1) ? xv1 : xv0, xb + s * 128 + u * 9216, sl + u * 1024);
                    else dmaS(wv, wb + (size_t)(s * 64 + (u - 16) * 4) * 3456, sl + u * 1024);
                }
            };
            const unsigned land = L0 + SY_LAND + d * 4;
            issue(0); issue(1);
            WAITVM(22);
            BAR();
            issue(2); issue(3);
            WAITVM(33); signal(land, 1, lane);
            WAITVM(22); signal(land, 2, lane);
            WAITVM(11); signal(land, 3, lane); spin4(L0 + SY_CONS, 1); issue(4);
            WAITVM(11); signal(land, 4, lane); spin4(L0 + SY_CONS, 2); issue(5);
            WAITVM(11); signal(land, 5, lane); spin4(L0 + SY_CONS, 3); issue(6);
            WAITVM(11); signal(land, 6, lane); spin4(L0 + SY_CONS, 4); issue(7);
            WAITVM(11); signal(land, 7, lane); spin4(L0 + SY_CONS, 5); issue(8);
            WAITVM(11); signal(land, 8, lane);
            WAITVM(0); signal(land, 9, lane);
        } else {
            WAITVM(0);
            BAR();
        }
        gather(d, get_nonce());
    } else {
        const u64 ep = __hip_atomic_load((const GAS u64*)ws, RLX);
        b1_stage();
        WAITVM(0);
        const u64 nonce = (((u64)rfl((unsigned)(ep >> 32)) << 32) | rfl((unsigned)ep)) + 1;
        if (lane == 0) *(volatile LAS u32x2*)(uintptr_t)(L0 + SY_NONCE) = u32x2{(unsigned)nonce, (unsigned)(nonce >> 32)};
        BAR();
        if (lane < 32) b1_compute();
        u32x4 kd0[2][4], kd1[2][4];
        auto kload = [&](int pair) {
#pragma unroll
            for (int c = 0; c < 2; ++c)
#pragma unroll
                for (int r = 0; r < 4; ++r) {
                    const int cq = r * 64 + lane;
                    const char* p = (const char*)ssk + (2 * pair + c) * 4096 + cq * 16;
                    kd0[c][r] = *(const u32x4*)p; kd1[c][r] = *(const u32x4*)(p + (cq < 255 ? 16 : 0));
                }
        };
        auto kcopy = [&](unsigned kb, int c) {
#pragma unroll
            for (int r = 0; r < 4; ++r) {
                const unsigned a = kb + (4 + r * 64 + lane) * 16;
                const u32x4 d0 = kd0[c][r], d1 = kd1[c][r];
                st128(a, d0);
                st128(a + KCS, u32x4{d0[1], d0[2], d0[3], d1[0]});
                st128(a + 2 * KCS, u32x4{d0[2], d0[3], d1[0], d1[1]});
                st128(a + 3 * KCS, u32x4{d0[3], d1[0], d1[1], d1[2]});
            }
            const u32x4 d0 = kd0[c][0];
            if (lane == 0) {
                st128(kb + 3 * 16, u32x4{0, 0, 0, 0});
                st128(kb + KCS + 3 * 16, u32x4{0, 0, 0, d0[0]});
                st128(kb + 2 * KCS + 3 * 16, u32x4{0, 0, d0[0], d0[1]});
                st128(kb + 3 * KCS + 3 * 16, u32x4{0, d0[0], d0[1], d0[2]});
            } else if (lane < 13) {
                const int e = lane - 1;
                st128(kb + (e / 3) * KCS + (e % 3) * 16, u32x4{0, 0, 0, 0});
            }
        };
        u32x4 rd[2][4]; unsigned rn[2][4];
        auto rload = [&](int pair) {
#pragma unroll
            for (int c = 0; c < 2; ++c)
#pragma unroll
                for (int r = 0; r < 4; ++r) {
                    const int cq = r * 64 + lane, off = WS_R0 + (2 * pair + c) * 4096 + cq * 16;
                    rd[c][r] = __builtin_amdgcn_raw_buffer_load_b128(wsr, off, 0, 16);
                    rn[c][r] = __builtin_amdgcn_raw_buffer_load_b32(wsr, off + (cq < 255 ? 16 : 0), 0, 16);
                }
        };
        auto rcopy = [&](unsigned rb0) {
#pragma unroll
            for (int c = 0; c < 2; ++c) {
                const unsigned rb = rb0 + c * RCH;
#pragma unroll
                for (int r = 0; r < 4; ++r) {
                    const int cq = r * 64 + lane;
                    const u32x4 dd = rd[c][r]; const unsigned nx = cq < 255 ? rn[c][r] : 0u;
                    st128(rb + cq * 16, dd);
                    st128(rb + RCS + cq * 16, u32x4{__builtin_amdgcn_alignbit(dd[1], dd[0], 16), __builtin_amdgcn_alignbit(dd[2], dd[1], 16), __builtin_amdgcn_alignbit(dd[3], dd[2], 16), __builtin_amdgcn_alignbit(nx, dd[3], 16)});
                }
                if (lane < 60) st128(rb + (lane >= 30 ? RCS : 0) + (256 + (lane >= 30 ? lane - 30 : lane)) * 16, u32x4{0, 0, 0, 0});
            }
        };
        WAITVM(0);
        spin4(L0 + SY_KVD, 1);
        if (lane == 0) __hip_atomic_store((GAS u64*)(ws + WS_KFLAG) + bc * 32 + br, nonce, RLX);
        STAMPW(11);
        if (!busy) { kload(pairB - 1); kcopy(L0 + L_KA, 0); kcopy(L0 + L_KA + KCH, 1); }
        kload(pairB);
        if (!busy) { kcopy(L0 + L_KB, 0); kcopy(L0 + L_KB + KCH, 1); }
        {
            const int cB = (2 * pairB) / 72, cA = (2 * pairB - 2) / 72;
            int idx = lane < 32 ? cB * 32 + lane : -1;
            if (!busy && lane >= 32) idx = cA * 32 + lane - 32;
            poll_idx((const GAS u64*)(ws + WS_KFLAG), idx, nonce);
        }
        STAMPW(12);
        if (!busy) { rload(pairB - 1); rcopy(L0 + L_RA); signal(L0 + SY_PREP, 1, lane); }
        rload(pairB);
        rcopy(L0 + L_RB);
        if (busy) {
            spin4(L0 + SY_CONS, 6); kcopy(L0 + L_KB, 0);
            spin4(L0 + SY_CONS, 7); kcopy(L0 + L_KB + KCH, 1);
        }
        signal(L0 + SY_PREP, 2, lane);
        STAMPW(13);
        gather(3, nonce);
    }
    STAMPW(16 + w);
    BAR();
    STAMP(14);
    const u64 nonce = get_nonce();
    auto wo_dma = [&](int k) {
        const int u = w + 8 * k;
        if (u < 82) {
            const int q = (u < 31 ? u : u - 31) * 64 + lane;
            int row = q / 9; const int cc = q - row * 9;
            row += u < 31 ? 0 : 216; row = row < 576 ? row : 575;
            dma16((const char*)wo + row * 1152 + bc * 144 + cc * 16, L0 + (u < 31 ? L_WO1 + u * 1024 : L_WO2 + (u - 31) * 1024));
        }
    };
    auto ypublish = [&]() {
        if (w < 4) {
            WAITVM(0);
            if (arrive(L0 + SY_YDR, 4, lane)) {
                __hip_atomic_store((GAS u64*)(ws + WS_YFLAG) + pairB, nonce, RLX);
                if (!busy) __hip_atomic_store((GAS u64*)(ws + WS_YFLAG) + pairB - 1, nonce, RLX);
            }
        }
    };
    const int id0 = tid, tk0 = id0 / 9, gr0 = id0 - 9 * tk0, id1 = 512 + lane, tk1 = id1 / 9, gr1 = id1 - 9 * tk1;
    u32x4 qg0, qg1 = u32x4{0, 0, 0, 0};
    auto q_prefetch = [&]() {
        qg0 = *(const u32x4*)((const char*)bq + (64 * br + tk0) * 1152 + bc * 144 + gr0 * 16);
        if (w == 5) qg1 = *(const u32x4*)((const char*)bq + (64 * br + tk1) * 1152 + bc * 144 + gr1 * 16);
    };
    {
        const int hd = w >> 2, qt = w & 3, cs = 4 * (gp & 1);
        u32x4 qm = lds128(L0 + L_QBLK + (2 * hd + hh) * PCS + (32 * qt + l31) * 16);
        u32x2 qr = lds64(L0 + L_QBLK + (2 * hd + 2) * PCS + (32 * qt + l31) * 16 + hh * 8);
        signal(L0 + SY_QRD + w * 4, 1, lane);
        {
            const int lo = cs + 18 * hd;
#pragma unroll
            for (int d = 0; d < 4; ++d) { const int c = 8 * (2 * hd + hh) + 2 * d; qm[d] = (c >= lo && c < lo + 18) ? qm[d] : 0u; }
#pragma unroll
            for (int d = 0; d < 2; ++d) { const int c = 8 * (2 * hd + 2) + 4 * hh + 2 * d; qr[d] = (c >= lo && c < lo + 18) ? qr[d] : 0u; }
        }
        const unsigned kA = L0 + L_KBLK + (2 * hd + hh) * PCS + l31 * 16, kB = L0 + L_KBLK + (2 * hd + 2) * PCS + l31 * 16 + hh * 8;
        unsigned vA, vsel;
        {
            const int q = (lane >> 2) & 3, p = lane & 3;
            const bool ones = (g4 & 1) && p > 0;
            const int c = (g4 & 1) ? cs + 16 : cs + (hd ? 20 : 0) + 4 * p;
            vA = ones ? L0 + L_ONES + 64 * ((hd ? 1 : 3) + hh) : L0 + L_VBLK + (c >> 3) * PCS + (4 * hh + q) * 16 + ((c >> 2) & 1) * 8;
            vsel = ones ? 0u : 0xffffffffu;
        }
        const int qq = l31 - 4 * hh;
        const int t_first = ai >= 2 ? qt : (ai == 1 ? 4 : 8), t_last = 8 + qt;
        const bool has_first = ai >= 2;
        auto toff = [&](int t) { return (unsigned)((t >> 2) * BLK + (t & 3) * 512); };
        auto qk = [&](int t, const f32x16& c) {
            const unsigned o = toff(t);
            const u32x4 km = lds128(kA + o);
            const u32x2 kr = lds64(kB + o);
            return mfma32k8(kr, qr, mfma32(km, qm, c));
        };
        auto pv = [&](int t, const f32x16& p, const f32x16& o) {
            const unsigned va = vA + (toff(t) & vsel);
            const u32x2 t0 = ldstr(va), t1 = ldstr(va + 128), t2 = ldstr(va + 256), t3 = ldstr(va + 384);
            const u32x4 p0 = u32x4{pk(p[0], p[1]), pk(p[2], p[3]), pk(p[4], p[5]), pk(p[6], p[7])};
            const u32x4 p1 = u32x4{pk(p[8], p[9]), pk(p[10], p[11]), pk(p[12], p[13]), pk(p[14], p[15])};
            return mfma32(u32x4{t2[0], t2[1], t3[0], t3[1]}, p1, mfma32(u32x4{t0[0], t0[1], t1[0], t1[1]}, p0, o));
        };
        const float NINF = -__builtin_inff();
        f32x16 z16;
#pragma unroll
        for (int r = 0; r < 16; ++r) z16[r] = 0.f;
        f32x16 sd = qk(t_last, z16);
        float m = NINF;
#pragma unroll
        for (int r = 0; r < 16; ++r) { const int c = (r & 3) + 8 * (r >> 2); sd[r] = c <= qq ? sd[r] : NINF; m = fmaxf(m, sd[r]); }
        m = fmaxf(m, __shfl_xor(m, 32));
        f32x16 o;
        float l;
        bool redo;
        if (ai >= 2) {
            f32x16 nm, p, s[2];
#pragma unroll
            for (int r = 0; r < 16; ++r) { nm[r] = -m; }
            s[0] = qk(qt, nm);
#pragma unroll
            for (int r = 0; r < 16; ++r) p[r] = __builtin_amdgcn_exp2f(sd[r] - m);
            o = pv(t_last, p, z16);
#pragma unroll
            for (int i = 0; i < 8; ++i) {
                if (i < 7) s[(i + 1) & 1] = qk(qt + i + 1, nm);
                f32x16 sc = s[i & 1];
                if (i == 0) {
#pragma unroll
                    for (int r = 0; r < 16; ++r) { const int c = (r & 3) + 8 * (r >> 2); sc[r] = c > qq ? sc[r] : NINF; }
                }
#pragma unroll
                for (int r = 0; r < 16; ++r) p[r] = __builtin_amdgcn_exp2f(sc[r]);
                o = pv(qt + i, p, o);
                if (i == 2) { ypublish(); q_prefetch(); }
                if (i >= 3) { wo_dma(2 * (i - 3)); wo_dma(2 * (i - 3) + 1); }
            }
            wo_dma(10);
            l = o[12];
            redo = !__all(l <= 1048576.f);
        } else {
            ypublish(); q_prefetch();
            for (int k = 0; k < 11; ++k) wo_dma(k);
            redo = true;
        }
        if (redo) {
            for (int attempt = ai >= 2 ? 1 : 0; attempt < 2; ++attempt) {
                if (attempt == 1) {
                    for (int t = t_first; t < t_last; ++t) {
                        f32x16 s = qk(t, z16);
#pragma unroll
                        for (int r = 0; r < 16; ++r) { const int c = (r & 3) + 8 * (r >> 2); const bool keep = !(has_first && t == t_first) || c > qq; m = fmaxf(m, keep ? s[r] : NINF); }
                    }
                    m = fmaxf(m, __shfl_xor(m, 32));
                }
                f32x16 nm, p;
#pragma unroll
                for (int r = 0; r < 16; ++r) { nm[r] = -m; p[r] = __builtin_amdgcn_exp2f(sd[r] - m); }
                o = pv(t_last, p, z16);
                for (int t = t_first; t < t_last; ++t) {
                    f32x16 s = qk(t, nm);
                    if (has_first && t == t_first) {
#pragma unroll
                        for (int r = 0; r < 16; ++r) { const int c = (r & 3) + 8 * (r >> 2); s[r] = c > qq ? s[r] : NINF; }
                    }
#pragma unroll
                    for (int r = 0; r < 16; ++r) p[r] = __builtin_amdgcn_exp2f(s[r]);
                    o = pv(t, p, o);
                }
                l = o[12];
                if (__all(l <= 1048576.f)) break;
            }
        }
        STAMP(15);
        const float il = 1.f / l;
        const int T16 = 8 * ai + 2 * qt + (l31 >> 4);
        const u32x4 om = u32x4{pk(o[0] * il, o[1] * il), pk(o[2] * il, o[3] * il), pk(o[4] * il, o[5] * il), pk(o[6] * il, o[7] * il)};
        __builtin_amdgcn_raw_buffer_store_b128(om, wsr, WS_OM + ((T16 * 16 + gp) * 64 + (2 * hd + hh) * 16 + (l31 & 15)) * 16, 0, 16);
        const unsigned ra = pk(o[8] * il, o[9] * il), rb2 = pk(o[10] * il, o[11] * il), rv = hd ? rb2 : ra;
        const u32x4 rq = u32x4{rv, (unsigned)__builtin_amdgcn_mov_dpp(rv, 0x55, 0xF, 0xF, true), (unsigned)__builtin_amdgcn_mov_dpp(rv, 0xAA, 0xF, 0xF, true), (unsigned)__builtin_amdgcn_mov_dpp(rv, 0xFF, 0xF, 0xF, true)};
        if (hh == 0 && (l31 & 3) == 0) __builtin_amdgcn_raw_buffer_store_b128(rq, wsr, WS_OR + ((((4 * ai + qt) * 16 + gp) * 2 + hd) * 32 + l31) * 4, 0, 16);
    }
    WAITVM(0);
    if (arrive(L0 + SY_ODR, 8, lane)) __hip_atomic_store((GAS u64*)(ws + WS_OFLAG) + ai * 16 + gp, nonce, RLX);
    STAMP(20);
    if (w < 4 || w == 7) {
        poll_idx((const GAS u64*)(ws + WS_YFLAG), lane < 36 ? 36 * bc + lane : -1, nonce);
        if (w < 4 || lane < 32) {
            const int chB = 72 * bc + 2 * cp, t0 = 64 * br + 8 * oct;
            const u32x4 y0 = __builtin_amdgcn_raw_buffer_load_b128(wsr, WS_Y + chB * 4096 + t0 * 2, 0, 16);
            const u32x4 y1 = __builtin_amdgcn_raw_buffer_load_b128(wsr, WS_Y + (chB + 1) * 4096 + t0 * 2, 0, 16);
            const float D0 = blo(dv), D1 = bhi(dv);
            unsigned zz[8];
#pragma unroll
            for (int t = 0; t < 8; ++t) {
                const int slot = (t & 1) * 4 + (t >> 1);
                const float ya = (slot & 1) ? bhi(y0[slot >> 1]) : blo(y0[slot >> 1]);
                const float yb = (slot & 1) ? bhi(y1[slot >> 1]) : blo(y1[slot >> 1]);
                const unsigned kd = pk(blo(kvp[t]) * D0, bhi(kvp[t]) * D1);
                zz[t] = pk(ya + blo(kd), yb + bhi(kd));
            }
            spin4(L0 + SY_QRD, 1); spin4(L0 + SY_QRD + 16, 1);
#pragma unroll
            for (int t = 0; t < 8; ++t) st32(L0 + L_Z + oct * 1184 + t * 144 + cp * 4, zz[t]);
        }
    } else if (w == 4) {
        poll_idx((const GAS u64*)(ws + WS_OFLAG), lane < 16 ? (br >> 1) * 16 + lane : -1, nonce);
    }
    STAMP(21);
    BAR();
    STAMP(22);
    {
        const int tt = w & 3, kh = w >> 2, T16 = 4 * br + tt;
        u32x4 of[9];
#pragma unroll
        for (int s = 0; s < 8; ++s) of[s] = __builtin_amdgcn_raw_buffer_load_b128(wsr, WS_OM + ((T16 * 16 + 8 * kh + s) * 64 + lane) * 16, 0, 16);
#pragma unroll
        for (int d = 0; d < 4; ++d) of[8][d] = __builtin_amdgcn_raw_buffer_load_b32(wsr, WS_OR + ((((T16 >> 1) * 16 + 8 * kh + 2 * g4 + (d >> 1)) * 2 + (d & 1)) * 32 + 16 * (T16 & 1) + l15) * 4, 0, 16);
        {
            auto gate = [&](const u32x4& qv, int tk, int gr) {
                const u32x4 z = lds128(L0 + L_Z + tk * 144 + (tk >> 3) * 32 + gr * 16);
                u32x4 r;
#pragma unroll
                for (int d = 0; d < 4; ++d) r[d] = pk(blo(qv[d]) * blo(z[d]), bhi(qv[d]) * bhi(z[d]));
                *(u32x4*)((char*)out + (size_t)(64 * br + tk) * 2304 + 1152 + bc * 144 + gr * 16) = r;
            };
            gate(qg0, tk0, gr0);
            if (w == 5) gate(qg1, tk1, gr1);
        }
        f32x4 acc[5];
#pragma unroll
        for (int j = 0; j < 5; ++j) acc[j] = f32x4{0, 0, 0, 0};
        const int q4 = (lane >> 2) & 3, p4 = lane & 3, hd = g4 >> 1, hq = g4 & 1;
        const unsigned mlane = L0 + (20 * hd + 4 * hq + q4) * 144 + p4 * 8;
        const int gr = 8 * kh + 2 * g4;
        const unsigned rlane = L0 + (gr < 6 ? L_WO1 + gr * 5184 : L_WO2 + (gr - 6) * 5184) + (16 + q4) * 144;
#pragma unroll
        for (int s = 0; s < 9; ++s) {
            const int gq = 8 * kh + s;
            const unsigned mrow = mlane + (gq < 6 ? L_WO1 + gq * 5184 : L_WO2 + (gq - 6) * 5184);
#pragma unroll
            for (int j = 0; j < 5; ++j) {
                u32x2 a, c;
                if (s < 8) {
                    const unsigned ad = j < 4 ? mrow + j * 32 : mrow - p4 * 8 + 128 + (p4 & 1) * 8;
                    a = ldstr(ad); c = ldstr(ad + 8 * 144);
                } else {
                    const unsigned ad = rlane + (j < 4 ? j * 32 + p4 * 8 : 128 + (p4 & 1) * 8);
                    a = ldstr(ad); c = ldstr(ad + 5184);
                }
                acc[j] = mfma16(u32x4{a[0], a[1], c[0], c[1]}, of[s], acc[j]);
            }
        }
        const unsigned sc = L0 + L_X + tt * 5120 + lane * 16;
        if (kh == 0) { stf4(sc + 2048, acc[2]); stf4(sc + 3072, acc[3]); }
        else { stf4(sc, acc[0]); stf4(sc + 1024, acc[1]); stf4(sc + 4096, acc[4]); }
        BAR();
        char* op = (char*)out + (size_t)(16 * T16 + l15) * 2304 + bc * 144;
        auto st2 = [&](const f32x4& va, const f32x4& vb, int j0) {
            const unsigned a0 = pk(va[0], va[1]), a1 = pk(va[2], va[3]), b0 = pk(vb[0], vb[1]), b1 = pk(vb[2], vb[3]);
            auto s0 = __builtin_amdgcn_permlane16_swap(a0, b0, false, false);
            auto s1 = __builtin_amdgcn_permlane16_swap(a1, b1, false, false);
            return u32x4{s0[0], s1[0], s0[1], s1[1]};
        };
        if (kh == 0) {
            const f32x4 v0 = acc[0] + ldsf4(sc), v1 = acc[1] + ldsf4(sc + 1024), v4 = acc[4] + ldsf4(sc + 4096);
            *(u32x4*)(op + (g4 & 1) * 32 + (g4 >> 1) * 16) = st2(v0, v1, 0);
            const u32x4 r4 = st2(v4, v4, 4);
            if (g4 == 0) *(u32x4*)(op + 128) = r4;
        } else {
            const f32x4 v2 = acc[2] + ldsf4(sc + 2048), v3 = acc[3] + ldsf4(sc + 3072);
            *(u32x4*)(op + 64 + (g4 & 1) * 32 + (g4 >> 1) * 16) = st2(v2, v3, 2);
        }
    }
    STAMP(23);
    if (b == 0 && w == 7) {
        for (int k = 0; k < 4; ++k) poll_idx((const GAS u64*)(ws + WS_KFLAG), 64 * k + lane, nonce);
        if (lane == 0) __hip_atomic_store((GAS u64*)ws, nonce, RLX);
    }
}

extern "C" void kernel_launch(void* const* d_in, const int* in_sizes, int n_in, void* d_out, int out_size, void* d_ws, size_t ws_size, hipStream_t stream) {
    fused_kernel<<<256, 512, 0, stream>>>((const unsigned short*)d_in[0], (const unsigned short*)d_in[1], (const unsigned short*)d_in[2], (const unsigned short*)d_in[3],
                                          (const unsigned short*)d_in[4], (const unsigned short*)d_in[5], (const unsigned short*)d_in[6], (const unsigned short*)d_in[7],
                                          (const unsigned short*)d_in[8], (unsigned short*)d_out, (char*)d_ws);
}
```

```cpp
#include <hip/hip_runtime.h>
#define AMPK 1
#define AMPF 10
#include <stdint.h>

#define GAS __attribute__((address_space(1)))
#define LAS __attribute__((address_space(3)))
#define DEVI __device__ __forceinline__
typedef unsigned long long u64;
typedef __attribute__((ext_vector_type(8))) __bf16 bf16x8;
typedef __attribute__((ext_vector_type(2))) __bf16 bf16x2;
typedef __attribute__((ext_vector_type(2))) float f32x2;
typedef __attribute__((ext_vector_type(4))) float f32x4;
typedef __attribute__((ext_vector_type(16))) float f32x16;
typedef __attribute__((ext_vector_type(4))) unsigned u32x4;
typedef __attribute__((ext_vector_type(2))) unsigned u32x2;
typedef __attribute__((ext_vector_type(4))) short s16x4;

constexpr int WS_ARR = 256;
constexpr int WS_R0 = 32768;
constexpr int WS_QKV = WS_R0 + 2359296;
constexpr int WS_OM = WS_QKV + 7077888;
constexpr int WS_OR = WS_OM + 2097152;
constexpr int WS_Y = WS_OR + 262144;
constexpr int WS_MB = WS_Y + 2359296;
constexpr int MBS = 4352, MB_Q = 0, MB_O = 128, MB_K = 256, MB_Y = 512;
constexpr int WS_END = WS_MB + 256 * MBS;
constexpr int WS_TS = 32 << 20;
constexpr int L_B1K = 131072, L_B1V = 141312;
constexpr int L_B1S = 151552;
constexpr int KCS = 4160, KCH = 4 * KCS;
constexpr int RCS = 4576, RCH = 2 * RCS;
constexpr int L_KB = 32768, L_RB = 131072;
constexpr int L_KA = 66048, L_RA = 0;
constexpr int L_SCR = 114688;
constexpr int BLK = 10752, PCS = 2112;
constexpr int L_KBLK = 0, L_VBLK = 66048, L_QBLK = 66048 + 3 * BLK;
constexpr int L_WO1 = 32768, L_WO2 = 109056;
constexpr int L_ONES = 161280;
constexpr int L_Z = L_QBLK;
constexpr int L_X = 0;
constexpr int L_SYNC = 163584;
constexpr int SY_LAND = L_SYNC, SY_CONS = L_SYNC + 16, SY_PW = L_SYNC + 32, SY_PR = L_SYNC + 48, SY_PREP = L_SYNC + 64, SY_YDR = L_SYNC + 72,
              SY_ODR = L_SYNC + 76, SY_NONCE = L_SYNC + 80, SY_QDR = L_SYNC + 96, SY_QRD = L_SYNC + 112, SY_QSEEN = L_SYNC + 144, SY_KC = L_SYNC + 160;
constexpr int LDS_BYTES = 163840;
constexpr unsigned SPIN_CAP = 1u << 17, LSPIN_CAP = 1u << 20;

#define STAMPW(k) do { } while (0)
#define STAMPC(k) do { } while (0)
#define STAMP(k) do { if ((k) == AMPK && tid == 0) vst32(L0 + L_SYNC + 240, (unsigned)__builtin_amdgcn_s_memrealtime()); } while (0)

DEVI f32x4 mfma16(u32x4 a, u32x4 b, f32x4 c) { return __builtin_amdgcn_mfma_f32_16x16x32_bf16(__builtin_bit_cast(bf16x8, a), __builtin_bit_cast(bf16x8, b), c, 0, 0, 0); }
DEVI f32x16 mfma32(u32x4 a, u32x4 b, f32x16 c) { return __builtin_amdgcn_mfma_f32_32x32x16_bf16(__builtin_bit_cast(bf16x8, a), __builtin_bit_cast(bf16x8, b), c, 0, 0, 0); }
DEVI f32x16 mfma32k8(u32x2 a, u32x2 b, f32x16 c) { return __builtin_amdgcn_mfma_f32_32x32x8bf16_1k(__builtin_bit_cast(s16x4, a), __builtin_bit_cast(s16x4, b), c, 0, 0, 0); }
DEVI unsigned pk(float lo, float hi) { f32x2 v = {lo, hi}; return __builtin_bit_cast(unsigned, __builtin_convertvector(v, bf16x2)); }
DEVI float blo(unsigned v) { return __builtin_bit_cast(float, v << 16); }
DEVI float bhi(unsigned v) { return __builtin_bit_cast(float, v & 0xffff0000u); }
DEVI u32x4 lds128(unsigned a) { return *(LAS const u32x4*)(uintptr_t)a; }
DEVI u32x2 lds64(unsigned a) { return *(LAS const u32x2*)(uintptr_t)a; }
DEVI unsigned lds32(unsigned a) { return *(LAS const unsigned*)(uintptr_t)a; }
DEVI void st128(unsigned a, u32x4 v) { *(LAS u32x4*)(uintptr_t)a = v; }
DEVI void st64(unsigned a, u32x2 v) { *(LAS u32x2*)(uintptr_t)a = v; }
DEVI void st32(unsigned a, unsigned v) { *(LAS unsigned*)(uintptr_t)a = v; }
DEVI f32x4 ldsf4(unsigned a) { return *(LAS const f32x4*)(uintptr_t)a; }
DEVI void stf4(unsigned a, f32x4 v) { *(LAS f32x4*)(uintptr_t)a = v; }
DEVI u32x2 ldstr(unsigned a) { return __builtin_bit_cast(u32x2, __builtin_amdgcn_ds_read_tr16_b64_v4i16((LAS s16x4*)(uintptr_t)a)); }
DEVI u32x4 vlds128(unsigned a) { return *(volatile LAS const u32x4*)(uintptr_t)a; }
DEVI unsigned vlds32(unsigned a) { return *(volatile LAS const unsigned*)(uintptr_t)a; }
DEVI void vst32(unsigned a, unsigned v) { *(volatile LAS unsigned*)(uintptr_t)a = v; }
DEVI void dma16(const void* g, unsigned lds) {
    unsigned sv;
    asm volatile("s_mov_b32 %0, m0\n\ts_mov_b32 m0, %2\n\ts_nop 0\n\tglobal_load_lds_dwordx4 %1, off\n\ts_mov_b32 m0, %0" : "=&s"(sv) : "v"(g), "s"(lds) : "memory");
}
DEVI void dmaS(unsigned voff, const void* sbase, unsigned lds) {
    unsigned sv;
    asm volatile("s_mov_b32 %0, m0\n\ts_mov_b32 m0, %3\n\ts_nop 0\n\tglobal_load_lds_dwordx4 %1, %2\n\ts_mov_b32 m0, %0" : "=&s"(sv) : "v"(voff), "s"(sbase), "s"(lds) : "memory");
}
#define WAITVM(n) asm volatile("s_waitcnt vmcnt(%0)" ::"i"(n) : "memory")
#define BAR() do { asm volatile("s_waitcnt lgkmcnt(0)\n\ts_barrier" ::: "memory"); } while (0)
#define CFENCE() asm volatile("" ::: "memory")
#define SB0() __builtin_amdgcn_sched_barrier(0)
#define RLX __ATOMIC_RELAXED, __HIP_MEMORY_SCOPE_AGENT
DEVI unsigned rfl(unsigned v) { return __builtin_amdgcn_readfirstlane(v); }
DEVI unsigned umin4(u32x4 v) { const unsigned a = v[0] < v[1] ? v[0] : v[1], c = v[2] < v[3] ? v[2] : v[3]; return a < c ? a : c; }

DEVI void spin4(unsigned a, unsigned need) {
#pragma nounroll
    for (unsigned s = 0; s < LSPIN_CAP; ++s) {
        if (rfl(umin4(vlds128(a))) >= need) break;
        __builtin_amdgcn_s_sleep(1);
    }
    CFENCE();
}
DEVI void spin1(unsigned a, unsigned need) {
#pragma nounroll
    for (unsigned s = 0; s < LSPIN_CAP; ++s) {
        if (rfl(vlds32(a)) >= need) break;
        __builtin_amdgcn_s_sleep(1);
    }
    CFENCE();
}
DEVI void signal(unsigned a, unsigned v, int lane) { CFENCE(); if (lane == 0) vst32(a, v); CFENCE(); }
DEVI bool arrive(unsigned a, unsigned n, int lane) {
    CFENCE();
    unsigned old = 0;
    if (lane == 0) old = __hip_atomic_fetch_add((LAS unsigned*)(uintptr_t)a, 1u, __ATOMIC_RELAXED, __HIP_MEMORY_SCOPE_WORKGROUP);
    CFENCE();
    return lane == 0 && old == n - 1;
}
#define GPRIO 3
#define OUTAUX 0
#define POLLS 2
DEVI void poll_idx(const GAS u64* f, int idx, u64 nonce) {
#pragma nounroll
    for (unsigned spins = 0; spins < SPIN_CAP; ++spins) {
        u64 v = nonce;
        if (idx >= 0) v = __hip_atomic_load(f + idx, RLX);
        if (__all(v == nonce)) break;
        __builtin_amdgcn_s_sleep(POLLS);
    }
    __builtin_amdgcn_fence(__ATOMIC_ACQUIRE, "wavefront");
    CFENCE();
}

DEVI int wg_attn(int ai, int gp) { return (30 - 2 * ai + (gp & 1)) * 8 + (gp >> 1); }
DEVI int wg_pair(int p) { return p < 224 ? (p % 28) * 8 + p / 28 : (28 + (((p - 224) >> 1) & 3)) * 8 + ((p - 224) >> 3); }
DEVI int wg_tile(int br, int bc) { return (4 * bc + (br & 3)) * 8 + (br >> 2); }

__device__ constexpr int CMU[36] = {0,0,0,0,1,1,1,1,2,2,2,0,3,3,3,2,4,4,1,0,5,5,4,3,6,2,1,0,7,6,5,4,3,2,1,0};
__device__ constexpr int CNU[36] = {7,6,5,4,6,5,4,3,5,4,3,3,4,3,2,2,3,2,2,2,2,1,1,1,1,1,1,1,0,0,0,0,0,0,0,0};

__global__ __launch_bounds__(512, 2) void fused_kernel(const unsigned short* __restrict__ x, const unsigned short* __restrict__ wqkv, const unsigned short* __restrict__ wo,
                                                       const unsigned short* __restrict__ bq, const unsigned short* __restrict__ bk, const unsigned short* __restrict__ bv,
                                                       const unsigned short* __restrict__ shk, const unsigned short* __restrict__ ssk, const unsigned short* __restrict__ ssd,
                                                       unsigned short* __restrict__ out, char* __restrict__ ws) {
    __shared__ __attribute__((aligned(256))) char smem[LDS_BYTES];
    const unsigned L0 = (unsigned)(uintptr_t)(LAS char*)smem;
    const int tid = threadIdx.x, lane = tid & 63, w = __builtin_amdgcn_readfirstlane(tid >> 6);
    const int b = blockIdx.x, xcd = b & 7, jj = b >> 3;
    const bool busy = jj < 28;
    const int ri = 4 * (xcd >> 1) + (jj & 3), cj = 7 * (xcd & 1) + (jj >> 2);
    const int br = 4 * xcd + (jj & 3), bc = jj >> 2;
    const int ai = (31 - jj) >> 1, gp = 2 * xcd + (jj & 1);
    const int pairB = busy ? xcd * 28 + jj : 225 + 2 * (xcd * 4 + jj - 28);
    const int l15 = lane & 15, g4 = lane >> 4, l31 = lane & 31, hh = lane >> 5;
    const __amdgpu_buffer_rsrc_t wsr = __builtin_amdgcn_make_buffer_rsrc(ws, 0, WS_END, 0x00020000);
    const __amdgpu_buffer_rsrc_t outr = __builtin_amdgcn_make_buffer_rsrc(out, 0, 2048 * 2304, 0x00020000);
    const unsigned amp0 = (unsigned)__builtin_amdgcn_s_memrealtime();
    STAMP(0);
    auto mb = [&](int wg, int off) { return (GAS u64*)(ws + WS_MB + wg * MBS + off); };
    auto get_nonce = [&]() {
        const u32x2 v = *(volatile LAS const u32x2*)(uintptr_t)(L0 + SY_NONCE);
        return ((u64)rfl(v[1]) << 32) | rfl(v[0]);
    };
    auto gather = [&](int sw, u64 nonce) {
        spin4(L0 + SY_CONS, 9);
        if (sw == 0) {
            const int j = lane >> 1, rr = j < 6 ? ai - 2 + (j < 3 ? j : j - 3) : ai;
            poll_idx(mb(b, MB_Q), (lane < 14 && rr >= 0) ? lane : -1, nonce);
            signal(L0 + SY_QSEEN, 1, lane);
        } else {
            spin1(L0 + SY_QSEEN, 1);
        }
        STAMPW(24 + sw);
        u32x4 g[18];
        const int p0 = (36 * gp - 4 * (gp & 1)) >> 3;
#pragma unroll
        for (int k = 0; k < 18; ++k) {
            const int u = sw + 4 * k, j = u / 10, e = u - 10 * j;
            const int sec = j < 3 ? 1 : (j < 6 ? 2 : 0), rr = j < 6 ? ai - 2 + (j < 3 ? j : j - 3) : ai;
            if (u < 70 && rr >= 0) g[k] = __builtin_amdgcn_raw_buffer_load_b128(wsr, lane * 16, WS_QKV + ((rr * 216 + 72 * sec + p0) * 128) * 16 + e * 1024, 16);
        }
#pragma unroll
        for (int k = 0; k < 18; ++k) {
            const int u = sw + 4 * k, j = u / 10, e = u - 10 * j;
            const int rr = j < 6 ? ai - 2 + (j < 3 ? j : j - 3) : ai;
            const unsigned dst = L0 + (j < 3 ? L_KBLK + j * BLK : L_VBLK + (j - 3) * BLK) + (e >> 1) * PCS + (e & 1) * 1024 + lane * 16;
            if (u < 70 && rr >= 0) st128(dst, g[k]);
        }
    };

    if (w < 4) {
        if (w == 0) {
            const u64 ep = __hip_atomic_load((const GAS u64*)ws, RLX);
            if (lane < 20 || (lane >= 24 && lane < 44)) vst32(L0 + L_SYNC + lane * 4, lane == 3 ? 0xffffffffu : 0u);
            const u64 nn = ep + 1;
            if (lane == 0) *(volatile LAS u32x2*)(uintptr_t)(L0 + SY_NONCE) = u32x2{(unsigned)nn, (unsigned)(nn >> 32)};
        }
        if (w == 1) {
#pragma unroll
            for (int i = 0; i < 3; ++i) { const int q = i * 64 + lane; if (q < 140) st128(L0 + L_ONES + q * 16, u32x4{0x3f803f80u, 0x3f803f80u, 0x3f803f80u, 0x3f803f80u}); }
        }
        BAR();
        STAMP(1);
        u32x4 kd0[2], kd1[2];
        auto kload = [&](int pair) {
#pragma unroll
            for (int r = 0; r < 2; ++r) {
                const int cq = (2 * (w & 1) + r) * 64 + lane;
                const char* p = (const char*)ssk + (2 * pair + (w >> 1)) * 4096 + cq * 16;
                kd0[r] = *(const u32x4*)p; kd1[r] = *(const u32x4*)(p + (cq < 255 ? 16 : 0));
            }
        };
        auto kcopy = [&](unsigned kb0) {
            const unsigned kb = kb0 + (w >> 1) * KCH;
#pragma unroll
            for (int r = 0; r < 2; ++r) {
                const unsigned a = kb + (4 + (2 * (w & 1) + r) * 64 + lane) * 16;
                const u32x4 d0 = kd0[r], d1 = kd1[r];
                st128(a, d0);
                st128(a + KCS, u32x4{d0[1], d0[2], d0[3], d1[0]});
                st128(a + 2 * KCS, u32x4{d0[2], d0[3], d1[0], d1[1]});
                st128(a + 3 * KCS, u32x4{d0[3], d1[0], d1[1], d1[2]});
            }
            if ((w & 1) == 0) {
                const u32x4 d0 = kd0[0];
                if (lane == 0) {
                    st128(kb + 3 * 16, u32x4{0, 0, 0, 0});
                    st128(kb + KCS + 3 * 16, u32x4{0, 0, 0, d0[0]});
                    st128(kb + 2 * KCS + 3 * 16, u32x4{0, 0, d0[0], d0[1]});
                    st128(kb + 3 * KCS + 3 * 16, u32x4{0, d0[0], d0[1], d0[2]});
                } else if (lane < 13) {
                    const int e = lane - 1;
                    st128(kb + (e / 3) * KCS + (e % 3) * 16, u32x4{0, 0, 0, 0});
                }
            }
        };
        if (!busy) { kload(pairB - 1); kcopy(L0 + L_KA); }
        kload(pairB);
        if (busy) {
            const int wt = w & 1, wc = w >> 1;
            const bool live = !(cj == 13 && wc == 1);
            if (live) {
                unsigned xo[4], wof[2];
#pragma unroll
                for (int j = 0; j < 4; ++j) xo[j] = L0 + (64 * wt + l31) * 128 + (((2 * j + hh) ^ ((l31 >> 1) & 7)) * 16);
                {
                    const int q = (lane >> 2) & 3, p = lane & 3;
#pragma unroll
                    for (int ct = 0; ct < 2; ++ct) wof[ct] = L0 + 16384 + (8 * hh + q) * 256 + (((8 * wc + 4 * ct + 2 * (g4 & 1) + (p >> 1)) ^ (q << 2)) * 16) + (p & 1) * 8;
                }
                f32x16 acc[2][2];
#pragma unroll
                for (int r = 0; r < 16; ++r) { acc[0][0][r] = 0.f; acc[0][1][r] = 0.f; acc[1][0][r] = 0.f; acc[1][1][r] = 0.f; }
                u32x4 xf[2][2];
                u32x2 wa[2][2], wc2[2][2];
                auto ldx = [&](int s, int j, int pb, int tt) { xf[pb][tt] = lds128(xo[j] + (s & 3) * 32768 + tt * 4096); };
                auto ldwa = [&](int s, int j, int pb, int ct) { wa[pb][ct] = ldstr(wof[ct] + (s & 3) * 32768 + j * 4096); };
                auto ldwc = [&](int s, int j, int pb, int ct) { wc2[pb][ct] = ldstr(wof[ct] + (s & 3) * 32768 + j * 4096 + 1024); };
                auto W = [&](int pb, int ct) { return u32x4{wa[pb][ct][0], wa[pb][ct][1], wc2[pb][ct][0], wc2[pb][ct][1]}; };
                spin4(L0 + SY_LAND, 1);
                STAMP(3); STAMPC(40);
                ldx(0, 0, 0, 0); ldwa(0, 0, 0, 0); ldwc(0, 0, 0, 0); ldx(0, 0, 0, 1); ldwa(0, 0, 0, 1); ldwc(0, 0, 0, 1);
#pragma unroll
                for (int s = 0; s < 9; ++s) {
                    u32x4 landv;
#pragma unroll
                    for (int j = 0; j < 4; ++j) {
                        const int pb = j & 1, nb = pb ^ 1;
                        const bool more = j < 3 || s < 8;
                        const int ns = j < 3 ? s : s + 1, nj = (j + 1) & 3;
                        SB0();
                        acc[0][0] = mfma32(W(pb, 0), xf[pb][0], acc[0][0]);
                        SB0();
                        if (j == 3 && s < 8) { if (rfl(umin4(landv)) < (unsigned)(s + 2)) spin4(L0 + SY_LAND, s + 2); CFENCE(); }
                        if (more) { ldx(ns, nj, nb, 0); ldwa(ns, nj, nb, 0); ldwc(ns, nj, nb, 0); }
                        SB0();
                        acc[0][1] = mfma32(W(pb, 0), xf[pb][1], acc[0][1]);
                        SB0();
                        if (more) { ldx(ns, nj, nb, 1); ldwa(ns, nj, nb, 1); }
                        SB0();
                        acc[1][0] = mfma32(W(pb, 1), xf[pb][0], acc[1][0]);
                        SB0();
                        if (more) { ldwc(ns, nj, nb, 1); }
                        if (j == 2) {
                            signal(L0 + SY_CONS + w * 4, s + 1, lane);
                        }
                        if (j == 1 && s < 8) landv = vlds128(L0 + SY_LAND);
                        SB0();
                        acc[1][1] = mfma32(W(pb, 1), xf[pb][1], acc[1][1]);
                    }
                }
                STAMP(4); STAMPC(41);
#pragma unroll
                for (int ct = 0; ct < 2; ++ct) {
                    const int n0 = 128 * cj + 64 * wc + 32 * ct;
                    const float sc = n0 < 576 ? 0.34004634f : 1.0f;
#pragma unroll
                    for (int tt = 0; tt < 2; ++tt) {
#pragma unroll
                        for (int pi = 0; pi < 2; ++pi) {
                            const f32x16& a = acc[ct][tt];
                            const unsigned a0 = pk(a[8 * pi] * sc, a[8 * pi + 1] * sc), a1 = pk(a[8 * pi + 2] * sc, a[8 * pi + 3] * sc);
                            const unsigned b0 = pk(a[8 * pi + 4] * sc, a[8 * pi + 5] * sc), b1 = pk(a[8 * pi + 6] * sc, a[8 * pi + 7] * sc);
                            auto s0 = __builtin_amdgcn_permlane32_swap(a0, b0, false, false);
                            auto s1 = __builtin_amdgcn_permlane32_swap(a1, b1, false, false);
                            const int off = WS_QKV + ((ri * 216 + (n0 >> 3) + 2 * pi + hh) * 128 + 64 * wt + 32 * tt + l31) * 16;
                            __builtin_amdgcn_raw_buffer_store_b128(u32x4{s0[0], s1[0], s0[1], s1[1]}, wsr, off, 0, 16);
                        }
                    }
                }
            } else {
                signal(L0 + SY_CONS + w * 4, 9, lane);
            }
            STAMP(5);
            spin4(L0 + SY_CONS, 7);
        }
        kcopy(L0 + L_KB);
        signal(L0 + SY_KC + w * 4, 1, lane);
        bool qdone = !busy;
        auto qdrain = [&]() {
            WAITVM(0);
            signal(L0 + SY_QDR + w * 4, 1, lane);
            STAMP(8);
        };
        spin1(L0 + SY_KC + (w ^ 1) * 4, 1);
        for (int pass = busy ? 1 : 0; pass < 2; ++pass) {
            if (!qdone && rfl(vlds32(L0 + SY_PREP)) < (unsigned)(pass + 1)) { qdrain(); qdone = true; }
            spin1(L0 + SY_PREP, pass + 1);
            STAMP(6 + 3 * pass);
            const int slot = w >> 1, h = w & 1;
            const unsigned kb = L0 + (pass ? L_KB : L_KA) + slot * KCH, rb = L0 + (pass ? L_RB : L_RA) + slot * RCH;
            const int ch = 2 * (pairB - 1 + pass) + slot;
            const unsigned la = kb + ((l15 + 1) & 3) * KCS + (g4 + ((l15 + 1) >> 2)) * 16 + h * 256;
            const unsigned lb = rb + ((l15 & 1) ? 0 : RCS) + (28 - 4 * (l15 >> 1) + g4) * 16 + h * 256;
            f32x4 acc[8];
#pragma unroll
            for (int n = 0; n < 8; ++n) acc[n] = f32x4{0, 0, 0, 0};
            u32x4 A[2][8], B[2][8];
#pragma unroll
            for (int m = 0; m < 8; ++m) { A[0][m] = lds128(la + m * 512); B[0][m] = lds128(lb + (7 - m) * 512); }
#pragma unroll
            for (int rr = 0; rr < 4; ++rr) {
                const int cur = rr & 1, nx = cur ^ 1;
#pragma unroll
                for (int i = 0; i < 36; ++i) {
                    SB0();
                    acc[CMU[i] + CNU[i]] = mfma16(A[cur][CMU[i]], B[cur][CNU[i]], acc[CMU[i] + CNU[i]]);
                    SB0();
                    if (rr < 3 && (i & 1) == 0 && i < 32) {
                        const int m = i >> 2;
                        if (i & 2) B[nx][7 - m] = lds128(lb + (rr + 1) * 64 + m * 512);
                        else A[nx][m] = lds128(la + (rr + 1) * 64 + m * 512);
                    }
                }
                if (rr == 1 && !qdone) { qdrain(); qdone = true; }
            }
            if (pass == 0) signal(L0 + SY_CONS + w * 4, 9, lane);
            const unsigned mine = L0 + L_SCR + w * 4096 + lane * 16, theirs = L0 + L_SCR + (w ^ 1) * 4096 + lane * 16;
            if (busy) spin4(L0 + SY_CONS, 9);
            else if (pass == 1) spin1(L0 + SY_PR + (w ^ 1) * 4, 1);
            f32x4 y[4];
            if (h) {
#pragma unroll
                for (int n = 0; n < 4; ++n) stf4(mine + n * 1024, acc[n]);
            } else {
#pragma unroll
                for (int n = 0; n < 4; ++n) stf4(mine + n * 1024, acc[4 + n]);
            }
            signal(L0 + SY_PW + w * 4, pass + 1, lane);
            spin1(L0 + SY_PW + (w ^ 1) * 4, pass + 1);
            if (h) {
#pragma unroll
                for (int n = 0; n < 4; ++n) y[n] = acc[4 + n] + ldsf4(theirs + n * 1024);
            } else {
#pragma unroll
                for (int n = 0; n < 4; ++n) y[n] = acc[n] + ldsf4(theirs + n * 1024);
            }
            signal(L0 + SY_PR + w * 4, pass + 1, lane);
            const bool odd = lane & 1;
#pragma unroll
            for (int i = 0; i < 4; i += 2) {
                const unsigned a0 = pk(y[i][0], y[i][1]), a1 = pk(y[i][2], y[i][3]), c0 = pk(y[i + 1][0], y[i + 1][1]), c1 = pk(y[i + 1][2], y[i + 1][3]);
                const unsigned r0 = __builtin_amdgcn_mov_dpp(odd ? a0 : c0, 0xB1, 0xF, 0xF, true), r1 = __builtin_amdgcn_mov_dpp(odd ? a1 : c1, 0xB1, 0xF, 0xF, true);
                const u32x4 v = odd ? u32x4{r0, r1, c0, c1} : u32x4{a0, a1, r0, r1};
                __builtin_amdgcn_raw_buffer_store_b128(v, wsr, WS_Y + ch * 4096 + (4 * h + i + (odd ? 1 : 0)) * 512 + ((l15 >> 1) * 4 + g4) * 16, 0, 16);
            }
            STAMP(7 + 3 * pass);
        }
    } else if (w < 7) {
        const int d = w - 4;
        if (busy) {
            const unsigned xv0 = (lane >> 3) * 1152 + (((lane & 7) ^ (lane >> 4)) * 16), xv1 = (lane >> 3) * 1152 + (((lane & 7) ^ (4 + (lane >> 4))) * 16);
            int wch = (lane & 15) ^ ((lane >> 4) << 2);
            if (cj == 13) wch &= 7;
            const unsigned wv = (lane >> 4) * 3456 + wch * 16;
            const char* xb = (const char*)x + (size_t)(128 * ri) * 1152;
            const char* wb = (const char*)wqkv + cj * 256;
            auto issue = [&](int s, int i0, int i1) {
                const unsigned sl = L0 + (s & 3) * 32768;
#pragma unroll
                for (int i = i0; i < i1; ++i) {
                    int u = d + 3 * i; u = u < 32 ? u : d;
                    if (u < 16) dmaS((u & 1) ? xv1 : xv0, xb + s * 128 + u * 9216, sl + u * 1024);
                    else dmaS(wv, wb + (size_t)(s * 64 + (u - 16) * 4) * 3456, sl + u * 1024);
                }
            };
            const unsigned land = L0 + SY_LAND + d * 4;
            issue(0, 0, 11); issue(1, 0, 11);
            BAR();
            issue(2, 0, 11);
            WAITVM(22); signal(land, 1, lane);
            issue(3, 0, 11);
            WAITVM(22); signal(land, 2, lane);
#pragma unroll
            for (int s = 0; s < 5; ++s) {
                spin4(L0 + SY_CONS, s + 1);
                issue(s + 4, 0, 6);
                WAITVM(17); signal(land, s + 3, lane);
                issue(s + 4, 6, 11);
            }
            WAITVM(11); signal(land, 8, lane);
            WAITVM(0); signal(land, 9, lane);
            if (d == 1) {
                const int i = lane / 6, r = lane - 6 * i, e = r & 1, dd = r >> 1;
                const int G = (cj == 0 ? 0 : (128 * cj - 39) / 36) + i, c = 36 * G - 4 * (G & 1) + 39 * e;
                const int sec = G >> 4, a2 = ri + dd;
                const bool ok = i < 6 && G < 48 && (c >> 7) == cj && (sec == 0 ? dd == 0 : a2 < 16);
                const int slot = sec == 0 ? 12 + e : 2 * (2 - dd + (sec == 2 ? 3 : 0)) + e;
                GAS u64* f = mb(wg_attn(a2, G & 15), MB_Q + slot * 8);
                const u64 nonce = get_nonce();
                spin4(L0 + SY_QDR, 1);
                if (ok) __hip_atomic_store(f, nonce, RLX);
                STAMPW(2);
            }
        } else {
            BAR();
        }
        gather(d, get_nonce());
    } else {
        for (int u = 0; u < 20; ++u) {
            const bool isk = u < 10;
            const int q = (isk ? u : u - 10) * 64 + lane;
            const int g = q / 74; int r = q - g * 74; r = r < 72 ? r : 71;
            int row = 8 * g + r / 9; const int cc = r - (r / 9) * 9;
            if (isk) {
                row = row < 68 ? row : 67;
                int tok = 64 * br - 4 + row; tok = tok < 0 ? 0 : tok;
                dma16((const char*)bk + tok * 1152 + bc * 144 + cc * 16, L0 + L_B1K + u * 1024);
            } else {
                row = row < 64 ? row : 63;
                dma16((const char*)bv + (64 * br + row) * 1152 + bc * 144 + cc * 16, L0 + L_B1V + (u - 10) * 1024);
            }
        }
        dma16(lane < 36 ? (const char*)shk + bc * 576 + lane * 16 : (const char*)shk, L0 + L_B1S);
        BAR();
        __builtin_amdgcn_s_setprio(GPRIO);
        WAITVM(0);
        STAMPW(28);
#pragma unroll
        for (int it = 0; it < 5; ++it) {
            const int bl = 64 * it + lane, cp = bl >> 3, oct = bl & 7;
            if (it < 4 || lane < 32) {
                const u32x4 shv = lds128(L0 + L_B1S + cp * 16);
                unsigned vv[8], kvp[8];
#pragma unroll
                for (int t = 0; t < 8; ++t) vv[t] = lds32(L0 + L_B1V + oct * 1184 + t * 144 + cp * 4);
                f32x2 k[11];
#pragma unroll
                for (int q = 0; q < 11; ++q) {
                    const unsigned v = lds32(L0 + L_B1K + oct * 1184 + (1 + q) * 144 + ((1 + q) >> 3) * 32 + cp * 4);
                    k[q] = f32x2{blo(v), bhi(v)};
                }
                if (br == 0 && oct == 0) { k[0] = f32x2{0.f, 0.f}; k[1] = k[0]; k[2] = k[0]; }
                const f32x2 s0 = {blo(shv[0]), blo(shv[2])}, s1 = {bhi(shv[0]), bhi(shv[2])}, s2 = {blo(shv[1]), blo(shv[3])}, s3 = {bhi(shv[1]), bhi(shv[3])};
#pragma unroll
                for (int t = 0; t < 8; ++t) {
                    const unsigned v = vv[t];
                    f32x2 a = k[t] * s3;
                    a = __builtin_elementwise_fma(k[t + 1], s2, a);
                    a = __builtin_elementwise_fma(k[t + 2], s1, a);
                    a = __builtin_elementwise_fma(k[t + 3], s0, a);
                    const unsigned ks = pk(a[0], a[1]);
                    kvp[t] = pk(blo(ks) * blo(v), bhi(ks) * bhi(v));
                }
                u32x4 r0, r1;
#pragma unroll
                for (int dd = 0; dd < 4; ++dd) {
                    r0[dd] = (kvp[7 - 2 * dd] & 0xffffu) | (kvp[6 - 2 * dd] << 16);
                    r1[dd] = (kvp[7 - 2 * dd] >> 16) | (kvp[6 - 2 * dd] & 0xffff0000u);
                }
                const int off = WS_R0 + (72 * bc + 2 * cp) * 4096 + (2040 - 64 * br - 8 * oct) * 2;
                __builtin_amdgcn_raw_buffer_store_b128(r0, wsr, off, 0, 16);
                __builtin_amdgcn_raw_buffer_store_b128(r1, wsr, off + 4096, 0, 16);
            }
        }
        STAMPW(29);
        const u64 nonce = get_nonce();
        GAS u64* kf = lane < 36 ? mb(wg_pair(36 * bc + lane), MB_K + br * 8) : (GAS u64*)(ws + WS_ARR) + b;
        WAITVM(0);
        if (lane < 37) __hip_atomic_store(kf, nonce, RLX);
        STAMPW(11);
        u32x4 rd[2][4]; unsigned rn[2][4];
        auto rload = [&](int pair) {
#pragma unroll
            for (int c = 0; c < 2; ++c)
#pragma unroll
                for (int r = 0; r < 4; ++r) {
                    const int cq = r * 64 + lane, off = WS_R0 + (2 * pair + c) * 4096 + cq * 16;
                    rd[c][r] = __builtin_amdgcn_raw_buffer_load_b128(wsr, off, 0, 16);
                    rn[c][r] = __builtin_amdgcn_raw_buffer_load_b32(wsr, off + (cq < 255 ? 16 : 0), 0, 16);
                }
        };
        auto rcopy = [&](unsigned rb0) {
#pragma unroll
            for (int c = 0; c < 2; ++c) {
                const unsigned rb = rb0 + c * RCH;
#pragma unroll
                for (int r = 0; r < 4; ++r) {
                    const int cq = r * 64 + lane;
                    const u32x4 dd = rd[c][r]; const unsigned nx = cq < 255 ? rn[c][r] : 0u;
                    st128(rb + cq * 16, dd);
                    st128(rb + RCS + cq * 16, u32x4{__builtin_amdgcn_alignbit(dd[1], dd[0], 16), __builtin_amdgcn_alignbit(dd[2], dd[1], 16), __builtin_amdgcn_alignbit(dd[3], dd[2], 16), __builtin_amdgcn_alignbit(nx, dd[3], 16)});
                }
                if (lane < 60) st128(rb + (lane >= 30 ? RCS : 0) + (256 + (lane >= 30 ? lane - 30 : lane)) * 16, u32x4{0, 0, 0, 0});
            }
        };
        poll_idx(mb(b, MB_K), lane < 32 ? lane : -1, nonce);
        STAMPW(12);
        if (!busy) { rload(pairB - 1); rcopy(L0 + L_RA); signal(L0 + SY_PREP, 1, lane); }
        rload(pairB);
        rcopy(L0 + L_RB);
        signal(L0 + SY_PREP, 2, lane);
        __builtin_amdgcn_s_setprio(0);
        STAMPW(13);
        gather(3, nonce);
    }
    STAMPW(16 + w);
    BAR();
    STAMP(14);
    const u64 nonce = get_nonce();
    const unsigned wov = (lane / 9) * 1152 + (lane % 9) * 16;
    auto wo_dma = [&](int k) {
        const int u = w + 8 * k;
        if (u < 83) {
            const int row0 = u < 31 ? 7 * u : 216 + 7 * (u - 31);
            const unsigned dst = L0 + (u < 31 ? L_WO1 + u * 1008 : L_WO2 + (u - 31) * 1008);
            if (u < 82 || lane < 27) dmaS(wov, (const char*)wo + row0 * 1152 + bc * 144, dst);
        }
    };
    auto ypublish = [&]() {
        if (w < 4) {
            const int p = pairB - (busy ? 0 : hh), pc = p / 36;
            GAS u64* f = mb(wg_tile(l31, pc), MB_Y + (p - 36 * pc) * 8);
            WAITVM(0);
            const bool last = __builtin_amdgcn_readfirstlane(arrive(L0 + SY_YDR, 4, lane) ? 1 : 0);
            if (last && (lane < 32 || !busy)) __hip_atomic_store(f, nonce, RLX);
        }
    };
    const int id0 = tid, tk0 = id0 / 9, gr0 = id0 - 9 * tk0, id1 = 512 + lane, tk1 = id1 / 9, gr1 = id1 - 9 * tk1;
    u32x4 qg0, qg1 = u32x4{0, 0, 0, 0};
    const int bl = w < 4 ? tid : 256 + l31, cp = bl >> 3, oct = bl & 7;
    u32x4 kv0 = u32x4{0, 0, 0, 0}, kv1 = u32x4{0, 0, 0, 0};
    unsigned dv = 0;
    auto q_prefetch = [&]() {
        qg0 = *(const u32x4*)((const char*)bq + (64 * br + tk0) * 1152 + bc * 144 + gr0 * 16);
        if (w == 5) qg1 = *(const u32x4*)((const char*)bq + (64 * br + tk1) * 1152 + bc * 144 + gr1 * 16);
        if (w < 4 || w == 7) {
            const int off = WS_R0 + (72 * bc + 2 * cp) * 4096 + (2040 - 64 * br - 8 * oct) * 2;
            kv0 = __builtin_amdgcn_raw_buffer_load_b128(wsr, off, 0, 16);
            kv1 = __builtin_amdgcn_raw_buffer_load_b128(wsr, off + 4096, 0, 16);
            dv = *(const unsigned*)(ssd + 72 * bc + 2 * cp);
        }
    };
    {
        const int hd = w >> 2, qt = w & 3, cs = 4 * (gp & 1);
        u32x4 qm = lds128(L0 + L_QBLK + (2 * hd + hh) * PCS + (32 * qt + l31) * 16);
        u32x2 qr = lds64(L0 + L_QBLK + (2 * hd + 2) * PCS + (32 * qt + l31) * 16 + hh * 8);
        signal(L0 + SY_QRD + w * 4, 1, lane);
        {
            const int lo = cs + 18 * hd;
#pragma unroll
            for (int d = 0; d < 4; ++d) { const int c = 8 * (2 * hd + hh) + 2 * d; qm[d] = (c >= lo && c < lo + 18) ? qm[d] : 0u; }
#pragma unroll
            for (int d = 0; d < 2; ++d) { const int c = 8 * (2 * hd + 2) + 4 * hh + 2 * d; qr[d] = (c >= lo && c < lo + 18) ? qr[d] : 0u; }
        }
        const unsigned kA = L0 + L_KBLK + (2 * hd + hh) * PCS + l31 * 16, kB = L0 + L_KBLK + (2 * hd + 2) * PCS + l31 * 16 + hh * 8;
        unsigned vA, vsel;
        {
            const int q = (lane >> 2) & 3, p = lane & 3;
            const bool ones = (g4 & 1) && p > 0;
            const int c = (g4 & 1) ? cs + 16 : cs + (hd ? 20 : 0) + 4 * p;
            vA = ones ? L0 + L_ONES + 64 * ((hd ? 1 : 3) + hh) : L0 + L_VBLK + (c >> 3) * PCS + (4 * hh + q) * 16 + ((c >> 2) & 1) * 8;
            vsel = ones ? 0u : 0xffffffffu;
        }
        const int qq = l31 - 4 * hh;
        const int t_first = ai >= 2 ? qt : (ai == 1 ? 4 : 8), t_last = 8 + qt;
        const bool has_first = ai >= 2;
        auto toff = [&](int t) { return (unsigned)((t >> 2) * BLK + (t & 3) * 512); };
        auto qk = [&](int t, const f32x16& c) {
            const unsigned o = toff(t);
            const u32x4 km = lds128(kA + o);
            const u32x2 kr = lds64(kB + o);
            return mfma32k8(kr, qr, mfma32(km, qm, c));
        };
        auto pv = [&](int t, const f32x16& p, const f32x16& o) {
            const unsigned va = vA + (toff(t) & vsel);
            const u32x2 t0 = ldstr(va), t1 = ldstr(va + 128), t2 = ldstr(va + 256), t3 = ldstr(va + 384);
            const u32x4 p0 = u32x4{pk(p[0], p[1]), pk(p[2], p[3]), pk(p[4], p[5]), pk(p[6], p[7])};
            const u32x4 p1 = u32x4{pk(p[8], p[9]), pk(p[10], p[11]), pk(p[12], p[13]), pk(p[14], p[15])};
            return mfma32(u32x4{t2[0], t2[1], t3[0], t3[1]}, p1, mfma32(u32x4{t0[0], t0[1], t1[0], t1[1]}, p0, o));
        };
        const float NINF = -__builtin_inff();
        f32x16 z16;
#pragma unroll
        for (int r = 0; r < 16; ++r) z16[r] = 0.f;
        f32x16 o, sd;
        float l, m;
        bool redo;
        if (ai >= 2) {
            u32x4 km[2]; u32x2 kr[2], vt[2][4];
            auto ldK = [&](int bf, int t) { const unsigned oo = toff(t); km[bf] = lds128(kA + oo); kr[bf] = lds64(kB + oo); };
            auto ldV = [&](int bf, int t) {
                const unsigned va = vA + (toff(t) & vsel);
                vt[bf][0] = ldstr(va); vt[bf][1] = ldstr(va + 128); vt[bf][2] = ldstr(va + 256); vt[bf][3] = ldstr(va + 384);
            };
            auto ex8 = [&](const f32x16& s, int h8) {
                return u32x4{pk(__builtin_amdgcn_exp2f(s[h8]), __builtin_amdgcn_exp2f(s[h8 + 1])), pk(__builtin_amdgcn_exp2f(s[h8 + 2]), __builtin_amdgcn_exp2f(s[h8 + 3])),
                             pk(__builtin_amdgcn_exp2f(s[h8 + 4]), __builtin_amdgcn_exp2f(s[h8 + 5])), pk(__builtin_amdgcn_exp2f(s[h8 + 6]), __builtin_amdgcn_exp2f(s[h8 + 7]))};
            };
            ldK(0, t_last); ldK(1, qt); ldV(0, t_last); ldV(1, qt);
            sd = mfma32k8(kr[0], qr, mfma32(km[0], qm, z16));
            SB0();
            ldK(0, qt + 1);
            m = NINF;
#pragma unroll
            for (int r = 0; r < 16; ++r) { const int c = (r & 3) + 8 * (r >> 2); sd[r] = c <= qq ? sd[r] : NINF; m = fmaxf(m, sd[r]); }
            m = fmaxf(m, __shfl_xor(m, 32));
            f32x16 nm, s[2], sm;
#pragma unroll
            for (int r = 0; r < 16; ++r) { nm[r] = -m; sm[r] = sd[r] - m; }
            s[0] = mfma32(km[1], qm, nm);
            SB0();
            {
                const u32x4 p0 = ex8(sm, 0);
                SB0();
                s[0] = mfma32k8(kr[1], qr, s[0]);
                o = mfma32(u32x4{vt[0][0][0], vt[0][0][1], vt[0][1][0], vt[0][1][1]}, p0, z16);
                SB0();
                const u32x4 p1 = ex8(sm, 8);
                SB0();
                o = mfma32(u32x4{vt[0][2][0], vt[0][2][1], vt[0][3][0], vt[0][3][1]}, p1, o);
            }
#pragma unroll
            for (int i = 0; i < 8; ++i) {
                const int kb2 = i & 1, vb = (i + 1) & 1;
                SB0();
                if (i < 7) s[(i + 1) & 1] = mfma32(km[kb2], qm, nm);
                SB0();
                f32x16 sc = s[i & 1];
                if (i == 0) {
#pragma unroll
                    for (int r = 0; r < 16; ++r) { const int c = (r & 3) + 8 * (r >> 2); sc[r] = c > qq ? sc[r] : NINF; }
                }
                const u32x4 p0 = ex8(sc, 0);
                SB0();
                if (i < 7) s[(i + 1) & 1] = mfma32k8(kr[kb2], qr, s[(i + 1) & 1]);
                if (i < 6) ldK(kb2 ^ 1, qt + i + 2);
                if (i < 7) ldV(vb ^ 1, qt + i + 1);
                SB0();
                o = mfma32(u32x4{vt[vb][0][0], vt[vb][0][1], vt[vb][1][0], vt[vb][1][1]}, p0, o);
                SB0();
                const u32x4 p1 = ex8(sc, 8);
                SB0();
                o = mfma32(u32x4{vt[vb][2][0], vt[vb][2][1], vt[vb][3][0], vt[vb][3][1]}, p1, o);
                SB0();
                if (i == 2) { ypublish(); q_prefetch(); }
                if (i >= 3) { wo_dma(2 * (i - 3)); wo_dma(2 * (i - 3) + 1); }
            }
            wo_dma(10);
            l = o[12];
            redo = !__all(l <= 1048576.f);
        } else {
            ypublish(); q_prefetch();
            for (int k = 0; k < 11; ++k) wo_dma(k);
            sd = qk(t_last, z16);
            m = NINF;
#pragma unroll
            for (int r = 0; r < 16; ++r) { const int c = (r & 3) + 8 * (r >> 2); sd[r] = c <= qq ? sd[r] : NINF; m = fmaxf(m, sd[r]); }
            m = fmaxf(m, __shfl_xor(m, 32));
            redo = true;
        }
        if (redo) {
            for (int attempt = ai >= 2 ? 1 : 0; attempt < 2; ++attempt) {
                if (attempt == 1) {
                    for (int t = t_first; t < t_last; ++t) {
                        f32x16 s = qk(t, z16);
#pragma unroll
                        for (int r = 0; r < 16; ++r) { const int c = (r & 3) + 8 * (r >> 2); const bool keep = !(has_first && t == t_first) || c > qq; m = fmaxf(m, keep ? s[r] : NINF); }
                    }
                    m = fmaxf(m, __shfl_xor(m, 32));
                }
                f32x16 nm, p;
#pragma unroll
                for (int r = 0; r < 16; ++r) { nm[r] = -m; p[r] = __builtin_amdgcn_exp2f(sd[r] - m); }
                o = pv(t_last, p, z16);
                for (int t = t_first; t < t_last; ++t) {
                    f32x16 s = qk(t, nm);
                    if (has_first && t == t_first) {
#pragma unroll
                        for (int r = 0; r < 16; ++r) { const int c = (r & 3) + 8 * (r >> 2); s[r] = c > qq ? s[r] : NINF; }
                    }
#pragma unroll
                    for (int r = 0; r < 16; ++r) p[r] = __builtin_amdgcn_exp2f(s[r]);
                    o = pv(t, p, o);
                }
                l = o[12];
                if (__all(l <= 1048576.f)) break;
            }
        }
        STAMP(15);
        const float il = 1.f / l;
        const int T16 = 8 * ai + 2 * qt + (l31 >> 4);
        const u32x4 om = u32x4{pk(o[0] * il, o[1] * il), pk(o[2] * il, o[3] * il), pk(o[4] * il, o[5] * il), pk(o[6] * il, o[7] * il)};
        __builtin_amdgcn_raw_buffer_store_b128(om, wsr, WS_OM + ((T16 * 16 + gp) * 64 + (2 * hd + hh) * 16 + (l31 & 15)) * 16, 0, 16);
        const unsigned ra = pk(o[8] * il, o[9] * il), rb2 = pk(o[10] * il, o[11] * il), rv = hd ? rb2 : ra;
        const u32x4 rq = u32x4{rv, (unsigned)__builtin_amdgcn_mov_dpp(rv, 0x55, 0xF, 0xF, true), (unsigned)__builtin_amdgcn_mov_dpp(rv, 0xAA, 0xF, 0xF, true), (unsigned)__builtin_amdgcn_mov_dpp(rv, 0xFF, 0xF, 0xF, true)};
        if (hh == 0 && (l31 & 3) == 0) __builtin_amdgcn_raw_buffer_store_b128(rq, wsr, WS_OR + ((((4 * ai + qt) * 16 + gp) * 2 + hd) * 32 + l31) * 4, 0, 16);
    }
    {
        GAS u64* f = mb(wg_tile(2 * ai + (lane & 1), (lane >> 1) & 7), MB_O + gp * 8);
        WAITVM(0);
        const bool last = __builtin_amdgcn_readfirstlane(arrive(L0 + SY_ODR, 8, lane) ? 1 : 0);
        if (last && lane < 16) __hip_atomic_store(f, nonce, RLX);
    }
    STAMP(20);
    if (w < 4 || w == 7) {
        poll_idx(mb(b, MB_Y), lane < 36 ? lane : -1, nonce);
        if (w < 4 || lane < 32) {
            const int chB = 72 * bc + 2 * cp, t0 = 64 * br + 8 * oct;
            const u32x4 y0 = __builtin_amdgcn_raw_buffer_load_b128(wsr, WS_Y + chB * 4096 + t0 * 2, 0, 16);
            const u32x4 y1 = __builtin_amdgcn_raw_buffer_load_b128(wsr, WS_Y + (chB + 1) * 4096 + t0 * 2, 0, 16);
            const float D0 = blo(dv), D1 = bhi(dv);
            unsigned zz[8];
#pragma unroll
            for (int t = 0; t < 8; ++t) {
                const int slot = (t & 1) * 4 + (t >> 1);
                const float ya = (slot & 1) ? bhi(y0[slot >> 1]) : blo(y0[slot >> 1]);
                const float yb = (slot & 1) ? bhi(y1[slot >> 1]) : blo(y1[slot >> 1]);
                const int j = 7 - t;
                const float ka = (j & 1) ? bhi(kv0[j >> 1]) : blo(kv0[j >> 1]);
                const float kb = (j & 1) ? bhi(kv1[j >> 1]) : blo(kv1[j >> 1]);
                const unsigned kd = pk(ka * D0, kb * D1);
                zz[t] = pk(ya + blo(kd), yb + bhi(kd));
            }
            spin4(L0 + SY_QRD, 1); spin4(L0 + SY_QRD + 16, 1);
#pragma unroll
            for (int t = 0; t < 8; ++t) st32(L0 + L_Z + oct * 1184 + t * 144 + cp * 4, zz[t]);
        }
    } else if (w == 4) {
        poll_idx(mb(b, MB_O), lane < 16 ? lane : -1, nonce);
    }
    STAMP(21);
    BAR();
    STAMP(22);
    {
        const int tt = w & 3, kh = w >> 2, T16 = 4 * br + tt;
        u32x4 of[9];
#pragma unroll
        for (int s = 0; s < 8; ++s) of[s] = __builtin_amdgcn_raw_buffer_load_b128(wsr, WS_OM + ((T16 * 16 + 8 * kh + s) * 64 + lane) * 16, 0, 16);
#pragma unroll
        for (int d = 0; d < 4; ++d) of[8][d] = __builtin_amdgcn_raw_buffer_load_b32(wsr, WS_OR + ((((T16 >> 1) * 16 + 8 * kh + 2 * g4 + (d >> 1)) * 2 + (d & 1)) * 32 + 16 * (T16 & 1) + l15) * 4, 0, 16);
        SB0();
        {
            auto gate = [&](const u32x4& qv, int tk, int gr) {
                const u32x4 z = lds128(L0 + L_Z + tk * 144 + (tk >> 3) * 32 + gr * 16);
                u32x4 r;
#pragma unroll
                for (int d = 0; d < 4; ++d) r[d] = pk(blo(qv[d]) * blo(z[d]), bhi(qv[d]) * bhi(z[d]));
                __builtin_amdgcn_raw_buffer_store_b128(r, outr, (64 * br + tk) * 2304 + 1152 + bc * 144 + gr * 16, 0, OUTAUX);
            };
            gate(qg0, tk0, gr0);
            if (w == 5) gate(qg1, tk1, gr1);
        }
        f32x4 acc[5];
#pragma unroll
        for (int j = 0; j < 5; ++j) acc[j] = f32x4{0, 0, 0, 0};
        const int q4 = (lane >> 2) & 3, p4 = lane & 3, hd = g4 >> 1, hq = g4 & 1;
        const unsigned mlane = L0 + (20 * hd + 4 * hq + q4) * 144 + p4 * 8;
        const unsigned mlane4 = L0 + (20 * hd + 4 * hq + q4) * 144 + 128 + (p4 & 1) * 8;
        const int gr = 8 * kh + 2 * g4;
        const unsigned rlane = L0 + (gr < 6 ? L_WO1 + gr * 5184 : L_WO2 + (gr - 6) * 5184) + (16 + q4) * 144;
        u32x2 fa[2][5], fc[2][5];
        auto ldw = [&](int bf, int s) {
            if (s < 8) {
                const int gq = 8 * kh + s;
                const unsigned gb = gq < 6 ? L_WO1 + gq * 5184 : L_WO2 + (gq - 6) * 5184;
                const unsigned m0 = mlane + gb, m4 = mlane4 + gb;
#pragma unroll
                for (int j = 0; j < 4; ++j) { fa[bf][j] = ldstr(m0 + j * 32); fc[bf][j] = ldstr(m0 + j * 32 + 8 * 144); }
                fa[bf][4] = ldstr(m4); fc[bf][4] = ldstr(m4 + 8 * 144);
            } else {
#pragma unroll
                for (int j = 0; j < 5; ++j) {
                    const unsigned ad = rlane + (j < 4 ? j * 32 + p4 * 8 : 128 + (p4 & 1) * 8);
                    fa[bf][j] = ldstr(ad); fc[bf][j] = ldstr(ad + 5184);
                }
            }
        };
        ldw(0, 0);
#pragma unroll
        for (int s = 0; s < 9; ++s) {
            SB0();
            if (s < 8) ldw((s + 1) & 1, s + 1);
            SB0();
#pragma unroll
            for (int j = 0; j < 5; ++j) acc[j] = mfma16(u32x4{fa[s & 1][j][0], fa[s & 1][j][1], fc[s & 1][j][0], fc[s & 1][j][1]}, of[s], acc[j]);
        }
        SB0();
        const unsigned sc = L0 + L_X + tt * 5120 + lane * 16;
        if (kh == 0) { stf4(sc + 2048, acc[2]); stf4(sc + 3072, acc[3]); }
        else { stf4(sc, acc[0]); stf4(sc + 1024, acc[1]); stf4(sc + 4096, acc[4]); }
        BAR();
        const int op = (16 * T16 + l15) * 2304 + bc * 144;
        auto st2 = [&](const f32x4& va, const f32x4& vb) {
            const unsigned a0 = pk(va[0], va[1]), a1 = pk(va[2], va[3]), b0 = pk(vb[0], vb[1]), b1 = pk(vb[2], vb[3]);
            auto s0 = __builtin_amdgcn_permlane16_swap(a0, b0, false, false);
            auto s1 = __builtin_amdgcn_permlane16_swap(a1, b1, false, false);
            return u32x4{s0[0], s1[0], s0[1], s1[1]};
        };
        if (kh == 0) {
            const f32x4 v0 = acc[0] + ldsf4(sc), v1 = acc[1] + ldsf4(sc + 1024), v4 = acc[4] + ldsf4(sc + 4096);
            __builtin_amdgcn_raw_buffer_store_b128(st2(v0, v1), outr, op + (g4 & 1) * 32 + (g4 >> 1) * 16, 0, OUTAUX);
            const u32x4 r4 = st2(v4, v4);
            if (g4 == 0) __builtin_amdgcn_raw_buffer_store_b128(r4, outr, op + 128, 0, OUTAUX);
        } else {
            const f32x4 v2 = acc[2] + ldsf4(sc + 2048), v3 = acc[3] + ldsf4(sc + 3072);
            __builtin_amdgcn_raw_buffer_store_b128(st2(v2, v3), outr, op + 64 + (g4 & 1) * 32 + (g4 >> 1) * 16, 0, OUTAUX);
        }
    }
    STAMP(23);
    if (b == 0 && w == 7) {
        for (int k = 0; k < 4; ++k) poll_idx((const GAS u64*)(ws + WS_ARR), 64 * k + lane, nonce);
        if (lane == 0) __hip_atomic_store((GAS u64*)ws, nonce, RLX);
    }
    BAR();
    if (b == 0 && w == 7) {
        unsigned dl = (vlds32(L0 + L_SYNC + 240) - amp0) * AMPF;
        dl = dl < 40000u ? dl : 40000u;
        const unsigned t1 = (unsigned)__builtin_amdgcn_s_memrealtime();
        for (int i = 0; i < 2000000; ++i) { if ((unsigned)__builtin_amdgcn_s_memrealtime() - t1 >= dl) break; __builtin_amdgcn_s_sleep(2); }
    }
}

extern "C" void kernel_launch(void* const* d_in, const int* in_sizes, int n_in, void* d_out, int out_size, void* d_ws, size_t ws_size, hipStream_t stream) {
    fused_kernel<<<256, 512, 0, stream>>>((const unsigned short*)d_in[0], (const unsigned short*)d_in[1], (const unsigned short*)d_in[2], (const unsigned short*)d_in[3],
                                          (const unsigned short*)d_in[4], (const unsigned short*)d_in[5], (const unsigned short*)d_in[6], (const unsigned short*)d_in[7],
                                          (const unsigned short*)d_in[8], (unsigned short*)d_out, (char*)d_ws);
}
```

```cpp
#include <hip/hip_runtime.h>
#include <stdint.h>

#define GAS __attribute__((address_space(1)))
#define LAS __attribute__((address_space(3)))
#define DEVI __device__ __forceinline__
typedef unsigned long long u64;
typedef __attribute__((ext_vector_type(8))) __bf16 bf16x8;
typedef __attribute__((ext_vector_type(2))) __bf16 bf16x2;
typedef __attribute__((ext_vector_type(2))) float f32x2;
typedef __attribute__((ext_vector_type(4))) float f32x4;
typedef __attribute__((ext_vector_type(16))) float f32x16;
typedef __attribute__((ext_vector_type(4))) unsigned u32x4;
typedef __attribute__((ext_vector_type(2))) unsigned u32x2;
typedef __attribute__((ext_vector_type(4))) short s16x4;

constexpr int WS_ARR = 256;
constexpr int WS_R0 = 32768;
constexpr int WS_QKV = WS_R0 + 2359296;
constexpr int WS_OM = WS_QKV + 7077888;
constexpr int WS_OR = WS_OM + 2097152;
constexpr int WS_Y = WS_OR + 262144;
constexpr int WS_MB = WS_Y + 2359296;
constexpr int MBS = 4352, MB_Q = 0, MB_O = 128, MB_K = 256, MB_Y = 512;
constexpr int WS_END = WS_MB + 256 * MBS;
constexpr int WS_TS = 32 << 20;
constexpr int L_B1K = 131072, L_B1V = 141312;
constexpr int L_B1S = 151552;
constexpr int KCS = 4160, KCH = 4 * KCS;
constexpr int RCS = 4576, RCH = 2 * RCS;
constexpr int L_KB = 32768, L_RB = 131072;
constexpr int L_KA = 66048, L_RA = 0;
constexpr int L_SCR = 114688;
constexpr int BLK = 10752, PCS = 2112;
constexpr int L_KBLK = 0, L_VBLK = 66048, L_QBLK = 66048 + 3 * BLK;
constexpr int L_WO1 = 32768, L_WO2 = 109056;
constexpr int L_ONES = 161280;
constexpr int L_Z = L_QBLK;
constexpr int L_X = 0;
constexpr int L_SYNC = 163584;
constexpr int SY_LAND = L_SYNC, SY_CONS = L_SYNC + 16, SY_PW = L_SYNC + 32, SY_PR = L_SYNC + 48, SY_PREP = L_SYNC + 64, SY_YDR = L_SYNC + 72,
              SY_ODR = L_SYNC + 76, SY_NONCE = L_SYNC + 80, SY_QDR = L_SYNC + 96, SY_QRD = L_SYNC + 112, SY_QSEEN = L_SYNC + 144, SY_KC = L_SYNC + 160;
constexpr int LDS_BYTES = 163840;
constexpr unsigned SPIN_CAP = 1u << 17, LSPIN_CAP = 1u << 20;

#define STAMPW(k) do { } while (0)
#define STAMPC(k) do { } while (0)
#define STAMP(k) do { } while (0)

DEVI f32x4 mfma16(u32x4 a, u32x4 b, f32x4 c) { return __builtin_amdgcn_mfma_f32_16x16x32_bf16(__builtin_bit_cast(bf16x8, a), __builtin_bit_cast(bf16x8, b), c, 0, 0, 0); }
DEVI f32x16 mfma32(u32x4 a, u32x4 b, f32x16 c) { return __builtin_amdgcn_mfma_f32_32x32x16_bf16(__builtin_bit_cast(bf16x8, a), __builtin_bit_cast(bf16x8, b), c, 0, 0, 0); }
DEVI f32x16 mfma32k8(u32x2 a, u32x2 b, f32x16 c) { return __builtin_amdgcn_mfma_f32_32x32x8bf16_1k(__builtin_bit_cast(s16x4, a), __builtin_bit_cast(s16x4, b), c, 0, 0, 0); }
DEVI unsigned pk(float lo, float hi) { f32x2 v = {lo, hi}; return __builtin_bit_cast(unsigned, __builtin_convertvector(v, bf16x2)); }
DEVI float blo(unsigned v) { return __builtin_bit_cast(float, v << 16); }
DEVI float bhi(unsigned v) { return __builtin_bit_cast(float, v & 0xffff0000u); }
DEVI u32x4 lds128(unsigned a) { return *(LAS const u32x4*)(uintptr_t)a; }
DEVI u32x2 lds64(unsigned a) { return *(LAS const u32x2*)(uintptr_t)a; }
DEVI unsigned lds32(unsigned a) { return *(LAS const unsigned*)(uintptr_t)a; }
DEVI void st128(unsigned a, u32x4 v) { *(LAS u32x4*)(uintptr_t)a = v; }
DEVI void st64(unsigned a, u32x2 v) { *(LAS u32x2*)(uintptr_t)a = v; }
DEVI void st32(unsigned a, unsigned v) { *(LAS unsigned*)(uintptr_t)a = v; }
DEVI f32x4 ldsf4(unsigned a) { return *(LAS const f32x4*)(uintptr_t)a; }
DEVI void stf4(unsigned a, f32x4 v) { *(LAS f32x4*)(uintptr_t)a = v; }
DEVI u32x2 ldstr(unsigned a) { return __builtin_bit_cast(u32x2, __builtin_amdgcn_ds_read_tr16_b64_v4i16((LAS s16x4*)(uintptr_t)a)); }
DEVI u32x4 vlds128(unsigned a) { return *(volatile LAS const u32x4*)(uintptr_t)a; }
DEVI unsigned vlds32(unsigned a) { return *(volatile LAS const unsigned*)(uintptr_t)a; }
DEVI void vst32(unsigned a, unsigned v) { *(volatile LAS unsigned*)(uintptr_t)a = v; }
DEVI void dma16(const void* g, unsigned lds) {
    unsigned sv;
    asm volatile("s_mov_b32 %0, m0\n\ts_mov_b32 m0, %2\n\ts_nop 0\n\tglobal_load_lds_dwordx4 %1, off\n\ts_mov_b32 m0, %0" : "=&s"(sv) : "v"(g), "s"(lds) : "memory");
}
DEVI void dmaS(unsigned voff, const void* sbase, unsigned lds) {
    unsigned sv;
    asm volatile("s_mov_b32 %0, m0\n\ts_mov_b32 m0, %3\n\ts_nop 0\n\tglobal_load_lds_dwordx4 %1, %2\n\ts_mov_b32 m0, %0" : "=&s"(sv) : "v"(voff), "s"(sbase), "s"(lds) : "memory");
}
#define WAITVM(n) asm volatile("s_waitcnt vmcnt(%0)" ::"i"(n) : "memory")
#define BAR() do { asm volatile("s_waitcnt lgkmcnt(0)\n\ts_barrier" ::: "memory"); } while (0)
#define CFENCE() asm volatile("" ::: "memory")
#define SB0() __builtin_amdgcn_sched_barrier(0)
#define RLX __ATOMIC_RELAXED, __HIP_MEMORY_SCOPE_AGENT
DEVI unsigned rfl(unsigned v) { return __builtin_amdgcn_readfirstlane(v); }
DEVI unsigned umin4(u32x4 v) { const unsigned a = v[0] < v[1] ? v[0] : v[1], c = v[2] < v[3] ? v[2] : v[3]; return a < c ? a : c; }

DEVI void spin4(unsigned a, unsigned need) {
#pragma nounroll
    for (unsigned s = 0; s < LSPIN_CAP; ++s) {
        if (rfl(umin4(vlds128(a))) >= need) break;
        __builtin_amdgcn_s_sleep(1);
    }
    CFENCE();
}
DEVI void spin1(unsigned a, unsigned need) {
#pragma nounroll
    for (unsigned s = 0; s < LSPIN_CAP; ++s) {
        if (rfl(vlds32(a)) >= need) break;
        __builtin_amdgcn_s_sleep(1);
    }
    CFENCE();
}
DEVI void signal(unsigned a, unsigned v, int lane) { CFENCE(); if (lane == 0) vst32(a, v); CFENCE(); }
DEVI bool arrive(unsigned a, unsigned n, int lane) {
    CFENCE();
    unsigned old = 0;
    if (lane == 0) old = __hip_atomic_fetch_add((LAS unsigned*)(uintptr_t)a, 1u, __ATOMIC_RELAXED, __HIP_MEMORY_SCOPE_WORKGROUP);
    CFENCE();
    return lane == 0 && old == n - 1;
}
#define GPRIO 3
#define OUTAUX 17
#define POLLS 2
DEVI void poll_idx(const GAS u64* f, int idx, u64 nonce) {
#pragma nounroll
    for (unsigned spins = 0; spins < SPIN_CAP; ++spins) {
        u64 v = nonce;
        if (idx >= 0) v = __hip_atomic_load(f + idx, RLX);
        if (__all(v == nonce)) break;
        __builtin_amdgcn_s_sleep(POLLS);
    }
    __builtin_amdgcn_fence(__ATOMIC_ACQUIRE, "wavefront");
    CFENCE();
}

DEVI int wg_attn(int ai, int gp) { return (30 - 2 * ai + (gp & 1)) * 8 + (gp >> 1); }
DEVI int wg_pair(int p) { return p < 224 ? (p % 28) * 8 + p / 28 : (28 + (((p - 224) >> 1) & 3)) * 8 + ((p - 224) >> 3); }
DEVI int wg_tile(int br, int bc) { return (4 * bc + (br & 3)) * 8 + (br >> 2); }

__device__ constexpr int CMU[36] = {0,0,0,0,1,1,1,1,2,2,2,0,3,3,3,2,4,4,1,0,5,5,4,3,6,2,1,0,7,6,5,4,3,2,1,0};
__device__ constexpr int CNU[36] = {7,6,5,4,6,5,4,3,5,4,3,3,4,3,2,2,3,2,2,2,2,1,1,1,1,1,1,1,0,0,0,0,0,0,0,0};

__global__ __launch_bounds__(512, 2) void fused_kernel(const unsigned short* __restrict__ x, const unsigned short* __restrict__ wqkv, const unsigned short* __restrict__ wo,
                                                       const unsigned short* __restrict__ bq, const unsigned short* __restrict__ bk, const unsigned short* __restrict__ bv,
                                                       const unsigned short* __restrict__ shk, const unsigned short* __restrict__ ssk, const unsigned short* __restrict__ ssd,
                                                       unsigned short* __restrict__ out, char* __restrict__ ws) {
    __shared__ __attribute__((aligned(256))) char smem[LDS_BYTES];
    const unsigned L0 = (unsigned)(uintptr_t)(LAS char*)smem;
    const int tid = threadIdx.x, lane = tid & 63, w = __builtin_amdgcn_readfirstlane(tid >> 6);
    const int b = blockIdx.x, xcd = b & 7, jj = b >> 3;
    const bool busy = jj < 28;
    const int ri = 4 * (xcd >> 1) + (jj & 3), cj = 7 * (xcd & 1) + (jj >> 2);
    const int br = 4 * xcd + (jj & 3), bc = jj >> 2;
    const int ai = (31 - jj) >> 1, gp = 2 * xcd + (jj & 1);
    const int pairB = busy ? xcd * 28 + jj : 225 + 2 * (xcd * 4 + jj - 28);
    const int l15 = lane & 15, g4 = lane >> 4, l31 = lane & 31, hh = lane >> 5;
    const __amdgpu_buffer_rsrc_t wsr = __builtin_amdgcn_make_buffer_rsrc(ws, 0, WS_END, 0x00020000);
    const __amdgpu_buffer_rsrc_t outr = __builtin_amdgcn_make_buffer_rsrc(out, 0, 2048 * 2304, 0x00020000);
    STAMP(0);
    auto mb = [&](int wg, int off) { return (GAS u64*)(ws + WS_MB + wg * MBS + off); };
    auto get_nonce = [&]() {
        const u32x2 v = *(volatile LAS const u32x2*)(uintptr_t)(L0 + SY_NONCE);
        return ((u64)rfl(v[1]) << 32) | rfl(v[0]);
    };
    auto gather = [&](int sw, u64 nonce) {
        spin4(L0 + SY_CONS, 9);
        if (sw == 0) {
            const int j = lane >> 1, rr = j < 6 ? ai - 2 + (j < 3 ? j : j - 3) : ai;
            poll_idx(mb(b, MB_Q), (lane < 14 && rr >= 0) ? lane : -1, nonce);
            signal(L0 + SY_QSEEN, 1, lane);
        } else {
            spin1(L0 + SY_QSEEN, 1);
        }
        STAMPW(24 + sw);
        u32x4 g[18];
        const int p0 = (36 * gp - 4 * (gp & 1)) >> 3;
#pragma unroll
        for (int k = 0; k < 18; ++k) {
            const int u = sw + 4 * k, j = u / 10, e = u - 10 * j;
            const int sec = j < 3 ? 1 : (j < 6 ? 2 : 0), rr = j < 6 ? ai - 2 + (j < 3 ? j : j - 3) : ai;
            if (u < 70 && rr >= 0) g[k] = __builtin_amdgcn_raw_buffer_load_b128(wsr, lane * 16, WS_QKV + ((rr * 216 + 72 * sec + p0) * 128) * 16 + e * 1024, 16);
        }
#pragma unroll
        for (int k = 0; k < 18; ++k) {
            const int u = sw + 4 * k, j = u / 10, e = u - 10 * j;
            const int rr = j < 6 ? ai - 2 + (j < 3 ? j : j - 3) : ai;
            const unsigned dst = L0 + (j < 3 ? L_KBLK + j * BLK : L_VBLK + (j - 3) * BLK) + (e >> 1) * PCS + (e & 1) * 1024 + lane * 16;
            if (u < 70 && rr >= 0) st128(dst, g[k]);
        }
    };

    if (w < 4) {
        if (w == 0) {
            const u64 ep = __hip_atomic_load((const GAS u64*)ws, RLX);
            if (lane < 20 || (lane >= 24 && lane < 44)) vst32(L0 + L_SYNC + lane * 4, lane == 3 ? 0xffffffffu : 0u);
            const u64 nn = ep + 1;
            if (lane == 0) *(volatile LAS u32x2*)(uintptr_t)(L0 + SY_NONCE) = u32x2{(unsigned)nn, (unsigned)(nn >> 32)};
        }
        if (w == 1) {
#pragma unroll
            for (int i = 0; i < 3; ++i) { const int q = i * 64 + lane; if (q < 140) st128(L0 + L_ONES + q * 16, u32x4{0x3f803f80u, 0x3f803f80u, 0x3f803f80u, 0x3f803f80u}); }
        }
        BAR();
        STAMP(1);
        u32x4 kd0[2], kd1[2];
        auto kload = [&](int pair) {
#pragma unroll
            for (int r = 0; r < 2; ++r) {
                const int cq = (2 * (w & 1) + r) * 64 + lane;
                const char* p = (const char*)ssk + (2 * pair + (w >> 1)) * 4096 + cq * 16;
                kd0[r] = *(const u32x4*)p; kd1[r] = *(const u32x4*)(p + (cq < 255 ? 16 : 0));
            }
        };
        auto kcopy = [&](unsigned kb0) {
            const unsigned kb = kb0 + (w >> 1) * KCH;
#pragma unroll
            for (int r = 0; r < 2; ++r) {
                const unsigned a = kb + (4 + (2 * (w & 1) + r) * 64 + lane) * 16;
                const u32x4 d0 = kd0[r], d1 = kd1[r];
                st128(a, d0);
                st128(a + KCS, u32x4{d0[1], d0[2], d0[3], d1[0]});
                st128(a + 2 * KCS, u32x4{d0[2], d0[3], d1[0], d1[1]});
                st128(a + 3 * KCS, u32x4{d0[3], d1[0], d1[1], d1[2]});
            }
            if ((w & 1) == 0) {
                const u32x4 d0 = kd0[0];
                if (lane == 0) {
                    st128(kb + 3 * 16, u32x4{0, 0, 0, 0});
                    st128(kb + KCS + 3 * 16, u32x4{0, 0, 0, d0[0]});
                    st128(kb + 2 * KCS + 3 * 16, u32x4{0, 0, d0[0], d0[1]});
                    st128(kb + 3 * KCS + 3 * 16, u32x4{0, d0[0], d0[1], d0[2]});
                } else if (lane < 13) {
                    const int e = lane - 1;
                    st128(kb + (e / 3) * KCS + (e % 3) * 16, u32x4{0, 0, 0, 0});
                }
            }
        };
        if (!busy) { kload(pairB - 1); kcopy(L0 + L_KA); }
        kload(pairB);
        if (busy) {
            const int wt = w & 1, wc = w >> 1;
            const bool live = !(cj == 13 && wc == 1);
            if (live) {
                unsigned xo[4], wof[2];
#pragma unroll
                for (int j = 0; j < 4; ++j) xo[j] = L0 + (64 * wt + l31) * 128 + (((2 * j + hh) ^ ((l31 >> 1) & 7)) * 16);
                {
                    const int q = (lane >> 2) & 3, p = lane & 3;
#pragma unroll
                    for (int ct = 0; ct < 2; ++ct) wof[ct] = L0 + 16384 + (8 * hh + q) * 256 + (((8 * wc + 4 * ct + 2 * (g4 & 1) + (p >> 1)) ^ (q << 2)) * 16) + (p & 1) * 8;
                }
                f32x16 acc[2][2];
#pragma unroll
                for (int r = 0; r < 16; ++r) { acc[0][0][r] = 0.f; acc[0][1][r] = 0.f; acc[1][0][r] = 0.f; acc[1][1][r] = 0.f; }
                u32x4 xf[2][2];
                u32x2 wa[2][2], wc2[2][2];
                auto ldx = [&](int s, int j, int pb, int tt) { xf[pb][tt] = lds128(xo[j] + (s & 3) * 32768 + tt * 4096); };
                auto ldwa = [&](int s, int j, int pb, int ct) { wa[pb][ct] = ldstr(wof[ct] + (s & 3) * 32768 + j * 4096); };
                auto ldwc = [&](int s, int j, int pb, int ct) { wc2[pb][ct] = ldstr(wof[ct] + (s & 3) * 32768 + j * 4096 + 1024); };
                auto W = [&](int pb, int ct) { return u32x4{wa[pb][ct][0], wa[pb][ct][1], wc2[pb][ct][0], wc2[pb][ct][1]}; };
                spin4(L0 + SY_LAND, 1);
                STAMP(3); STAMPC(40);
                ldx(0, 0, 0, 0); ldwa(0, 0, 0, 0); ldwc(0, 0, 0, 0); ldx(0, 0, 0, 1); ldwa(0, 0, 0, 1); ldwc(0, 0, 0, 1);
#pragma unroll
                for (int s = 0; s < 9; ++s) {
                    u32x4 landv;
#pragma unroll
                    for (int j = 0; j < 4; ++j) {
                        const int pb = j & 1, nb = pb ^ 1;
                        const bool more = j < 3 || s < 8;
                        const int ns = j < 3 ? s : s + 1, nj = (j + 1) & 3;
                        SB0();
                        acc[0][0] = mfma32(W(pb, 0), xf[pb][0], acc[0][0]);
                        SB0();
                        if (j == 3 && s < 8) { if (rfl(umin4(landv)) < (unsigned)(s + 2)) spin4(L0 + SY_LAND, s + 2); CFENCE(); }
                        if (more) { ldx(ns, nj, nb, 0); ldwa(ns, nj, nb, 0); ldwc(ns, nj, nb, 0); }
                        SB0();
                        acc[0][1] = mfma32(W(pb, 0), xf[pb][1], acc[0][1]);
                        SB0();
                        if (more) { ldx(ns, nj, nb, 1); ldwa(ns, nj, nb, 1); }
                        SB0();
                        acc[1][0] = mfma32(W(pb, 1), xf[pb][0], acc[1][0]);
                        SB0();
                        if (more) { ldwc(ns, nj, nb, 1); }
                        if (j == 2) {
                            signal(L0 + SY_CONS + w * 4, s + 1, lane);
                        }
                        if (j == 1 && s < 8) landv = vlds128(L0 + SY_LAND);
                        SB0();
                        acc[1][1] = mfma32(W(pb, 1), xf[pb][1], acc[1][1]);
                    }
                }
                STAMP(4); STAMPC(41);
#pragma unroll
                for (int ct = 0; ct < 2; ++ct) {
                    const int n0 = 128 * cj + 64 * wc + 32 * ct;
                    const float sc = n0 < 576 ? 0.34004634f : 1.0f;
#pragma unroll
                    for (int tt = 0; tt < 2; ++tt) {
#pragma unroll
                        for (int pi = 0; pi < 2; ++pi) {
                            const f32x16& a = acc[ct][tt];
                            const unsigned a0 = pk(a[8 * pi] * sc, a[8 * pi + 1] * sc), a1 = pk(a[8 * pi + 2] * sc, a[8 * pi + 3] * sc);
                            const unsigned b0 = pk(a[8 * pi + 4] * sc, a[8 * pi + 5] * sc), b1 = pk(a[8 * pi + 6] * sc, a[8 * pi + 7] * sc);
                            auto s0 = __builtin_amdgcn_permlane32_swap(a0, b0, false, false);
                            auto s1 = __builtin_amdgcn_permlane32_swap(a1, b1, false, false);
                            const int off = WS_QKV + ((ri * 216 + (n0 >> 3) + 2 * pi + hh) * 128 + 64 * wt + 32 * tt + l31) * 16;
                            __builtin_amdgcn_raw_buffer_store_b128(u32x4{s0[0], s1[0], s0[1], s1[1]}, wsr, off, 0, 16);
                        }
                    }
                }
            } else {
                signal(L0 + SY_CONS + w * 4, 9, lane);
            }
            STAMP(5);
            spin4(L0 + SY_CONS, 7);
        }
        kcopy(L0 + L_KB);
        signal(L0 + SY_KC + w * 4, 1, lane);
        bool qdone = !busy;
        auto qdrain = [&]() {
            WAITVM(0);
            signal(L0 + SY_QDR + w * 4, 1, lane);
            STAMP(8);
        };
        spin1(L0 + SY_KC + (w ^ 1) * 4, 1);
        for (int pass = busy ? 1 : 0; pass < 2; ++pass) {
            if (!qdone && rfl(vlds32(L0 + SY_PREP)) < (unsigned)(pass + 1)) { qdrain(); qdone = true; }
            spin1(L0 + SY_PREP, pass + 1);
            STAMP(6 + 3 * pass);
            const int slot = w >> 1, h = w & 1;
            const unsigned kb = L0 + (pass ? L_KB : L_KA) + slot * KCH, rb = L0 + (pass ? L_RB : L_RA) + slot * RCH;
            const int ch = 2 * (pairB - 1 + pass) + slot;
            const unsigned la = kb + ((l15 + 1) & 3) * KCS + (g4 + ((l15 + 1) >> 2)) * 16 + h * 256;
            const unsigned lb = rb + ((l15 & 1) ? 0 : RCS) + (28 - 4 * (l15 >> 1) + g4) * 16 + h * 256;
            f32x4 acc[8];
#pragma unroll
            for (int n = 0; n < 8; ++n) acc[n] = f32x4{0, 0, 0, 0};
            u32x4 A[2][8], B[2][8];
#pragma unroll
            for (int m = 0; m < 8; ++m) { A[0][m] = lds128(la + m * 512); B[0][m] = lds128(lb + (7 - m) * 512); }
#pragma unroll
            for (int rr = 0; rr < 4; ++rr) {
                const int cur = rr & 1, nx = cur ^ 1;
#pragma unroll
                for (int i = 0; i < 36; ++i) {
                    SB0();
                    acc[CMU[i] + CNU[i]] = mfma16(A[cur][CMU[i]], B[cur][CNU[i]], acc[CMU[i] + CNU[i]]);
                    SB0();
                    if (rr < 3 && (i & 1) == 0 && i < 32) {
                        const int m = i >> 2;
                        if (i & 2) B[nx][7 - m] = lds128(lb + (rr + 1) * 64 + m * 512);
                        else A[nx][m] = lds128(la + (rr + 1) * 64 + m * 512);
                    }
                }
                if (rr == 1 && !qdone) { qdrain(); qdone = true; }
            }
            if (pass == 0) signal(L0 + SY_CONS + w * 4, 9, lane);
            const unsigned mine = L0 + L_SCR + w * 4096 + lane * 16, theirs = L0 + L_SCR + (w ^ 1) * 4096 + lane * 16;
            if (busy) spin4(L0 + SY_CONS, 9);
            else if (pass == 1) spin1(L0 + SY_PR + (w ^ 1) * 4, 1);
            f32x4 y[4];
            if (h) {
#pragma unroll
                for (int n = 0; n < 4; ++n) stf4(mine + n * 1024, acc[n]);
            } else {
#pragma unroll
                for (int n = 0; n < 4; ++n) stf4(mine + n * 1024, acc[4 + n]);
            }
            signal(L0 + SY_PW + w * 4, pass + 1, lane);
            spin1(L0 + SY_PW + (w ^ 1) * 4, pass + 1);
            if (h) {
#pragma unroll
                for (int n = 0; n < 4; ++n) y[n] = acc[4 + n] + ldsf4(theirs + n * 1024);
            } else {
#pragma unroll
                for (int n = 0; n < 4; ++n) y[n] = acc[n] + ldsf4(theirs + n * 1024);
            }
            signal(L0 + SY_PR + w * 4, pass + 1, lane);
            const bool odd = lane & 1;
#pragma unroll
            for (int i = 0; i < 4; i += 2) {
                const unsigned a0 = pk(y[i][0], y[i][1]), a1 = pk(y[i][2], y[i][3]), c0 = pk(y[i + 1][0], y[i + 1][1]), c1 = pk(y[i + 1][2], y[i + 1][3]);
                const unsigned r0 = __builtin_amdgcn_mov_dpp(odd ? a0 : c0, 0xB1, 0xF, 0xF, true), r1 = __builtin_amdgcn_mov_dpp(odd ? a1 : c1, 0xB1, 0xF, 0xF, true);
                const u32x4 v = odd ? u32x4{r0, r1, c0, c1} : u32x4{a0, a1, r0, r1};
                __builtin_amdgcn_raw_buffer_store_b128(v, wsr, WS_Y + ch * 4096 + (4 * h + i + (odd ? 1 : 0)) * 512 + ((l15 >> 1) * 4 + g4) * 16, 0, 16);
            }
            STAMP(7 + 3 * pass);
        }
    } else if (w < 7) {
        const int d = w - 4;
        if (busy) {
            const unsigned xv0 = (lane >> 3) * 1152 + (((lane & 7) ^ (lane >> 4)) * 16), xv1 = (lane >> 3) * 1152 + (((lane & 7) ^ (4 + (lane >> 4))) * 16);
            int wch = (lane & 15) ^ ((lane >> 4) << 2);
            if (cj == 13) wch &= 7;
            const unsigned wv = (lane >> 4) * 3456 + wch * 16;
            const char* xb = (const char*)x + (size_t)(128 * ri) * 1152;
            const char* wb = (const char*)wqkv + cj * 256;
            auto issue = [&](int s, int i0, int i1) {
                const unsigned sl = L0 + (s & 3) * 32768;
#pragma unroll
                for (int i = i0; i < i1; ++i) {
                    int u = d + 3 * i; u = u < 32 ? u : d;
                    if (u < 16) dmaS((u & 1) ? xv1 : xv0, xb + s * 128 + u * 9216, sl + u * 1024);
                    else dmaS(wv, wb + (size_t)(s * 64 + (u - 16) * 4) * 3456, sl + u * 1024);
                }
            };
            const unsigned land = L0 + SY_LAND + d * 4;
            issue(0, 0, 11); issue(1, 0, 11);
            BAR();
            issue(2, 0, 11);
            WAITVM(22); signal(land, 1, lane);
            issue(3, 0, 11);
            WAITVM(22); signal(land, 2, lane);
#pragma unroll
            for (int s = 0; s < 5; ++s) {
                spin4(L0 + SY_CONS, s + 1);
                issue(s + 4, 0, 6);
                WAITVM(17); signal(land, s + 3, lane);
                issue(s + 4, 6, 11);
            }
            WAITVM(11); signal(land, 8, lane);
            WAITVM(0); signal(land, 9, lane);
            if (d == 1) {
                const int i = lane / 6, r = lane - 6 * i, e = r & 1, dd = r >> 1;
                const int G = (cj == 0 ? 0 : (128 * cj - 39) / 36) + i, c = 36 * G - 4 * (G & 1) + 39 * e;
                const int sec = G >> 4, a2 = ri + dd;
                const bool ok = i < 6 && G < 48 && (c >> 7) == cj && (sec == 0 ? dd == 0 : a2 < 16);
                const int slot = sec == 0 ? 12 + e : 2 * (2 - dd + (sec == 2 ? 3 : 0)) + e;
                GAS u64* f = mb(wg_attn(a2, G & 15), MB_Q + slot * 8);
                const u64 nonce = get_nonce();
                spin4(L0 + SY_QDR, 1);
                if (ok) __hip_atomic_store(f, nonce, RLX);
                STAMPW(2);
            }
        } else {
            BAR();
        }
        gather(d, get_nonce());
    } else {
        for (int u = 0; u < 20; ++u) {
            const bool isk = u < 10;
            const int q = (isk ? u : u - 10) * 64 + lane;
            const int g = q / 74; int r = q - g * 74; r = r < 72 ? r : 71;
            int row = 8 * g + r / 9; const int cc = r - (r / 9) * 9;
            if (isk) {
                row = row < 68 ? row : 67;
                int tok = 64 * br - 4 + row; tok = tok < 0 ? 0 : tok;
                dma16((const char*)bk + tok * 1152 + bc * 144 + cc * 16, L0 + L_B1K + u * 1024);
            } else {
                row = row < 64 ? row : 63;
                dma16((const char*)bv + (64 * br + row) * 1152 + bc * 144 + cc * 16, L0 + L_B1V + (u - 10) * 1024);
            }
        }
        dma16(lane < 36 ? (const char*)shk + bc * 576 + lane * 16 : (const char*)shk, L0 + L_B1S);
        BAR();
        __builtin_amdgcn_s_setprio(GPRIO);
        WAITVM(0);
        STAMPW(28);
#pragma unroll
        for (int it = 0; it < 5; ++it) {
            const int bl = 64 * it + lane, cp = bl >> 3, oct = bl & 7;
            if (it < 4 || lane < 32) {
                const u32x4 shv = lds128(L0 + L_B1S + cp * 16);
                unsigned vv[8], kvp[8];
#pragma unroll
                for (int t = 0; t < 8; ++t) vv[t] = lds32(L0 + L_B1V + oct * 1184 + t * 144 + cp * 4);
                f32x2 k[11];
#pragma unroll
                for (int q = 0; q < 11; ++q) {
                    const unsigned v = lds32(L0 + L_B1K + oct * 1184 + (1 + q) * 144 + ((1 + q) >> 3) * 32 + cp * 4);
                    k[q] = f32x2{blo(v), bhi(v)};
                }
                if (br == 0 && oct == 0) { k[0] = f32x2{0.f, 0.f}; k[1] = k[0]; k[2] = k[0]; }
                const f32x2 s0 = {blo(shv[0]), blo(shv[2])}, s1 = {bhi(shv[0]), bhi(shv[2])}, s2 = {blo(shv[1]), blo(shv[3])}, s3 = {bhi(shv[1]), bhi(shv[3])};
#pragma unroll
                for (int t = 0; t < 8; ++t) {
                    const unsigned v = vv[t];
                    f32x2 a = k[t] * s3;
                    a = __builtin_elementwise_fma(k[t + 1], s2, a);
                    a = __builtin_elementwise_fma(k[t + 2], s1, a);
                    a = __builtin_elementwise_fma(k[t + 3], s0, a);
                    const unsigned ks = pk(a[0], a[1]);
                    kvp[t] = pk(blo(ks) * blo(v), bhi(ks) * bhi(v));
                }
                u32x4 r0, r1;
#pragma unroll
                for (int dd = 0; dd < 4; ++dd) {
                    r0[dd] = (kvp[7 - 2 * dd] & 0xffffu) | (kvp[6 - 2 * dd] << 16);
                    r1[dd] = (kvp[7 - 2 * dd] >> 16) | (kvp[6 - 2 * dd] & 0xffff0000u);
                }
                const int off = WS_R0 + (72 * bc + 2 * cp) * 4096 + (2040 - 64 * br - 8 * oct) * 2;
                __builtin_amdgcn_raw_buffer_store_b128(r0, wsr, off, 0, 16);
                __builtin_amdgcn_raw_buffer_store_b128(r1, wsr, off + 4096, 0, 16);
            }
        }
        STAMPW(29);
        const u64 nonce = get_nonce();
        GAS u64* kf = lane < 36 ? mb(wg_pair(36 * bc + lane), MB_K + br * 8) : (GAS u64*)(ws + WS_ARR) + b;
        WAITVM(0);
        if (lane < 37) __hip_atomic_store(kf, nonce, RLX);
        STAMPW(11);
        u32x4 rd[2][4]; unsigned rn[2][4];
        auto rload = [&](int pair) {
#pragma unroll
            for (int c = 0; c < 2; ++c)
#pragma unroll
                for (int r = 0; r < 4; ++r) {
                    const int cq = r * 64 + lane, off = WS_R0 + (2 * pair + c) * 4096 + cq * 16;
                    rd[c][r] = __builtin_amdgcn_raw_buffer_load_b128(wsr, off, 0, 16);
                    rn[c][r] = __builtin_amdgcn_raw_buffer_load_b32(wsr, off + (cq < 255 ? 16 : 0), 0, 16);
                }
        };
        auto rcopy = [&](unsigned rb0) {
#pragma unroll
            for (int c = 0; c < 2; ++c) {
                const unsigned rb = rb0 + c * RCH;
#pragma unroll
                for (int r = 0; r < 4; ++r) {
                    const int cq = r * 64 + lane;
                    const u32x4 dd = rd[c][r]; const unsigned nx = cq < 255 ? rn[c][r] : 0u;
                    st128(rb + cq * 16, dd);
                    st128(rb + RCS + cq * 16, u32x4{__builtin_amdgcn_alignbit(dd[1], dd[0], 16), __builtin_amdgcn_alignbit(dd[2], dd[1], 16), __builtin_amdgcn_alignbit(dd[3], dd[2], 16), __builtin_amdgcn_alignbit(nx, dd[3], 16)});
                }
                if (lane < 60) st128(rb + (lane >= 30 ? RCS : 0) + (256 + (lane >= 30 ? lane - 30 : lane)) * 16, u32x4{0, 0, 0, 0});
            }
        };
        poll_idx(mb(b, MB_K), lane < 32 ? lane : -1, nonce);
        STAMPW(12);
        if (!busy) { rload(pairB - 1); rcopy(L0 + L_RA); signal(L0 + SY_PREP, 1, lane); }
        rload(pairB);
        rcopy(L0 + L_RB);
        signal(L0 + SY_PREP, 2, lane);
        __builtin_amdgcn_s_setprio(0);
        STAMPW(13);
        gather(3, nonce);
    }
    STAMPW(16 + w);
    BAR();
    STAMP(14);
    const u64 nonce = get_nonce();
    const unsigned wov = (lane / 9) * 1152 + (lane % 9) * 16;
    auto wo_dma = [&](int k) {
        const int u = w + 8 * k;
        if (u < 83) {
            const int row0 = u < 31 ? 7 * u : 216 + 7 * (u - 31);
            const unsigned dst = L0 + (u < 31 ? L_WO1 + u * 1008 : L_WO2 + (u - 31) * 1008);
            if (u < 82 || lane < 27) dmaS(wov, (const char*)wo + row0 * 1152 + bc * 144, dst);
        }
    };
    auto ypublish = [&]() {
        if (w < 4) {
            const int p = pairB - (busy ? 0 : hh), pc = p / 36;
            GAS u64* f = mb(wg_tile(l31, pc), MB_Y + (p - 36 * pc) * 8);
            WAITVM(0);
            const bool last = __builtin_amdgcn_readfirstlane(arrive(L0 + SY_YDR, 4, lane) ? 1 : 0);
            if (last && (lane < 32 || !busy)) __hip_atomic_store(f, nonce, RLX);
        }
    };
    const int id0 = tid, tk0 = id0 / 9, gr0 = id0 - 9 * tk0, id1 = 512 + lane, tk1 = id1 / 9, gr1 = id1 - 9 * tk1;
    u32x4 qg0, qg1 = u32x4{0, 0, 0, 0};
    const int bl = w < 4 ? tid : 256 + l31, cp = bl >> 3, oct = bl & 7;
    u32x4 kv0 = u32x4{0, 0, 0, 0}, kv1 = u32x4{0, 0, 0, 0};
    unsigned dv = 0;
    auto q_prefetch = [&]() {
        qg0 = *(const u32x4*)((const char*)bq + (64 * br + tk0) * 1152 + bc * 144 + gr0 * 16);
        if (w == 5) qg1 = *(const u32x4*)((const char*)bq + (64 * br + tk1) * 1152 + bc * 144 + gr1 * 16);
        if (w < 4 || w == 7) {
            const int off = WS_R0 + (72 * bc + 2 * cp) * 4096 + (2040 - 64 * br - 8 * oct) * 2;
            kv0 = __builtin_amdgcn_raw_buffer_load_b128(wsr, off, 0, 16);
            kv1 = __builtin_amdgcn_raw_buffer_load_b128(wsr, off + 4096, 0, 16);
            dv = *(const unsigned*)(ssd + 72 * bc + 2 * cp);
        }
    };
    {
        const int hd = w >> 2, qt = w & 3, cs = 4 * (gp & 1);
        u32x4 qm = lds128(L0 + L_QBLK + (2 * hd + hh) * PCS + (32 * qt + l31) * 16);
        u32x2 qr = lds64(L0 + L_QBLK + (2 * hd + 2) * PCS + (32 * qt + l31) * 16 + hh * 8);
        signal(L0 + SY_QRD + w * 4, 1, lane);
        {
            const int lo = cs + 18 * hd;
#pragma unroll
            for (int d = 0; d < 4; ++d) { const int c = 8 * (2 * hd + hh) + 2 * d; qm[d] = (c >= lo && c < lo + 18) ? qm[d] : 0u; }
#pragma unroll
            for (int d = 0; d < 2; ++d) { const int c = 8 * (2 * hd + 2) + 4 * hh + 2 * d; qr[d] = (c >= lo && c < lo + 18) ? qr[d] : 0u; }
        }
        const unsigned kA = L0 + L_KBLK + (2 * hd + hh) * PCS + l31 * 16, kB = L0 + L_KBLK + (2 * hd + 2) * PCS + l31 * 16 + hh * 8;
        unsigned vA, vsel;
        {
            const int q = (lane >> 2) & 3, p = lane & 3;
            const bool ones = (g4 & 1) && p > 0;
            const int c = (g4 & 1) ? cs + 16 : cs + (hd ? 20 : 0) + 4 * p;
            vA = ones ? L0 + L_ONES + 64 * ((hd ? 1 : 3) + hh) : L0 + L_VBLK + (c >> 3) * PCS + (4 * hh + q) * 16 + ((c >> 2) & 1) * 8;
            vsel = ones ? 0u : 0xffffffffu;
        }
        const int qq = l31 - 4 * hh;
        const int t_first = ai >= 2 ? qt : (ai == 1 ? 4 : 8), t_last = 8 + qt;
        const bool has_first = ai >= 2;
        auto toff = [&](int t) { return (unsigned)((t >> 2) * BLK + (t & 3) * 512); };
        auto qk = [&](int t, const f32x16& c) {
            const unsigned o = toff(t);
            const u32x4 km = lds128(kA + o);
            const u32x2 kr = lds64(kB + o);
            return mfma32k8(kr, qr, mfma32(km, qm, c));
        };
        auto pv = [&](int t, const f32x16& p, const f32x16& o) {
            const unsigned va = vA + (toff(t) & vsel);
            const u32x2 t0 = ldstr(va), t1 = ldstr(va + 128), t2 = ldstr(va + 256), t3 = ldstr(va + 384);
            const u32x4 p0 = u32x4{pk(p[0], p[1]), pk(p[2], p[3]), pk(p[4], p[5]), pk(p[6], p[7])};
            const u32x4 p1 = u32x4{pk(p[8], p[9]), pk(p[10], p[11]), pk(p[12], p[13]), pk(p[14], p[15])};
            return mfma32(u32x4{t2[0], t2[1], t3[0], t3[1]}, p1, mfma32(u32x4{t0[0], t0[1], t1[0], t1[1]}, p0, o));
        };
        const float NINF = -__builtin_inff();
        f32x16 z16;
#pragma unroll
        for (int r = 0; r < 16; ++r) z16[r] = 0.f;
        f32x16 o, sd;
        float l, m;
        bool redo;
        if (ai >= 2) {
            u32x4 km[2]; u32x2 kr[2], vt[2][4];
            auto ldK = [&](int bf, int t) { const unsigned oo = toff(t); km[bf] = lds128(kA + oo); kr[bf] = lds64(kB + oo); };
            auto ldV = [&](int bf, int t) {
                const unsigned va = vA + (toff(t) & vsel);
                vt[bf][0] = ldstr(va); vt[bf][1] = ldstr(va + 128); vt[bf][2] = ldstr(va + 256); vt[bf][3] = ldstr(va + 384);
            };
            auto ex8 = [&](const f32x16& s, int h8) {
                return u32x4{pk(__builtin_amdgcn_exp2f(s[h8]), __builtin_amdgcn_exp2f(s[h8 + 1])), pk(__builtin_amdgcn_exp2f(s[h8 + 2]), __builtin_amdgcn_exp2f(s[h8 + 3])),
                             pk(__builtin_amdgcn_exp2f(s[h8 + 4]), __builtin_amdgcn_exp2f(s[h8 + 5])), pk(__builtin_amdgcn_exp2f(s[h8 + 6]), __builtin_amdgcn_exp2f(s[h8 + 7]))};
            };
            ldK(0, t_last); ldK(1, qt); ldV(0, t_last); ldV(1, qt);
            sd = mfma32k8(kr[0], qr, mfma32(km[0], qm, z16));
            SB0();
            ldK(0, qt + 1);
            m = NINF;
#pragma unroll
            for (int r = 0; r < 16; ++r) { const int c = (r & 3) + 8 * (r >> 2); sd[r] = c <= qq ? sd[r] : NINF; m = fmaxf(m, sd[r]); }
            m = fmaxf(m, __shfl_xor(m, 32));
            f32x16 nm, s[2], sm;
#pragma unroll
            for (int r = 0; r < 16; ++r) { nm[r] = -m; sm[r] = sd[r] - m; }
            s[0] = mfma32(km[1], qm, nm);
            SB0();
            {
                const u32x4 p0 = ex8(sm, 0);
                SB0();
                s[0] = mfma32k8(kr[1], qr, s[0]);
                o = mfma32(u32x4{vt[0][0][0], vt[0][0][1], vt[0][1][0], vt[0][1][1]}, p0, z16);
                SB0();
                const u32x4 p1 = ex8(sm, 8);
                SB0();
                o = mfma32(u32x4{vt[0][2][0], vt[0][2][1], vt[0][3][0], vt[0][3][1]}, p1, o);
            }
#pragma unroll
            for (int i = 0; i < 8; ++i) {
                const int kb2 = i & 1, vb = (i + 1) & 1;
                SB0();
                if (i < 7) s[(i + 1) & 1] = mfma32(km[kb2], qm, nm);
                SB0();
                f32x16 sc = s[i & 1];
                if (i == 0) {
#pragma unroll
                    for (int r = 0; r < 16; ++r) { const int c = (r & 3) + 8 * (r >> 2); sc[r] = c > qq ? sc[r] : NINF; }
                }
                const u32x4 p0 = ex8(sc, 0);
                SB0();
                if (i < 7) s[(i + 1) & 1] = mfma32k8(kr[kb2], qr, s[(i + 1) & 1]);
                if (i < 6) ldK(kb2 ^ 1, qt + i + 2);
                if (i < 7) ldV(vb ^ 1, qt + i + 1);
                SB0();
                o = mfma32(u32x4{vt[vb][0][0], vt[vb][0][1], vt[vb][1][0], vt[vb][1][1]}, p0, o);
                SB0();
                const u32x4 p1 = ex8(sc, 8);
                SB0();
                o = mfma32(u32x4{vt[vb][2][0], vt[vb][2][1], vt[vb][3][0], vt[vb][3][1]}, p1, o);
                SB0();
                if (i == 2) { ypublish(); q_prefetch(); }
                if (i >= 3) { wo_dma(2 * (i - 3)); wo_dma(2 * (i - 3) + 1); }
            }
            wo_dma(10);
            l = o[12];
            redo = !__all(l <= 1048576.f);
        } else {
            ypublish(); q_prefetch();
            for (int k = 0; k < 11; ++k) wo_dma(k);
            sd = qk(t_last, z16);
            m = NINF;
#pragma unroll
            for (int r = 0; r < 16; ++r) { const int c = (r & 3) + 8 * (r >> 2); sd[r] = c <= qq ? sd[r] : NINF; m = fmaxf(m, sd[r]); }
            m = fmaxf(m, __shfl_xor(m, 32));
            redo = true;
        }
        if (redo) {
            for (int attempt = ai >= 2 ? 1 : 0; attempt < 2; ++attempt) {
                if (attempt == 1) {
                    for (int t = t_first; t < t_last; ++t) {
                        f32x16 s = qk(t, z16);
#pragma unroll
                        for (int r = 0; r < 16; ++r) { const int c = (r & 3) + 8 * (r >> 2); const bool keep = !(has_first && t == t_first) || c > qq; m = fmaxf(m, keep ? s[r] : NINF); }
                    }
                    m = fmaxf(m, __shfl_xor(m, 32));
                }
                f32x16 nm, p;
#pragma unroll
                for (int r = 0; r < 16; ++r) { nm[r] = -m; p[r] = __builtin_amdgcn_exp2f(sd[r] - m); }
                o = pv(t_last, p, z16);
                for (int t = t_first; t < t_last; ++t) {
                    f32x16 s = qk(t, nm);
                    if (has_first && t == t_first) {
#pragma unroll
                        for (int r = 0; r < 16; ++r) { const int c = (r & 3) + 8 * (r >> 2); s[r] = c > qq ? s[r] : NINF; }
                    }
#pragma unroll
                    for (int r = 0; r < 16; ++r) p[r] = __builtin_amdgcn_exp2f(s[r]);
                    o = pv(t, p, o);
                }
                l = o[12];
                if (__all(l <= 1048576.f)) break;
            }
        }
        STAMP(15);
        const float il = 1.f / l;
        const int T16 = 8 * ai + 2 * qt + (l31 >> 4);
        const u32x4 om = u32x4{pk(o[0] * il, o[1] * il), pk(o[2] * il, o[3] * il), pk(o[4] * il, o[5] * il), pk(o[6] * il, o[7] * il)};
        __builtin_amdgcn_raw_buffer_store_b128(om, wsr, WS_OM + ((T16 * 16 + gp) * 64 + (2 * hd + hh) * 16 + (l31 & 15)) * 16, 0, 16);
        const unsigned ra = pk(o[8] * il, o[9] * il), rb2 = pk(o[10] * il, o[11] * il), rv = hd ? rb2 : ra;
        const u32x4 rq = u32x4{rv, (unsigned)__builtin_amdgcn_mov_dpp(rv, 0x55, 0xF, 0xF, true), (unsigned)__builtin_amdgcn_mov_dpp(rv, 0xAA, 0xF, 0xF, true), (unsigned)__builtin_amdgcn_mov_dpp(rv, 0xFF, 0xF, 0xF, true)};
        if (hh == 0 && (l31 & 3) == 0) __builtin_amdgcn_raw_buffer_store_b128(rq, wsr, WS_OR + ((((4 * ai + qt) * 16 + gp) * 2 + hd) * 32 + l31) * 4, 0, 16);
    }
    {
        GAS u64* f = mb(wg_tile(2 * ai + (lane & 1), (lane >> 1) & 7), MB_O + gp * 8);
        WAITVM(0);
        const bool last = __builtin_amdgcn_readfirstlane(arrive(L0 + SY_ODR, 8, lane) ? 1 : 0);
        if (last && lane < 16) __hip_atomic_store(f, nonce, RLX);
    }
    STAMP(20);
    if (w < 4 || w == 7) {
        poll_idx(mb(b, MB_Y), lane < 36 ? lane : -1, nonce);
        if (w < 4 || lane < 32) {
            const int chB = 72 * bc + 2 * cp, t0 = 64 * br + 8 * oct;
            const u32x4 y0 = __builtin_amdgcn_raw_buffer_load_b128(wsr, WS_Y + chB * 4096 + t0 * 2, 0, 16);
            const u32x4 y1 = __builtin_amdgcn_raw_buffer_load_b128(wsr, WS_Y + (chB + 1) * 4096 + t0 * 2, 0, 16);
            const float D0 = blo(dv), D1 = bhi(dv);
            unsigned zz[8];
#pragma unroll
            for (int t = 0; t < 8; ++t) {
                const int slot = (t & 1) * 4 + (t >> 1);
                const float ya = (slot & 1) ? bhi(y0[slot >> 1]) : blo(y0[slot >> 1]);
                const float yb = (slot & 1) ? bhi(y1[slot >> 1]) : blo(y1[slot >> 1]);
                const int j = 7 - t;
                const float ka = (j & 1) ? bhi(kv0[j >> 1]) : blo(kv0[j >> 1]);
                const float kb = (j & 1) ? bhi(kv1[j >> 1]) : blo(kv1[j >> 1]);
                const unsigned kd = pk(ka * D0, kb * D1);
                zz[t] = pk(ya + blo(kd), yb + bhi(kd));
            }
            spin4(L0 + SY_QRD, 1); spin4(L0 + SY_QRD + 16, 1);
#pragma unroll
            for (int t = 0; t < 8; ++t) st32(L0 + L_Z + oct * 1184 + t * 144 + cp * 4, zz[t]);
        }
    } else if (w == 4) {
        poll_idx(mb(b, MB_O), lane < 16 ? lane : -1, nonce);
    }
    STAMP(21);
    BAR();
    STAMP(22);
    {
        const int tt = w & 3, kh = w >> 2, T16 = 4 * br + tt;
        u32x4 of[9];
#pragma unroll
        for (int s = 0; s < 8; ++s) of[s] = __builtin_amdgcn_raw_buffer_load_b128(wsr, WS_OM + ((T16 * 16 + 8 * kh + s) * 64 + lane) * 16, 0, 16);
#pragma unroll
        for (int d = 0; d < 4; ++d) of[8][d] = __builtin_amdgcn_raw_buffer_load_b32(wsr, WS_OR + ((((T16 >> 1) * 16 + 8 * kh + 2 * g4 + (d >> 1)) * 2 + (d & 1)) * 32 + 16 * (T16 & 1) + l15) * 4, 0, 16);
        SB0();
        {
            auto gate = [&](const u32x4& qv, int tk, int gr) {
                const u32x4 z = lds128(L0 + L_Z + tk * 144 + (tk >> 3) * 32 + gr * 16);
                u32x4 r;
#pragma unroll
                for (int d = 0; d < 4; ++d) r[d] = pk(blo(qv[d]) * blo(z[d]), bhi(qv[d]) * bhi(z[d]));
                __builtin_amdgcn_raw_buffer_store_b128(r, outr, (64 * br + tk) * 2304 + 1152 + bc * 144 + gr * 16, 0, OUTAUX);
            };
            gate(qg0, tk0, gr0);
            if (w == 5) gate(qg1, tk1, gr1);
        }
        f32x4 acc[5];
#pragma unroll
        for (int j = 0; j < 5; ++j) acc[j] = f32x4{0, 0, 0, 0};
        const int q4 = (lane >> 2) & 3, p4 = lane & 3, hd = g4 >> 1, hq = g4 & 1;
        const unsigned mlane = L0 + (20 * hd + 4 * hq + q4) * 144 + p4 * 8;
        const unsigned mlane4 = L0 + (20 * hd + 4 * hq + q4) * 144 + 128 + (p4 & 1) * 8;
        const int gr = 8 * kh + 2 * g4;
        const unsigned rlane = L0 + (gr < 6 ? L_WO1 + gr * 5184 : L_WO2 + (gr - 6) * 5184) + (16 + q4) * 144;
        u32x2 fa[2][5], fc[2][5];
        auto ldw = [&](int bf, int s) {
            if (s < 8) {
                const int gq = 8 * kh + s;
                const unsigned gb = gq < 6 ? L_WO1 + gq * 5184 : L_WO2 + (gq - 6) * 5184;
                const unsigned m0 = mlane + gb, m4 = mlane4 + gb;
#pragma unroll
                for (int j = 0; j < 4; ++j) { fa[bf][j] = ldstr(m0 + j * 32); fc[bf][j] = ldstr(m0 + j * 32 + 8 * 144); }
                fa[bf][4] = ldstr(m4); fc[bf][4] = ldstr(m4 + 8 * 144);
            } else {
#pragma unroll
                for (int j = 0; j < 5; ++j) {
                    const unsigned ad = rlane + (j < 4 ? j * 32 + p4 * 8 : 128 + (p4 & 1) * 8);
                    fa[bf][j] = ldstr(ad); fc[bf][j] = ldstr(ad + 5184);
                }
            }
        };
        ldw(0, 0);
#pragma unroll
        for (int s = 0; s < 9; ++s) {
            SB0();
            if (s < 8) ldw((s + 1) & 1, s + 1);
            SB0();
#pragma unroll
            for (int j = 0; j < 5; ++j) acc[j] = mfma16(u32x4{fa[s & 1][j][0], fa[s & 1][j][1], fc[s & 1][j][0], fc[s & 1][j][1]}, of[s], acc[j]);
        }
        SB0();
        const unsigned sc = L0 + L_X + tt * 5120 + lane * 16;
        if (kh == 0) { stf4(sc + 2048, acc[2]); stf4(sc + 3072, acc[3]); }
        else { stf4(sc, acc[0]); stf4(sc + 1024, acc[1]); stf4(sc + 4096, acc[4]); }
        BAR();
        const int op = (16 * T16 + l15) * 2304 + bc * 144;
        auto st2 = [&](const f32x4& va, const f32x4& vb) {
            const unsigned a0 = pk(va[0], va[1]), a1 = pk(va[2], va[3]), b0 = pk(vb[0], vb[1]), b1 = pk(vb[2], vb[3]);
            auto s0 = __builtin_amdgcn_permlane16_swap(a0, b0, false, false);
            auto s1 = __builtin_amdgcn_permlane16_swap(a1, b1, false, false);
            return u32x4{s0[0], s1[0], s0[1], s1[1]};
        };
        if (kh == 0) {
            const f32x4 v0 = acc[0] + ldsf4(sc), v1 = acc[1] + ldsf4(sc + 1024), v4 = acc[4] + ldsf4(sc + 4096);
            __builtin_amdgcn_raw_buffer_store_b128(st2(v0, v1), outr, op + (g4 & 1) * 32 + (g4 >> 1) * 16, 0, OUTAUX);
            const u32x4 r4 = st2(v4, v4);
            if (g4 == 0) __builtin_amdgcn_raw_buffer_store_b128(r4, outr, op + 128, 0, OUTAUX);
        } else {
            const f32x4 v2 = acc[2] + ldsf4(sc + 2048), v3 = acc[3] + ldsf4(sc + 3072);
            __builtin_amdgcn_raw_buffer_store_b128(st2(v2, v3), outr, op + 64 + (g4 & 1) * 32 + (g4 >> 1) * 16, 0, OUTAUX);
        }
    }
    STAMP(23);
    if (b == 0 && w == 7) {
        for (int k = 0; k < 4; ++k) poll_idx((const GAS u64*)(ws + WS_ARR), 64 * k + lane, nonce);
        if (lane == 0) __hip_atomic_store((GAS u64*)ws, nonce, RLX);
    }
}

extern "C" void kernel_launch(void* const* d_in, const int* in_sizes, int n_in, void* d_out, int out_size, void* d_ws, size_t ws_size, hipStream_t stream) {
    fused_kernel<<<256, 512, 0, stream>>>((const unsigned short*)d_in[0], (const unsigned short*)d_in[1], (const unsigned short*)d_in[2], (const unsigned short*)d_in[3],
                                          (const unsigned short*)d_in[4], (const unsigned short*)d_in[5], (const unsigned short*)d_in[6], (const unsigned short*)d_in[7],
                                          (const unsigned short*)d_in[8], (unsigned short*)d_out, (char*)d_ws);
}
```

```cpp
#include <hip/hip_runtime.h>
#include <stdint.h>

#define GAS __attribute__((address_space(1)))
#define LAS __attribute__((address_space(3)))
#define DEVI __device__ __forceinline__
typedef unsigned long long u64;
typedef __attribute__((ext_vector_type(8))) __bf16 bf16x8;
typedef __attribute__((ext_vector_type(2))) __bf16 bf16x2;
typedef __attribute__((ext_vector_type(2))) float f32x2;
typedef __attribute__((ext_vector_type(4))) float f32x4;
typedef __attribute__((ext_vector_type(16))) float f32x16;
typedef __attribute__((ext_vector_type(4))) unsigned u32x4;
typedef __attribute__((ext_vector_type(2))) unsigned u32x2;
typedef __attribute__((ext_vector_type(3))) unsigned u32x3;
typedef __attribute__((ext_vector_type(4))) short s16x4;

constexpr int WS_ARR = 256;
constexpr int WS_R0 = 32768;
constexpr int WS_QKV = WS_R0 + 2359296;
constexpr int WS_OM = WS_QKV + 7077888;
constexpr int WS_OR = WS_OM + 2097152;
constexpr int WS_Y = WS_OR + 262144;
constexpr int WS_MB = WS_Y + 2359296;
constexpr int MBS = 4352, MB_Q = 0, MB_O = 128, MB_K = 256, MB_Y = 512;
constexpr int WS_END = WS_MB + 256 * MBS;
constexpr int WS_TS = 32 << 20;
constexpr int B1P = 1056, B1_V = 9 * B1P, B1_S = 17 * B1P, B1_O = B1_S + 512;
constexpr int L_DUMMY = 157952;
#define PF 1
constexpr int PFN = 1;
constexpr int L_B1A = 131072, L_B1B = 99328;
constexpr int KCS = 4160, KCH = 4 * KCS;
constexpr int RCS = 4576, RCH = 2 * RCS;
constexpr int L_KB = 32768, L_RB = 131072;
constexpr int L_KA = 66048, L_RA = 0;
constexpr int L_SCR = 114688;
constexpr int BLK = 10752, PCS = 2112;
constexpr int L_KBLK = 0, L_VBLK = 66048, L_QBLK = 66048 + 3 * BLK;
constexpr int L_WO1 = 32768, L_WO2 = 109056;
constexpr int L_ONES = 161280;
constexpr int L_Z = L_QBLK;
constexpr int L_X = 0;
constexpr int L_SYNC = -256;
constexpr int SY_LAND = L_SYNC, SY_CONS = L_SYNC + 16, SY_PW = L_SYNC + 32, SY_PR = L_SYNC + 48, SY_PREP = L_SYNC + 160, SY_ISS = L_SYNC + 68, SY_YDR = L_SYNC + 72,
              SY_ODR = L_SYNC + 76, SY_NONCE = L_SYNC + 80, SY_QDR = L_SYNC + 96, SY_QRD = L_SYNC + 112, SY_QSEEN = L_SYNC + 144, SY_NRDY = L_SYNC + 148, SY_BL = L_SYNC + 152,
              SY_B1B = L_SYNC + 156, SY_B1 = L_SYNC + 176;
constexpr int LDS_BYTES = 163840;
constexpr unsigned SPIN_CAP = 1u << 17, LSPIN_CAP = 1u << 20;

#define STAMPW(k) do { } while (0)
#define STAMPC(k) do { } while (0)
#define STAMP(k) do { } while (0)

DEVI f32x4 mfma16(u32x4 a, u32x4 b, f32x4 c) { return __builtin_amdgcn_mfma_f32_16x16x32_bf16(__builtin_bit_cast(bf16x8, a), __builtin_bit_cast(bf16x8, b), c, 0, 0, 0); }
DEVI f32x16 mfma32(u32x4 a, u32x4 b, f32x16 c) { return __builtin_amdgcn_mfma_f32_32x32x16_bf16(__builtin_bit_cast(bf16x8, a), __builtin_bit_cast(bf16x8, b), c, 0, 0, 0); }
DEVI f32x16 mfma32k8(u32x2 a, u32x2 b, f32x16 c) { return __builtin_amdgcn_mfma_f32_32x32x8bf16_1k(__builtin_bit_cast(s16x4, a), __builtin_bit_cast(s16x4, b), c, 0, 0, 0); }
DEVI unsigned pk(float lo, float hi) { f32x2 v = {lo, hi}; return __builtin_bit_cast(unsigned, __builtin_convertvector(v, bf16x2)); }
DEVI float blo(unsigned v) { return __builtin_bit_cast(float, v << 16); }
DEVI float bhi(unsigned v) { return __builtin_bit_cast(float, v & 0xffff0000u); }
DEVI u32x4 lds128(unsigned a) { return *(LAS const u32x4*)(uintptr_t)a; }
DEVI u32x2 lds64(unsigned a) { return *(LAS const u32x2*)(uintptr_t)a; }
DEVI unsigned lds32(unsigned a) { return *(LAS const unsigned*)(uintptr_t)a; }
DEVI void st128(unsigned a, u32x4 v) { *(LAS u32x4*)(uintptr_t)a = v; }
DEVI void st64(unsigned a, u32x2 v) { *(LAS u32x2*)(uintptr_t)a = v; }
DEVI void st32(unsigned a, unsigned v) { *(LAS unsigned*)(uintptr_t)a = v; }
DEVI f32x4 ldsf4(unsigned a) { return *(LAS const f32x4*)(uintptr_t)a; }
DEVI void stf4(unsigned a, f32x4 v) { *(LAS f32x4*)(uintptr_t)a = v; }
DEVI u32x2 ldstr(unsigned a) { return __builtin_bit_cast(u32x2, __builtin_amdgcn_ds_read_tr16_b64_v4i16((LAS s16x4*)(uintptr_t)a)); }
DEVI u32x4 vlds128(unsigned a) { return *(volatile LAS const u32x4*)(uintptr_t)a; }
DEVI unsigned vlds32(unsigned a) { return *(volatile LAS const unsigned*)(uintptr_t)a; }
DEVI void vst32(unsigned a, unsigned v) { *(volatile LAS unsigned*)(uintptr_t)a = v; }
DEVI void dma16(const void* g, unsigned lds) {
    unsigned sv;
    asm volatile("s_mov_b32 %0, m0\n\ts_mov_b32 m0, %2\n\ts_nop 0\n\tglobal_load_lds_dwordx4 %1, off\n\ts_mov_b32 m0, %0" : "=&s"(sv) : "v"(g), "s"(lds) : "memory");
}
DEVI void touch(unsigned voff, const void* sbase, unsigned lds) {
    unsigned sv;
    asm volatile("s_mov_b32 %0, m0\n\ts_mov_b32 m0, %3\n\ts_nop 0\n\tglobal_load_lds_dword %1, %2\n\ts_mov_b32 m0, %0" : "=&s"(sv) : "v"(voff), "s"(sbase), "s"(lds) : "memory");
}
DEVI void dmaS(unsigned voff, const void* sbase, unsigned lds) {
    unsigned sv;
    asm volatile("s_mov_b32 %0, m0\n\ts_mov_b32 m0, %3\n\ts_nop 0\n\tglobal_load_lds_dwordx4 %1, %2\n\ts_mov_b32 m0, %0" : "=&s"(sv) : "v"(voff), "s"(sbase), "s"(lds) : "memory");
}
#define DPACE ""
#define DMA1(v, b, o) "s_add_u32 m0, %[sl], " o "\n\ts_nop 0\n\tglobal_load_lds_dwordx4 " v ", " b "\n\t" DPACE
DEVI void dmaA(const unsigned* vo, const void* xs, const void* b5, const void* wn, unsigned sld) {
    asm volatile(DMA1("%0", "%8", "0") DMA1("%1", "%8", "0xc00") DMA1("%2", "%8", "0x1800") DMA1("%3", "%8", "0x2400") DMA1("%4", "%8", "0x3000")
                 DMA1("%5", "%9", "0x3c00") DMA1("%6", "%10", "0x4800") DMA1("%7", "%10", "0x5400")
                 ::"v"(vo[0]), "v"(vo[1]), "v"(vo[2]), "v"(vo[3]), "v"(vo[4]), "v"(vo[5]), "v"(vo[6]), "v"(vo[7]), "s"(xs), "s"(b5), "s"(wn), [sl] "s"(sld) : "memory", "scc");
}
DEVI void dmaB(const unsigned* vo, const void* wn, const void* b10, unsigned sld, unsigned o10) {
    asm volatile(DMA1("%0", "%3", "0x6000") DMA1("%1", "%3", "0x6c00") DMA1("%2", "%4", "%5")
                 ::"v"(vo[8]), "v"(vo[9]), "v"(vo[10]), "s"(wn), "s"(b10), "s"(o10), [sl] "s"(sld) : "memory", "scc");
}
#define XCNT do { } while (0)
#define WAITVM(n) asm volatile("s_waitcnt vmcnt(%0)" ::"i"(n) : "memory")
#define BAR() do { asm volatile("s_waitcnt lgkmcnt(0)\n\ts_barrier" ::: "memory"); } while (0)
#define GST(k) do { } while (0)
#define LST(k) do { } while (0)
#define SBAR() do { asm volatile("s_barrier" ::: "memory"); } while (0)
#define EARLY2 0
#define BPRIO 2
#define CPF 1
#define TD 4
#define KVS 2
#define KP0 5
#define QDR_RR 0
#define PFS 4
#define CFENCE() asm volatile("" ::: "memory")
#define SB0() __builtin_amdgcn_sched_barrier(0)
#define RLX __ATOMIC_RELAXED, __HIP_MEMORY_SCOPE_AGENT
template <int V> struct IC { static constexpr int value = V; };
template <int N, int I = 0, class F> DEVI void static_for(F&& f) { if constexpr (I < N) { f(IC<I>{}); static_for<N, I + 1>(f); } }
DEVI unsigned rfl(unsigned v) { return __builtin_amdgcn_readfirstlane(v); }
DEVI unsigned umin4(u32x4 v) { const unsigned a = v[0] < v[1] ? v[0] : v[1], c = v[2] < v[3] ? v[2] : v[3]; return a < c ? a : c; }

template <int SLP = 1>
DEVI void spin4(unsigned a, unsigned need) {
#pragma nounroll
    for (unsigned s = 0; s < LSPIN_CAP; ++s) {
        if (__all(vlds32(a + (threadIdx.x & 3) * 4) >= need)) break;
        __builtin_amdgcn_s_sleep(SLP);
    }
    CFENCE();
}
DEVI void spin4v(unsigned addr, unsigned needv) {
#pragma nounroll
    for (unsigned s = 0; s < LSPIN_CAP; ++s) {
        if (__all(vlds32(addr) >= needv)) break;
        __builtin_amdgcn_s_sleep(1);
    }
    CFENCE();
}
DEVI void spin1(unsigned a, unsigned need) {
#pragma nounroll
    for (unsigned s = 0; s < LSPIN_CAP; ++s) {
        if (rfl(vlds32(a)) >= need) break;
        __builtin_amdgcn_s_sleep(1);
    }
    CFENCE();
}
DEVI void signal(unsigned a, unsigned v, int lane) { CFENCE(); if (lane == 0) vst32(a, v); CFENCE(); }
DEVI bool arrive(unsigned a, unsigned n, int lane) {
    CFENCE();
    unsigned old = 0;
    if (lane == 0) old = __hip_atomic_fetch_add((LAS unsigned*)(uintptr_t)a, 1u, __ATOMIC_RELAXED, __HIP_MEMORY_SCOPE_WORKGROUP);
    CFENCE();
    return lane == 0 && old == n - 1;
}
#define LSLP 2
#define CPRIO 0
#define TL 0
constexpr unsigned CU = 1;
#define LJ 2
#define ISSAT 26
#define GPRIO 3
#define OUTAUX 17
#define POLLS 2
DEVI void poll_idx(const GAS u64* f, int idx, u64 nonce) {
#pragma nounroll
    for (unsigned spins = 0; spins < SPIN_CAP; ++spins) {
        u64 v = nonce;
        if (idx >= 0) v = __hip_atomic_load(f + idx, RLX);
        if (__all(v == nonce)) break;
        __builtin_amdgcn_s_sleep(POLLS);
    }
    __builtin_amdgcn_fence(__ATOMIC_ACQUIRE, "wavefront");
    CFENCE();
}

DEVI int wg_attn(int ai, int gp) { return (30 - 2 * ai + (gp & 1)) * 8 + (gp >> 1); }
DEVI int wg_pair(int p) { return p < 224 ? (p % 28) * 8 + p / 28 : (28 + (((p - 224) >> 1) & 3)) * 8 + ((p - 224) >> 3); }
DEVI int wg_tile(int br, int bc) { return (4 * bc + (br & 3)) * 8 + (br >> 2); }

__device__ constexpr int CMU[36] = {0,0,0,0,1,1,1,1,2,2,2,0,3,3,3,2,4,4,1,0,5,5,4,3,6,2,1,0,7,6,5,4,3,2,1,0};
__device__ constexpr int CNU[36] = {7,6,5,4,6,5,4,3,5,4,3,3,4,3,2,2,3,2,2,2,2,1,1,1,1,1,1,1,0,0,0,0,0,0,0,0};

__global__ __launch_bounds__(512, 2) void fused_kernel(const unsigned short* __restrict__ x, const unsigned short* __restrict__ wqkv, const unsigned short* __restrict__ wo,
                                                       const unsigned short* __restrict__ bq, const unsigned short* __restrict__ bk, const unsigned short* __restrict__ bv,
                                                       const unsigned short* __restrict__ shk, const unsigned short* __restrict__ ssk, const unsigned short* __restrict__ ssd,
                                                       unsigned short* __restrict__ out, char* __restrict__ ws) {
    __shared__ __attribute__((aligned(256))) char smem[LDS_BYTES];
    const unsigned L0 = (unsigned)(uintptr_t)(LAS char*)smem + 256;
    const int tid = threadIdx.x, lane = tid & 63, w = __builtin_amdgcn_readfirstlane(tid >> 6);
    const int b = blockIdx.x, xcd = b & 7, jj = b >> 3;
    const bool busy = jj < 28;
    const int ri = 4 * (xcd >> 1) + (jj & 3), cj = 7 * (xcd & 1) + (jj >> 2);
    const int br = 4 * xcd + (jj & 3), bc = jj >> 2;
    const int ai = (31 - jj) >> 1, gp = 2 * xcd + (jj & 1);
    const int pairB = busy ? xcd * 28 + jj : 225 + 2 * (xcd * 4 + jj - 28);
    const int l15 = lane & 15, g4 = lane >> 4, l31 = lane & 31, hh = lane >> 5;
    const __amdgpu_buffer_rsrc_t wsr = __builtin_amdgcn_make_buffer_rsrc(ws, 0, WS_END, 0x00020000);
    const __amdgpu_buffer_rsrc_t outr = __builtin_amdgcn_make_buffer_rsrc(out, 0, 2048 * 2304, 0x00020000);
    STAMP(0);
    {
        const char* pc; unsigned len;
        asm volatile("s_getpc_b64 %0\n.Lcpf_here:\n\ts_mov_b32 %1, .Lcpf_end-.Lcpf_here" : "=s"(pc), "=s"(len));
        const unsigned o = (jj * 16 + lane) * 128;
        if (w == 0 && lane < 16 && o < len) touch(o, pc, L0 + L_DUMMY);
    }
    asm volatile("" ::"s"(x), "s"(wqkv), "s"(wo), "s"(bq), "s"(bk), "s"(bv), "s"(shk), "s"(ssk), "s"(ssd), "s"(out), "s"(ws));
    const int tileA = busy ? xcd * 28 + jj : 224 + 2 * (xcd * 4 + jj - 28);
    {
        const unsigned b1v = (lane >> 3) * 1152 + (lane & 7) * 16, shv = l31 * 16;
#pragma nounroll
        for (int pp = w; pp < (busy ? 18 : 36); pp += 8) {
            const int second = pp >= 18, p = pp - 18 * second, T = tileA + second, tb = T & 31, cb = T >> 5;
            int row = 64 * tb + 8 * p - (p < 9 ? 8 : 72);
            row = row < 0 ? 0 : row;
            const char* base = p < 17 ? (const char*)(p < 9 ? bk : bv) + row * 1152 + cb * 128 : (const char*)shk + cb * 512;
            dmaS(p < 17 ? b1v : shv, base, L0 + (second ? L_B1B : L_B1A) + p * B1P);
        }
    }
    int ntouch = 0;
    if (w < 4) {
        int stp = 0;
#pragma nounroll
        for (int q = w; q < 7; q += 4) {
            const int n = jj + 32 * q, st = n / 22, r = n - 22 * st;
            if (n < 198) {
                const int idx = (r - 8) * 64 + lane, kr = idx / 14, c = idx - 14 * kr;
                const unsigned vx = (512 * (xcd >> 1) + 64 * r + lane) * 1152 + st * 128;
                const unsigned vw = (64 * st + kr) * 3456 + (xcd & 1) * 1792 + ((xcd & 1) && c == 13 ? 12 : c) * 128;
#pragma nounroll
                for (; stp < st; ++stp) __builtin_amdgcn_s_sleep(TD);
                touch(r < 8 ? vx : vw, r < 8 ? (const char*)x : (const char*)wqkv, L0 + L_DUMMY);
                ++ntouch;
            }
        }
    }
    auto b1_pass = [&](int T, unsigned lb, int it) {
        const int oct = lane & 7, cp = 8 * it + (lane >> 3);
        const unsigned kb = lb + oct * B1P + cp * 4;
        const u32x4 shv = lds128(lb + B1_S + cp * 16);
        unsigned vv[8], kvp[8];
        f32x2 k[11];
#pragma unroll
        for (int q = 0; q < 11; ++q) {
            const unsigned v = lds32(kb + ((5 + q) >> 3) * B1P + ((5 + q) & 7) * 128);
            k[q] = f32x2{blo(v), bhi(v)};
        }
#pragma unroll
        for (int t = 0; t < 8; ++t) vv[t] = lds32(kb + B1_V + t * 128);
        if ((T & 31) == 0 && oct == 0) { k[0] = f32x2{0.f, 0.f}; k[1] = k[0]; k[2] = k[0]; }
        const f32x2 s0 = {blo(shv[0]), blo(shv[2])}, s1 = {bhi(shv[0]), bhi(shv[2])}, s2 = {blo(shv[1]), blo(shv[3])}, s3 = {bhi(shv[1]), bhi(shv[3])};
#pragma unroll
        for (int t = 0; t < 8; ++t) {
            const unsigned v = vv[t];
            f32x2 a = k[t] * s3;
            a = __builtin_elementwise_fma(k[t + 1], s2, a);
            a = __builtin_elementwise_fma(k[t + 2], s1, a);
            a = __builtin_elementwise_fma(k[t + 3], s0, a);
            const unsigned ks = pk(a[0], a[1]);
            const f32x2 kv2 = f32x2{blo(ks), bhi(ks)} * f32x2{blo(v), bhi(v)};
            kvp[t] = pk(kv2[0], kv2[1]);
        }
        u32x4 r0, r1;
#pragma unroll
        for (int dd = 0; dd < 4; ++dd) {
            r0[dd] = __builtin_amdgcn_perm(kvp[6 - 2 * dd], kvp[7 - 2 * dd], 0x05040100u);
            r1[dd] = __builtin_amdgcn_perm(kvp[6 - 2 * dd], kvp[7 - 2 * dd], 0x07060302u);
        }
        const int off = WS_R0 + (64 * (T >> 5) + 2 * cp) * 4096 + (2040 - 64 * (T & 31) - 8 * oct) * 2;
        __builtin_amdgcn_raw_buffer_store_b128(r0, wsr, off, 0, 16);
        __builtin_amdgcn_raw_buffer_store_b128(r1, wsr, off + 4096, 0, 16);
    };
    auto mb = [&](int wg, int off) { return (GAS u64*)(ws + WS_MB + wg * MBS + off); };
    auto get_nonce = [&]() {
        const u32x2 v = *(volatile LAS const u32x2*)(uintptr_t)(L0 + SY_NONCE);
        return ((u64)rfl(v[1]) << 32) | rfl(v[0]);
    };
    auto gather = [&](int sw, u64 nonce) {
        if (!busy) spin4(L0 + SY_CONS, 1);
        if (sw == 0) {
            const int j = lane >> 1, rr = j < 6 ? ai - 2 + (j < 3 ? j : j - 3) : ai;
            poll_idx(mb(b, MB_Q), (lane < 14 && rr >= 0) ? lane : -1, nonce);
            signal(L0 + SY_QSEEN, 1, lane);
        } else {
            spin1(L0 + SY_QSEEN, 1);
        }
        STAMPW(24 + sw);
        u32x4 g[18];
        const int p0 = (36 * gp - 4 * (gp & 1)) >> 3;
#pragma unroll
        for (int k = 0; k < 18; ++k) {
            const int u = sw + 4 * k, j = u / 10, e = u - 10 * j;
            const int sec = j < 3 ? 1 : (j < 6 ? 2 : 0), rr = j < 6 ? ai - 2 + (j < 3 ? j : j - 3) : ai;
            if (u < 70 && rr >= 0) g[k] = __builtin_amdgcn_raw_buffer_load_b128(wsr, lane * 16, WS_QKV + ((rr * 216 + 72 * sec + p0) * 128) * 16 + e * 1024, 16);
        }
#pragma unroll
        for (int k = 0; k < 18; ++k) {
            const int u = sw + 4 * k, j = u / 10, e = u - 10 * j;
            const int rr = j < 6 ? ai - 2 + (j < 3 ? j : j - 3) : ai;
            const unsigned dst = L0 + (j < 3 ? L_KBLK + j * BLK : L_VBLK + (j - 3) * BLK) + (e >> 1) * PCS + (e & 1) * 1024 + lane * 16;
            if (u < 70 && rr >= 0) st128(dst, g[k]);
        }
    };

    if (w < 4) {
        if (w == 0) {
            if (lane < 48) vst32(L0 + L_SYNC + lane * 4, 0u);
        } else if (w == 1) {
#pragma unroll
            for (int i = 0; i < 3; ++i) { const int q = i * 64 + lane; if (q < 140) st128(L0 + L_ONES + q * 16, u32x4{0x3f803f80u, 0x3f803f80u, 0x3f803f80u, 0x3f803f80u}); }
        }
        const int wt = w & 1, wc = w >> 1;
        unsigned xo[4], wof[2];
#pragma unroll
        for (int j = 0; j < 4; ++j) { xo[j] = (64 * wt + l31) * 128 + (((2 * j + hh) ^ ((l31 >> 1) & 7)) * 16); asm volatile("" : "+v"(xo[j])); }
        {
            const int q = (lane >> 2) & 3, p = lane & 3;
#pragma unroll
            for (int ct = 0; ct < 2; ++ct) {
                wof[ct] = 16384 + (8 * hh + q) * 256 + (((8 * wc + 4 * ct + 2 * (g4 & 1) + (p >> 1)) ^ (q << 2)) * 16) + (p & 1) * 8;
                asm volatile("" : "+v"(wof[ct]));
            }
        }
        f32x16 acc[2][2];
#pragma unroll
        for (int r = 0; r < 16; ++r) { acc[0][0][r] = 0.f; acc[0][1][r] = 0.f; acc[1][0][r] = 0.f; acc[1][1][r] = 0.f; }
        asm volatile("" : "+v"(acc[0][0]), "+v"(acc[0][1]), "+v"(acc[1][0]), "+v"(acc[1][1]));
        if (ntouch == 2) WAITVM(2); else WAITVM(1);
        STAMP(28);
        BAR();
        STAMP(1);
        __builtin_amdgcn_s_setprio(BPRIO);
        b1_pass(tileA, L0 + L_B1A, w);
        STAMP(29);
        if (!busy) {
            WAITVM(0);
            BAR();
        } else {
            const bool live = !(cj == 13 && wc == 1);
            if (live) {
                u32x4 xf[2][2];
                u32x2 wa[2][2], wc2[2][2];
                auto adv = [&](unsigned& a) { a = (a + 32768u) & 0x1ffffu; asm volatile("" : "+v"(a)); };
                auto ldx = [&](int pb, int j, int tt) { xf[pb][tt] = lds128(L0 + xo[j] + tt * 4096); };
                auto ldwa = [&](int j, int pb, int ct) { wa[pb][ct] = ldstr(L0 + wof[ct] + j * 4096); };
                auto ldwc = [&](int j, int pb, int ct) { wc2[pb][ct] = ldstr(L0 + wof[ct] + j * 4096 + 1024); };
                auto W = [&](int pb, int ct) { return u32x4{wa[pb][ct][0], wa[pb][ct][1], wc2[pb][ct][0], wc2[pb][ct][1]}; };
                BAR();
                STAMP(3); STAMPC(40);
                __builtin_amdgcn_s_setprio(CPRIO);
                ldx(0, 0, 0); ldwa(0, 0, 0); ldwc(0, 0, 0); ldx(0, 0, 1); ldwa(0, 0, 1); ldwc(0, 0, 1);
                adv(xo[0]);
#pragma nounroll
                for (int s = 0; s < 9; ++s) {
#pragma unroll
                    for (int j = 0; j < 4; ++j) {
                        const int pb = j & 1, nb = pb ^ 1, nj = (j + 1) & 3;
                        SB0();
                        acc[0][0] = mfma32(W(pb, 0), xf[pb][0], acc[0][0]);
                        SB0();
                        if (j == 3) {
                            if (s == KVS) WAITVM(0);
                            BAR();
                        }
                        ldx(nb, nj, 0); ldwa(nj, nb, 0); ldwc(nj, nb, 0);
                        SB0();
                        acc[0][1] = mfma32(W(pb, 0), xf[pb][1], acc[0][1]);
                        SB0();
                        ldx(nb, nj, 1); ldwa(nj, nb, 1);
                        adv(xo[nj]);
                        SB0();
                        acc[1][0] = mfma32(W(pb, 1), xf[pb][0], acc[1][0]);
                        SB0();
                        ldwc(nj, nb, 1);
                        if (j == 2) { adv(wof[0]); adv(wof[1]); }
                        SB0();
                        acc[1][1] = mfma32(W(pb, 1), xf[pb][1], acc[1][1]);
                    }
                }
                __builtin_amdgcn_s_setprio(0);
                STAMP(4); STAMPC(41);
#pragma unroll
                for (int ct = 0; ct < 2; ++ct) {
                    const int n0 = 128 * cj + 64 * wc + 32 * ct;
                    const float sc = n0 < 576 ? 0.34004634f : 1.0f;
#pragma unroll
                    for (int tt = 0; tt < 2; ++tt) {
#pragma unroll
                        for (int pi = 0; pi < 2; ++pi) {
                            const f32x16& a = acc[ct][tt];
                            const unsigned a0 = pk(a[8 * pi] * sc, a[8 * pi + 1] * sc), a1 = pk(a[8 * pi + 2] * sc, a[8 * pi + 3] * sc);
                            const unsigned b0 = pk(a[8 * pi + 4] * sc, a[8 * pi + 5] * sc), b1 = pk(a[8 * pi + 6] * sc, a[8 * pi + 7] * sc);
                            auto s0 = __builtin_amdgcn_permlane32_swap(a0, b0, false, false);
                            auto s1 = __builtin_amdgcn_permlane32_swap(a1, b1, false, false);
                            const int off = WS_QKV + ((ri * 216 + (n0 >> 3) + 2 * pi + hh) * 128 + 64 * wt + 32 * tt + l31) * 16;
                            __builtin_amdgcn_raw_buffer_store_b128(u32x4{s0[0], s1[0], s0[1], s1[1]}, wsr, off, 0, 16);
                        }
                    }
                }
            } else {
                BAR();
#pragma nounroll
                for (int s = 0; s < 9; ++s) { if (s == KVS) WAITVM(0); BAR(); }
            }
            STAMP(5);
        }
        bool qdone = !busy;
        auto qdrain = [&]() {
            WAITVM(0);
            signal(L0 + SY_QDR + w * 4, 1, lane);
            STAMP(8);
        };
        for (int pass = busy ? 1 : 0; pass < 2; ++pass) {
            if (!qdone && !__all(vlds32(L0 + SY_PREP + (lane & 3) * 4) >= (unsigned)(pass + 1))) { qdrain(); qdone = true; }
            spin4(L0 + SY_PREP, pass + 1);
            STAMP(6 + 3 * pass);
            const int slot = w >> 1, h = w & 1;
            const unsigned kb = L0 + (pass ? L_KB : L_KA) + slot * KCH, rb = L0 + (pass ? L_RB : L_RA) + slot * RCH;
            const int ch = 2 * (pairB - 1 + pass) + slot;
            const unsigned la = kb + ((l15 + 1) & 3) * KCS + (g4 + ((l15 + 1) >> 2)) * 16 + h * 256;
            const unsigned lb = rb + ((l15 & 1) ? 0 : RCS) + (28 - 4 * (l15 >> 1) + g4) * 16 + h * 256;
            f32x4 acc[8];
#pragma unroll
            for (int n = 0; n < 8; ++n) acc[n] = f32x4{0, 0, 0, 0};
            u32x4 A[2][8], B[2][8];
#pragma unroll
            for (int m = 0; m < 8; ++m) { A[0][m] = lds128(la + m * 512); B[0][m] = lds128(lb + (7 - m) * 512); }
#pragma unroll
            for (int rr = 0; rr < 4; ++rr) {
                const int cur = rr & 1, nx = cur ^ 1;
#pragma unroll
                for (int i = 0; i < 36; ++i) {
                    SB0();
                    acc[CMU[i] + CNU[i]] = mfma16(A[cur][CMU[i]], B[cur][CNU[i]], acc[CMU[i] + CNU[i]]);
                    SB0();
                    if (rr < 3 && (i & 1) == 0 && i < 32) {
                        const int m = i >> 2;
                        if (i & 2) B[nx][7 - m] = lds128(lb + (rr + 1) * 64 + m * 512);
                        else A[nx][m] = lds128(la + (rr + 1) * 64 + m * 512);
                    }
                }
                if (rr == QDR_RR && !qdone) { qdrain(); qdone = true; }
            }
            if (pass == 0) signal(L0 + SY_CONS + w * 4, 1, lane);
            const unsigned mine = L0 + L_SCR + w * 4096 + lane * 16, theirs = L0 + L_SCR + (w ^ 1) * 4096 + lane * 16;
            if (!busy && pass == 1) spin1(L0 + SY_PR + (w ^ 1) * 4, 1);
            f32x4 y[4];
            if (h) {
#pragma unroll
                for (int n = 0; n < 4; ++n) stf4(mine + n * 1024, acc[n]);
            } else {
#pragma unroll
                for (int n = 0; n < 4; ++n) stf4(mine + n * 1024, acc[4 + n]);
            }
            signal(L0 + SY_PW + w * 4, pass + 1, lane);
            spin1(L0 + SY_PW + (w ^ 1) * 4, pass + 1);
            if (h) {
#pragma unroll
                for (int n = 0; n < 4; ++n) y[n] = acc[4 + n] + ldsf4(theirs + n * 1024);
            } else {
#pragma unroll
                for (int n = 0; n < 4; ++n) y[n] = acc[n] + ldsf4(theirs + n * 1024);
            }
            signal(L0 + SY_PR + w * 4, pass + 1, lane);
            const bool odd = lane & 1;
#pragma unroll
            for (int i = 0; i < 4; i += 2) {
                const unsigned a0 = pk(y[i][0], y[i][1]), a1 = pk(y[i][2], y[i][3]), c0 = pk(y[i + 1][0], y[i + 1][1]), c1 = pk(y[i + 1][2], y[i + 1][3]);
                const unsigned r0 = __builtin_amdgcn_mov_dpp(odd ? a0 : c0, 0xB1, 0xF, 0xF, true), r1 = __builtin_amdgcn_mov_dpp(odd ? a1 : c1, 0xB1, 0xF, 0xF, true);
                const u32x4 v = odd ? u32x4{r0, r1, c0, c1} : u32x4{a0, a1, r0, r1};
                __builtin_amdgcn_raw_buffer_store_b128(v, wsr, WS_Y + ch * 4096 + (4 * h + i + (odd ? 1 : 0)) * 512 + ((l15 >> 1) * 4 + g4) * 16, 0, 16);
            }
            STAMP(7 + 3 * pass);
        }
    } else if (w < 7) {
        const int d = w - 4;
        u32x4 kd0[3];
        u32x3 kd1[3];
        auto kload = [&](int pair) {
#pragma unroll
            for (int i = 0; i < 3; ++i) {
                const int u = d + 3 * i < 8 ? d + 3 * i : 7, cq = (u & 3) * 64 + lane;
                const char* p = (const char*)ssk + (2 * pair + (u >> 2)) * 4096 + cq * 16;
                kd0[i] = *(const u32x4*)p; kd1[i] = *(const u32x3*)(p + (cq < 255 ? 16 : 0));
            }
        };
        auto kcopy = [&](unsigned kb0) {
#pragma unroll
            for (int i = 0; i < 3; ++i) {
                const int u = d + 3 * i;
                if (u < 8) {
                    const unsigned kb = kb0 + (u >> 2) * KCH, a = kb + (4 + (u & 3) * 64 + lane) * 16;
                    const u32x4 d0 = kd0[i]; const u32x3 d1 = kd1[i];
                    st128(a, d0);
                    st128(a + KCS, u32x4{d0[1], d0[2], d0[3], d1[0]});
                    st128(a + 2 * KCS, u32x4{d0[2], d0[3], d1[0], d1[1]});
                    st128(a + 3 * KCS, u32x4{d0[3], d1[0], d1[1], d1[2]});
                    if ((u & 3) == 0) {
                        if (lane == 0) {
                            st128(kb + 3 * 16, u32x4{0, 0, 0, 0});
                            st128(kb + KCS + 3 * 16, u32x4{0, 0, 0, d0[0]});
                            st128(kb + 2 * KCS + 3 * 16, u32x4{0, 0, d0[0], d0[1]});
                            st128(kb + 3 * KCS + 3 * 16, u32x4{0, d0[0], d0[1], d0[2]});
                        } else if (lane < 13) {
                            const int e = lane - 1;
                            st128(kb + (e / 3) * KCS + (e % 3) * 16, u32x4{0, 0, 0, 0});
                        }
                    }
                }
            }
        };
        if (busy) {
            int wch = (lane & 15) ^ ((lane >> 4) << 2);
            if (cj == 13) wch &= 7;
            const unsigned wv = (lane >> 4) * 3456 + wch * 16;
            const char* xb = (const char*)x + (size_t)(128 * ri) * 1152;
            const char* wb = (const char*)wqkv + cj * 256;
            unsigned vo[11];
            auto xoff = [&](int u) { return (unsigned)((lane >> 3) * 1152 + (((lane & 7) ^ (((u & 1) << 2) + (lane >> 4))) * 16) + u * 9216); };
            auto woff = [&](int u) { return wv + (u - 16) * 4 * 3456; };
            const unsigned m0_ = 0u - (unsigned)(d == 0), m2_ = 0u - (unsigned)(d == 2);
#pragma unroll
            for (int i = 0; i < 11; ++i) {
                const int u = d + 3 * i;
                vo[i] = i < 5 ? xoff(u) : (i == 5 ? (xoff(15) & m0_) | (woff(u) & ~m0_) : (i < 10 ? woff(u) : (xoff(2) & m2_) | (woff(u) & ~m2_)));
                asm volatile("" : "+v"(vo[i]));
            }
            const unsigned o10 = d < 2 ? 30720 : 0;
            auto issue = [&](int n) {
                const char* xs = xb + n * 128;
                const char* wn = wb + (size_t)n * (64 * 3456);
                dmaA(vo, xs, d == 0 ? xs : wn, wn, L0 + (n & 3) * 32768 + d * 1024);
                dmaB(vo, wn, d < 2 ? wn : xs, L0 + (n & 3) * 32768 + d * 1024, o10);
            };
            u64 lts = 0;
            LST(0);
            issue(0); LST(1);
            WAITVM(11);
            SBAR();
            issue(1); LST(2);
            issue(2); LST(3);
            WAITVM(22);
            LST(4);
            SBAR();
            issue(3);
            LST(5);
#pragma nounroll
            for (int s = 0; s < 5; ++s) {
                WAITVM(22);
                if (s == 0) LST(6);
                if (s == 1) LST(7);
                SBAR();
                issue(s + 4);
            }
            SB0();
            kload(pairB);
            SB0();
            WAITVM(28); SBAR();
            WAITVM(17); SBAR();
            WAITVM(6); SBAR();
            SBAR();
            CFENCE();
            kcopy(L0 + L_KB);
            signal(L0 + SY_PREP + d * 4, 2, lane);
            if (d == 1) {
                const int i = lane / 6, r = lane - 6 * i, e = r & 1, dd = r >> 1;
                const int G = (cj == 0 ? 0 : (128 * cj - 39) / 36) + i, c = 36 * G - 4 * (G & 1) + 39 * e;
                const int sec = G >> 4, a2 = ri + dd;
                const bool ok = i < 6 && G < 48 && (c >> 7) == cj && (sec == 0 ? dd == 0 : a2 < 16);
                const int slot = sec == 0 ? 12 + e : 2 * (2 - dd + (sec == 2 ? 3 : 0)) + e;
                GAS u64* f = mb(wg_attn(a2, G & 15), MB_Q + slot * 8);
                const u64 nonce = get_nonce();
                spin4(L0 + SY_QDR, 1);
                if (ok) __hip_atomic_store(f, nonce, RLX);
                STAMPW(2);
            }
        } else {
            WAITVM(0);
            SBAR();
            b1_pass(tileA + 1, L0 + L_B1B, d);
            kload(pairB - 1);
            WAITVM(0);
            SBAR();
            CFENCE();
            kcopy(L0 + L_KA);
            kload(pairB);
            signal(L0 + SY_PREP + d * 4, 1, lane);
            kcopy(L0 + L_KB);
            signal(L0 + SY_PREP + d * 4, 2, lane);
            __builtin_amdgcn_s_waitcnt(0x0F70);
        }
        gather(d, get_nonce());
    } else {
        const u64 ep = __hip_atomic_load((const GAS u64*)ws, RLX);
        WAITVM(0);
        u64 nonce = ep + 1;
        asm volatile("" : "+v"(nonce));
        SBAR();
        __builtin_amdgcn_s_setprio(GPRIO);
        touch(lane * 128, (const char*)ssk + (busy ? pairB : pairB - 1) * 8192, L0 + L_DUMMY);
        if (!busy) touch(lane * 128, (const char*)ssk + pairB * 8192, L0 + L_DUMMY);
        const int kt = tileA + hh;
        GAS u64* kf = mb(wg_pair(32 * (kt >> 5) + l31), MB_K + (kt & 31) * 8);
        auto kflags = [&]() {
            if (lane < 32 || !busy) __hip_atomic_store(kf, nonce, RLX);
            if (lane == 0) __hip_atomic_store((GAS u64*)(ws + WS_ARR) + b, nonce, RLX);
            STAMPW(11);
        };
        u32x4 rd[2][4]; unsigned rn[2][4];
        auto rload = [&](int pair) {
#pragma unroll
            for (int c = 0; c < 2; ++c)
#pragma unroll
                for (int r = 0; r < 4; ++r) {
                    const int cq = r * 64 + lane, off = WS_R0 + (2 * pair + c) * 4096 + cq * 16;
                    rd[c][r] = __builtin_amdgcn_raw_buffer_load_b128(wsr, off, 0, 16);
                    rn[c][r] = __builtin_amdgcn_raw_buffer_load_b32(wsr, off + (cq < 255 ? 16 : 0), 0, 16);
                }
        };
        auto rcopy = [&](unsigned rb0) {
#pragma unroll
            for (int c = 0; c < 2; ++c) {
                const unsigned rb = rb0 + c * RCH;
#pragma unroll
                for (int r = 0; r < 4; ++r) {
                    const int cq = r * 64 + lane;
                    const u32x4 dd = rd[c][r]; const unsigned nx = cq < 255 ? rn[c][r] : 0u;
                    st128(rb + cq * 16, dd);
                    st128(rb + RCS + cq * 16, u32x4{__builtin_amdgcn_alignbit(dd[1], dd[0], 16), __builtin_amdgcn_alignbit(dd[2], dd[1], 16), __builtin_amdgcn_alignbit(dd[3], dd[2], 16), __builtin_amdgcn_alignbit(nx, dd[3], 16)});
                }
                if (lane < 60) st128(rb + (lane >= 30 ? RCS : 0) + (256 + (lane >= 30 ? lane - 30 : lane)) * 16, u32x4{0, 0, 0, 0});
            }
        };
        if (!busy) b1_pass(tileA + 1, L0 + L_B1B, 3);
        if (lane == 0) *(volatile LAS u32x2*)(uintptr_t)(L0 + SY_NONCE) = u32x2{(unsigned)nonce, (unsigned)(nonce >> 32)};
        u64 gts = 0;
        if (busy) {
            SBAR();
            GST(47);
#pragma nounroll
            for (int s = 0; s <= KVS; ++s) { SBAR(); GST(48 + s); }
            kflags();
            const GAS u64* kp = mb(b, MB_K) + l31;
            int s = KVS + 1;
#pragma nounroll
            for (; s < KP0; ++s) { SBAR(); GST(48 + s); }
            bool got = false;
#pragma nounroll
            while (s < 7) {
                const u64 v = __hip_atomic_load(kp, RLX);
                SBAR(); GST(48 + s); SBAR(); GST(49 + s);
                s += 2;
                if (__all(v == nonce)) { got = true; break; }
            }
            if (!got) {
#pragma nounroll
                for (; s < 8; ++s) { SBAR(); GST(48 + s); }
                BAR();
                GST(56);
                s = 9;
                poll_idx(mb(b, MB_K), lane < 32 ? lane : -1, nonce);
            }
            __builtin_amdgcn_fence(__ATOMIC_ACQUIRE, "wavefront");
            STAMPW(12);
            rload(pairB);
            if (s < 9) {
#pragma nounroll
                for (; s < 8; ++s) { SBAR(); GST(48 + s); }
                BAR();
                GST(56);
            }
        } else {
            WAITVM(0);
            BAR();
            kflags();
            poll_idx(mb(b, MB_K), lane < 32 ? lane : -1, nonce);
            STAMPW(12);
            rload(pairB - 1); rcopy(L0 + L_RA); signal(L0 + SY_PREP + 12, 1, lane);
            rload(pairB);
            __builtin_amdgcn_s_waitcnt(0x0F70);
        }
        rcopy(L0 + L_RB);
        signal(L0 + SY_PREP + 12, 2, lane);
        __builtin_amdgcn_s_setprio(0);
        STAMPW(13);
        gather(3, nonce);
    }
    STAMPW(16 + w);
    BAR();
    STAMP(14);
    const u64 nonce = get_nonce();
    const unsigned wov = (lane / 9) * 1152 + (lane % 9) * 16;
    auto wo_dma = [&](int k) {
        const int u = w + 8 * k;
        if (u < 83) {
            const int row0 = u < 31 ? 7 * u : 216 + 7 * (u - 31);
            const unsigned dst = L0 + (u < 31 ? L_WO1 + u * 1008 : L_WO2 + (u - 31) * 1008);
            if (u < 82 || lane < 27) dmaS(wov, (const char*)wo + row0 * 1152 + bc * 144, dst);
        }
    };
    auto ypublish = [&]() {
        if (w < 4) {
            const int p = pairB - (busy ? 0 : hh), pc = p / 36;
            GAS u64* f = mb(wg_tile(l31, pc), MB_Y + (p - 36 * pc) * 8);
            WAITVM(0);
            const bool last = __builtin_amdgcn_readfirstlane(arrive(L0 + SY_YDR, 4, lane) ? 1 : 0);
            if (last && (lane < 32 || !busy)) __hip_atomic_store(f, nonce, RLX);
        }
    };
    const int id0 = tid, tk0 = id0 / 9, gr0 = id0 - 9 * tk0, id1 = 512 + lane, tk1 = id1 / 9, gr1 = id1 - 9 * tk1;
    u32x4 qg0, qg1 = u32x4{0, 0, 0, 0};
    const int bl = w < 4 ? tid : 256 + l31, cp = bl >> 3, oct = bl & 7;
    unsigned dv = 0;
    auto q_prefetch = [&]() {
        qg0 = *(const u32x4*)((const char*)bq + (64 * br + tk0) * 1152 + bc * 144 + gr0 * 16);
        if (w == 5) qg1 = *(const u32x4*)((const char*)bq + (64 * br + tk1) * 1152 + bc * 144 + gr1 * 16);
        if (w < 4 || w == 7) dv = *(const unsigned*)(ssd + 72 * bc + 2 * cp);
    };
    {
        const int hd = w >> 2, qt = w & 3, cs = 4 * (gp & 1);
        u32x4 qm = lds128(L0 + L_QBLK + (2 * hd + hh) * PCS + (32 * qt + l31) * 16);
        u32x2 qr = lds64(L0 + L_QBLK + (2 * hd + 2) * PCS + (32 * qt + l31) * 16 + hh * 8);
        signal(L0 + SY_QRD + w * 4, 1, lane);
        {
            const int lo = cs + 18 * hd;
#pragma unroll
            for (int d = 0; d < 4; ++d) { const int c = 8 * (2 * hd + hh) + 2 * d; qm[d] = (c >= lo && c < lo + 18) ? qm[d] : 0u; }
#pragma unroll
            for (int d = 0; d < 2; ++d) { const int c = 8 * (2 * hd + 2) + 4 * hh + 2 * d; qr[d] = (c >= lo && c < lo + 18) ? qr[d] : 0u; }
        }
        const unsigned kA = L0 + L_KBLK + (2 * hd + hh) * PCS + l31 * 16, kB = L0 + L_KBLK + (2 * hd + 2) * PCS + l31 * 16 + hh * 8;
        unsigned vA, vsel;
        {
            const int q = (lane >> 2) & 3, p = lane & 3;
            const bool ones = (g4 & 1) && p > 0;
            const int c = (g4 & 1) ? cs + 16 : cs + (hd ? 20 : 0) + 4 * p;
            vA = ones ? L0 + L_ONES + 64 * ((hd ? 1 : 3) + hh) : L0 + L_VBLK + (c >> 3) * PCS + (4 * hh + q) * 16 + ((c >> 2) & 1) * 8;
            vsel = ones ? 0u : 0xffffffffu;
        }
        const int qq = l31 - 4 * hh;
        const int t_first = ai >= 2 ? qt : (ai == 1 ? 4 : 8), t_last = 8 + qt;
        const bool has_first = ai >= 2;
        auto toff = [&](int t) { return (unsigned)((t >> 2) * BLK + (t & 3) * 512); };
        auto qk = [&](int t, const f32x16& c) {
            const unsigned o = toff(t);
            const u32x4 km = lds128(kA + o);
            const u32x2 kr = lds64(kB + o);
            return mfma32k8(kr, qr, mfma32(km, qm, c));
        };
        auto pv = [&](int t, const f32x16& p, const f32x16& o) {
            const unsigned va = vA + (toff(t) & vsel);
            const u32x2 t0 = ldstr(va), t1 = ldstr(va + 128), t2 = ldstr(va + 256), t3 = ldstr(va + 384);
            const u32x4 p0 = u32x4{pk(p[0], p[1]), pk(p[2], p[3]), pk(p[4], p[5]), pk(p[6], p[7])};
            const u32x4 p1 = u32x4{pk(p[8], p[9]), pk(p[10], p[11]), pk(p[12], p[13]), pk(p[14], p[15])};
            return mfma32(u32x4{t2[0], t2[1], t3[0], t3[1]}, p1, mfma32(u32x4{t0[0], t0[1], t1[0], t1[1]}, p0, o));
        };
        const float NINF = -__builtin_inff();
        f32x16 z16;
#pragma unroll
        for (int r = 0; r < 16; ++r) z16[r] = 0.f;
        f32x16 o, sd;
        float l, m;
        bool redo;
        if (ai >= 2) {
            u32x4 km[2]; u32x2 kr[2], vt[2][4];
            auto ldK = [&](int bf, int t) { const unsigned oo = toff(t); km[bf] = lds128(kA + oo); kr[bf] = lds64(kB + oo); };
            auto ldV = [&](int bf, int t) {
                const unsigned va = vA + (toff(t) & vsel);
                vt[bf][0] = ldstr(va); vt[bf][1] = ldstr(va + 128); vt[bf][2] = ldstr(va + 256); vt[bf][3] = ldstr(va + 384);
            };
            auto ex8 = [&](const f32x16& s, int h8) {
                return u32x4{pk(__builtin_amdgcn_exp2f(s[h8]), __builtin_amdgcn_exp2f(s[h8 + 1])), pk(__builtin_amdgcn_exp2f(s[h8 + 2]), __builtin_amdgcn_exp2f(s[h8 + 3])),
                             pk(__builtin_amdgcn_exp2f(s[h8 + 4]), __builtin_amdgcn_exp2f(s[h8 + 5])), pk(__builtin_amdgcn_exp2f(s[h8 + 6]), __builtin_amdgcn_exp2f(s[h8 + 7]))};
            };
            ldK(0, t_last); ldK(1, qt); ldV(0, t_last); ldV(1, qt);
            sd = mfma32k8(kr[0], qr, mfma32(km[0], qm, z16));
            SB0();
            ldK(0, qt + 1);
            m = NINF;
#pragma unroll
            for (int r = 0; r < 16; ++r) { const int c = (r & 3) + 8 * (r >> 2); sd[r] = c <= qq ? sd[r] : NINF; m = fmaxf(m, sd[r]); }
            m = fmaxf(m, __shfl_xor(m, 32));
            f32x16 nm, s[2], sm;
#pragma unroll
            for (int r = 0; r < 16; ++r) { nm[r] = -m; sm[r] = sd[r] - m; }
            s[0] = mfma32(km[1], qm, nm);
            SB0();
            {
                const u32x4 p0 = ex8(sm, 0);
                SB0();
                s[0] = mfma32k8(kr[1], qr, s[0]);
                o = mfma32(u32x4{vt[0][0][0], vt[0][0][1], vt[0][1][0], vt[0][1][1]}, p0, z16);
                SB0();
                const u32x4 p1 = ex8(sm, 8);
                SB0();
                o = mfma32(u32x4{vt[0][2][0], vt[0][2][1], vt[0][3][0], vt[0][3][1]}, p1, o);
            }
#pragma unroll
            for (int i = 0; i < 8; ++i) {
                const int kb2 = i & 1, vb = (i + 1) & 1;
                SB0();
                if (i < 7) s[(i + 1) & 1] = mfma32(km[kb2], qm, nm);
                SB0();
                f32x16 sc = s[i & 1];
                if (i == 0) {
#pragma unroll
                    for (int r = 0; r < 16; ++r) { const int c = (r & 3) + 8 * (r >> 2); sc[r] = c > qq ? sc[r] : NINF; }
                }
                const u32x4 p0 = ex8(sc, 0);
                SB0();
                if (i < 7) s[(i + 1) & 1] = mfma32k8(kr[kb2], qr, s[(i + 1) & 1]);
                if (i < 6) ldK(kb2 ^ 1, qt + i + 2);
                if (i < 7) ldV(vb ^ 1, qt + i + 1);
                SB0();
                o = mfma32(u32x4{vt[vb][0][0], vt[vb][0][1], vt[vb][1][0], vt[vb][1][1]}, p0, o);
                SB0();
                const u32x4 p1 = ex8(sc, 8);
                SB0();
                o = mfma32(u32x4{vt[vb][2][0], vt[vb][2][1], vt[vb][3][0], vt[vb][3][1]}, p1, o);
                SB0();
                if (i == 2) { ypublish(); q_prefetch(); }
                if (i >= 3) { wo_dma(2 * (i - 3)); wo_dma(2 * (i - 3) + 1); }
            }
            wo_dma(10);
            l = o[12];
            redo = !__all(l <= 1048576.f);
        } else {
            ypublish(); q_prefetch();
            for (int k = 0; k < 11; ++k) wo_dma(k);
            sd = qk(t_last, z16);
            m = NINF;
#pragma unroll
            for (int r = 0; r < 16; ++r) { const int c = (r & 3) + 8 * (r >> 2); sd[r] = c <= qq ? sd[r] : NINF; m = fmaxf(m, sd[r]); }
            m = fmaxf(m, __shfl_xor(m, 32));
            redo = true;
        }
        if (redo) {
            for (int attempt = ai >= 2 ? 1 : 0; attempt < 2; ++attempt) {
                if (attempt == 1) {
                    for (int t = t_first; t < t_last; ++t) {
                        f32x16 s = qk(t, z16);
#pragma unroll
                        for (int r = 0; r < 16; ++r) { const int c = (r & 3) + 8 * (r >> 2); const bool keep = !(has_first && t == t_first) || c > qq; m = fmaxf(m, keep ? s[r] : NINF); }
                    }
                    m = fmaxf(m, __shfl_xor(m, 32));
                }
                f32x16 nm, p;
#pragma unroll
                for (int r = 0; r < 16; ++r) { nm[r] = -m; p[r] = __builtin_amdgcn_exp2f(sd[r] - m); }
                o = pv(t_last, p, z16);
                for (int t = t_first; t < t_last; ++t) {
                    f32x16 s = qk(t, nm);
                    if (has_first && t == t_first) {
#pragma unroll
                        for (int r = 0; r < 16; ++r) { const int c = (r & 3) + 8 * (r >> 2); s[r] = c > qq ? s[r] : NINF; }
                    }
#pragma unroll
                    for (int r = 0; r < 16; ++r) p[r] = __builtin_amdgcn_exp2f(s[r]);
                    o = pv(t, p, o);
                }
                l = o[12];
                if (__all(l <= 1048576.f)) break;
            }
        }
        STAMP(15);
        const float il = 1.f / l;
        const int T16 = 8 * ai + 2 * qt + (l31 >> 4);
        const u32x4 om = u32x4{pk(o[0] * il, o[1] * il), pk(o[2] * il, o[3] * il), pk(o[4] * il, o[5] * il), pk(o[6] * il, o[7] * il)};
        __builtin_amdgcn_raw_buffer_store_b128(om, wsr, WS_OM + ((T16 * 16 + gp) * 64 + (2 * hd + hh) * 16 + (l31 & 15)) * 16, 0, 16);
        const unsigned ra = pk(o[8] * il, o[9] * il), rb2 = pk(o[10] * il, o[11] * il), rv = hd ? rb2 : ra;
        const u32x4 rq = u32x4{rv, (unsigned)__builtin_amdgcn_mov_dpp(rv, 0x55, 0xF, 0xF, true), (unsigned)__builtin_amdgcn_mov_dpp(rv, 0xAA, 0xF, 0xF, true), (unsigned)__builtin_amdgcn_mov_dpp(rv, 0xFF, 0xF, 0xF, true)};
        if (hh == 0 && (l31 & 3) == 0) __builtin_amdgcn_raw_buffer_store_b128(rq, wsr, WS_OR + ((((4 * ai + qt) * 16 + gp) * 2 + hd) * 32 + l31) * 4, 0, 16);
    }
    {
        GAS u64* f = mb(wg_tile(2 * ai + (lane & 1), (lane >> 1) & 7), MB_O + gp * 8);
        WAITVM(0);
        const bool last = __builtin_amdgcn_readfirstlane(arrive(L0 + SY_ODR, 8, lane) ? 1 : 0);
        if (last && lane < 16) __hip_atomic_store(f, nonce, RLX);
    }
    STAMP(20);
    if (w < 4 || w == 7) {
        poll_idx(mb(b, MB_Y), lane < 36 ? lane : -1, nonce);
        if (w < 4 || lane < 32) {
            const int chB = 72 * bc + 2 * cp, t0 = 64 * br + 8 * oct;
            const u32x4 y0 = __builtin_amdgcn_raw_buffer_load_b128(wsr, WS_Y + chB * 4096 + t0 * 2, 0, 16);
            const u32x4 y1 = __builtin_amdgcn_raw_buffer_load_b128(wsr, WS_Y + (chB + 1) * 4096 + t0 * 2, 0, 16);
            const u32x4 kv0 = __builtin_amdgcn_raw_buffer_load_b128(wsr, WS_R0 + chB * 4096 + (2040 - t0) * 2, 0, 16);
            const u32x4 kv1 = __builtin_amdgcn_raw_buffer_load_b128(wsr, WS_R0 + (chB + 1) * 4096 + (2040 - t0) * 2, 0, 16);
            const float D0 = blo(dv), D1 = bhi(dv);
            unsigned zz[8];
#pragma unroll
            for (int t = 0; t < 8; ++t) {
                const int slot = (t & 1) * 4 + (t >> 1);
                const float ya = (slot & 1) ? bhi(y0[slot >> 1]) : blo(y0[slot >> 1]);
                const float yb = (slot & 1) ? bhi(y1[slot >> 1]) : blo(y1[slot >> 1]);
                const int j = 7 - t;
                const float ka = (j & 1) ? bhi(kv0[j >> 1]) : blo(kv0[j >> 1]);
                const float kb = (j & 1) ? bhi(kv1[j >> 1]) : blo(kv1[j >> 1]);
                const unsigned kd = pk(ka * D0, kb * D1);
                zz[t] = pk(ya + blo(kd), yb + bhi(kd));
            }
            spin4(L0 + SY_QRD, 1); spin4(L0 + SY_QRD + 16, 1);
#pragma unroll
            for (int t = 0; t < 8; ++t) st32(L0 + L_Z + oct * 1184 + t * 144 + cp * 4, zz[t]);
        }
    } else if (w == 4) {
        poll_idx(mb(b, MB_O), lane < 16 ? lane : -1, nonce);
    }
    STAMP(21);
    BAR();
    STAMP(22);
    {
        const int tt = w & 3, kh = w >> 2, T16 = 4 * br + tt;
        u32x4 of[9];
#pragma unroll
        for (int s = 0; s < 8; ++s) of[s] = __builtin_amdgcn_raw_buffer_load_b128(wsr, WS_OM + ((T16 * 16 + 8 * kh + s) * 64 + lane) * 16, 0, 16);
#pragma unroll
        for (int d = 0; d < 4; ++d) of[8][d] = __builtin_amdgcn_raw_buffer_load_b32(wsr, WS_OR + ((((T16 >> 1) * 16 + 8 * kh + 2 * g4 + (d >> 1)) * 2 + (d & 1)) * 32 + 16 * (T16 & 1) + l15) * 4, 0, 16);
        SB0();
        {
            auto gate = [&](const u32x4& qv, int tk, int gr) {
                const u32x4 z = lds128(L0 + L_Z + tk * 144 + (tk >> 3) * 32 + gr * 16);
                u32x4 r;
#pragma unroll
                for (int d = 0; d < 4; ++d) r[d] = pk(blo(qv[d]) * blo(z[d]), bhi(qv[d]) * bhi(z[d]));
                __builtin_amdgcn_raw_buffer_store_b128(r, outr, (64 * br + tk) * 2304 + 1152 + bc * 144 + gr * 16, 0, OUTAUX);
            };
            gate(qg0, tk0, gr0);
            if (w == 5) gate(qg1, tk1, gr1);
        }
        f32x4 acc[5];
#pragma unroll
        for (int j = 0; j < 5; ++j) acc[j] = f32x4{0, 0, 0, 0};
        const int q4 = (lane >> 2) & 3, p4 = lane & 3, hd = g4 >> 1, hq = g4 & 1;
        const unsigned mlane = L0 + (20 * hd + 4 * hq + q4) * 144 + p4 * 8;
        const unsigned mlane4 = L0 + (20 * hd + 4 * hq + q4) * 144 + 128 + (p4 & 1) * 8;
        const int gr = 8 * kh + 2 * g4;
        const unsigned rlane = L0 + (gr < 6 ? L_WO1 + gr * 5184 : L_WO2 + (gr - 6) * 5184) + (16 + q4) * 144;
        u32x2 fa[2][5], fc[2][5];
        auto ldw = [&](int bf, int s) {
            if (s < 8) {
                const int gq = 8 * kh + s;
                const unsigned gb = gq < 6 ? L_WO1 + gq * 5184 : L_WO2 + (gq - 6) * 5184;
                const unsigned m0 = mlane + gb, m4 = mlane4 + gb;
#pragma unroll
                for (int j = 0; j < 4; ++j) { fa[bf][j] = ldstr(m0 + j * 32); fc[bf][j] = ldstr(m0 + j * 32 + 8 * 144); }
                fa[bf][4] = ldstr(m4); fc[bf][4] = ldstr(m4 + 8 * 144);
            } else {
#pragma unroll
                for (int j = 0; j < 5; ++j) {
                    const unsigned ad = rlane + (j < 4 ? j * 32 + p4 * 8 : 128 + (p4 & 1) * 8);
                    fa[bf][j] = ldstr(ad); fc[bf][j] = ldstr(ad + 5184);
                }
            }
        };
        ldw(0, 0);
#pragma unroll
        for (int s = 0; s < 9; ++s) {
            SB0();
            if (s < 8) ldw((s + 1) & 1, s + 1);
            SB0();
#pragma unroll
            for (int j = 0; j < 5; ++j) acc[j] = mfma16(u32x4{fa[s & 1][j][0], fa[s & 1][j][1], fc[s & 1][j][0], fc[s & 1][j][1]}, of[s], acc[j]);
        }
        SB0();
        const unsigned sc = L0 + L_X + tt * 5120 + lane * 16;
        if (kh == 0) { stf4(sc + 2048, acc[2]); stf4(sc + 3072, acc[3]); }
        else { stf4(sc, acc[0]); stf4(sc + 1024, acc[1]); stf4(sc + 4096, acc[4]); }
        BAR();
        const int op = (16 * T16 + l15) * 2304 + bc * 144;
        auto st2 = [&](const f32x4& va, const f32x4& vb) {
            const unsigned a0 = pk(va[0], va[1]), a1 = pk(va[2], va[3]), b0 = pk(vb[0], vb[1]), b1 = pk(vb[2], vb[3]);
            auto s0 = __builtin_amdgcn_permlane16_swap(a0, b0, false, false);
            auto s1 = __builtin_amdgcn_permlane16_swap(a1, b1, false, false);
            return u32x4{s0[0], s1[0], s0[1], s1[1]};
        };
        if (kh == 0) {
            const f32x4 v0 = acc[0] + ldsf4(sc), v1 = acc[1] + ldsf4(sc + 1024), v4 = acc[4] + ldsf4(sc + 4096);
            __builtin_amdgcn_raw_buffer_store_b128(st2(v0, v1), outr, op + (g4 & 1) * 32 + (g4 >> 1) * 16, 0, OUTAUX);
            const u32x4 r4 = st2(v4, v4);
            if (g4 == 0) __builtin_amdgcn_raw_buffer_store_b128(r4, outr, op + 128, 0, OUTAUX);
        } else {
            const f32x4 v2 = acc[2] + ldsf4(sc + 2048), v3 = acc[3] + ldsf4(sc + 3072);
            __builtin_amdgcn_raw_buffer_store_b128(st2(v2, v3), outr, op + 64 + (g4 & 1) * 32 + (g4 >> 1) * 16, 0, OUTAUX);
        }
    }
    STAMP(23);
    if (b == 0 && w == 7) {
        for (int k = 0; k < 4; ++k) poll_idx((const GAS u64*)(ws + WS_ARR), 64 * k + lane, nonce);
        if (lane == 0) __hip_atomic_store((GAS u64*)ws, nonce, RLX);
    }
    asm volatile(".Lcpf_end:");
}

extern "C" void kernel_launch(void* const* d_in, const int* in_sizes, int n_in, void* d_out, int out_size, void* d_ws, size_t ws_size, hipStream_t stream) {
    fused_kernel<<<256, 512, 0, stream>>>((const unsigned short*)d_in[0], (const unsigned short*)d_in[1], (const unsigned short*)d_in[2], (const unsigned short*)d_in[3],
                                          (const unsigned short*)d_in[4], (const unsigned short*)d_in[5], (const unsigned short*)d_in[6], (const unsigned short*)d_in[7],
                                          (const unsigned short*)d_in[8], (unsigned short*)d_out, (char*)d_ws);
}
```

```cpp
#include <hip/hip_runtime.h>
#include <stdint.h>

#define GAS __attribute__((address_space(1)))
#define LAS __attribute__((address_space(3)))
#define DEVI __device__ __forceinline__
typedef unsigned long long u64;
typedef __attribute__((ext_vector_type(8))) __bf16 bf16x8;
typedef __attribute__((ext_vector_type(2))) __bf16 bf16x2;
typedef __attribute__((ext_vector_type(2))) float f32x2;
typedef __attribute__((ext_vector_type(4))) float f32x4;
typedef __attribute__((ext_vector_type(16))) float f32x16;
typedef __attribute__((ext_vector_type(4))) unsigned u32x4;
typedef __attribute__((ext_vector_type(2))) unsigned u32x2;
typedef __attribute__((ext_vector_type(3))) unsigned u32x3;
typedef __attribute__((ext_vector_type(4))) short s16x4;

constexpr int WS_ARR = 256;
constexpr int WS_R0 = 32768;
constexpr int WS_QKV = WS_R0 + 2359296;
constexpr int WS_OM = WS_QKV + 7077888;
constexpr int WS_OR = WS_OM + 2097152;
constexpr int WS_Y = WS_OR + 262144;
constexpr int WS_MB = WS_Y + 2359296;
constexpr int MBS = 4352, MB_Q = 0, MB_O = 128, MB_K = 256, MB_Y = 512;
constexpr int WS_END = WS_MB + 256 * MBS;
constexpr int WS_TS = 32 << 20;
constexpr int B1P = 1056, B1_V = 9 * B1P, B1_S = 17 * B1P, B1_O = B1_S + 512;
constexpr int L_DUMMY = 157952;
constexpr int L_DUMMY2 = 161056;
#define NEWA 1
#define PF 2
constexpr int PFN = 1;
constexpr int L_B1A = 131072, L_B1B = 99328;
constexpr int KCS = 4160, KCH = 4 * KCS;
constexpr int RCS = 4576, RCH = 2 * RCS;
constexpr int L_KB = 32768, L_RB = 131072;
constexpr int L_KA = 66048, L_RA = 0;
constexpr int L_SCR = 114688;
constexpr int BLK = 10752, PCS = 2112;
constexpr int L_KBLK = 0, L_VBLK = 66048, L_QBLK = 66048 + 3 * BLK;
constexpr int L_WO1 = 32768, L_WO2 = 109056;
constexpr int L_ONES = 161280;
constexpr int L_Z = L_QBLK;
constexpr int L_X = 0;
constexpr int L_SYNC = -256;
constexpr int SY_LAND = L_SYNC, SY_CONS = L_SYNC + 16, SY_PW = L_SYNC + 32, SY_PR = L_SYNC + 48, SY_PREP = L_SYNC + 160, SY_ISS = L_SYNC + 68, SY_YDR = L_SYNC + 72,
              SY_ODR = L_SYNC + 76, SY_NONCE = L_SYNC + 80, SY_QDR = L_SYNC + 96, SY_QRD = L_SYNC + 112, SY_QSEEN = L_SYNC + 144, SY_NRDY = L_SYNC + 148, SY_BL = L_SYNC + 152,
              SY_B1B = L_SYNC + 156, SY_B1 = L_SYNC + 176;
constexpr int LDS_BYTES = 163840;
constexpr unsigned SPIN_CAP = 1u << 17, LSPIN_CAP = 1u << 20;

#define STAMPW(k) do { } while (0)
#define STAMPC(k) do { } while (0)
#define STAMP(k) do { } while (0)

DEVI f32x4 mfma16(u32x4 a, u32x4 b, f32x4 c) { return __builtin_amdgcn_mfma_f32_16x16x32_bf16(__builtin_bit_cast(bf16x8, a), __builtin_bit_cast(bf16x8, b), c, 0, 0, 0); }
DEVI f32x16 mfma32(u32x4 a, u32x4 b, f32x16 c) { return __builtin_amdgcn_mfma_f32_32x32x16_bf16(__builtin_bit_cast(bf16x8, a), __builtin_bit_cast(bf16x8, b), c, 0, 0, 0); }
DEVI f32x16 mfma32k8(u32x2 a, u32x2 b, f32x16 c) { return __builtin_amdgcn_mfma_f32_32x32x8bf16_1k(__builtin_bit_cast(s16x4, a), __builtin_bit_cast(s16x4, b), c, 0, 0, 0); }
DEVI unsigned pk(float lo, float hi) { f32x2 v = {lo, hi}; return __builtin_bit_cast(unsigned, __builtin_convertvector(v, bf16x2)); }
constexpr unsigned NOTYET = 0xffffffffu;
DEVI unsigned pkh(float lo, float hi) { const unsigned v = pk(lo, hi); return v < NOTYET - 1 ? v : NOTYET - 1; }
DEVI float blo(unsigned v) { return __builtin_bit_cast(float, v << 16); }
DEVI float bhi(unsigned v) { return __builtin_bit_cast(float, v & 0xffff0000u); }
DEVI u32x4 lds128(unsigned a) { return *(LAS const u32x4*)(uintptr_t)a; }
DEVI u32x2 lds64(unsigned a) { return *(LAS const u32x2*)(uintptr_t)a; }
DEVI unsigned lds32(unsigned a) { return *(LAS const unsigned*)(uintptr_t)a; }
DEVI void st128(unsigned a, u32x4 v) { *(LAS u32x4*)(uintptr_t)a = v; }
DEVI void st64(unsigned a, u32x2 v) { *(LAS u32x2*)(uintptr_t)a = v; }
DEVI void st32(unsigned a, unsigned v) { *(LAS unsigned*)(uintptr_t)a = v; }
DEVI f32x4 ldsf4(unsigned a) { return *(LAS const f32x4*)(uintptr_t)a; }
DEVI void stf4(unsigned a, f32x4 v) { *(LAS f32x4*)(uintptr_t)a = v; }
DEVI u32x2 ldstr(unsigned a) { return __builtin_bit_cast(u32x2, __builtin_amdgcn_ds_read_tr16_b64_v4i16((LAS s16x4*)(uintptr_t)a)); }
DEVI u32x4 vlds128(unsigned a) { return *(volatile LAS const u32x4*)(uintptr_t)a; }
DEVI unsigned vlds32(unsigned a) { return *(volatile LAS const unsigned*)(uintptr_t)a; }
DEVI void vst32(unsigned a, unsigned v) { *(volatile LAS unsigned*)(uintptr_t)a = v; }
#pragma clang diagnostic ignored "-Winline-asm"
DEVI void touch(unsigned voff, const void* sbase, unsigned lds) {
    asm volatile("s_mov_b32 m0, %2\n\ts_nop 0\n\tglobal_load_lds_dword %0, %1" ::"v"(voff), "s"(sbase), "s"(lds) : "memory", "m0");
}
DEVI void touch1(unsigned voff, const void* sbase, unsigned lds) {
    asm volatile("s_mov_b32 m0, %2\n\ts_nop 0\n\tglobal_load_lds_dword %0, %1 sc1" ::"v"(voff), "s"(sbase), "s"(lds) : "memory", "m0");
}
#define TLBW 0
#define TLBS 65536
DEVI void dmaS(unsigned voff, const void* sbase, unsigned lds) {
    asm volatile("s_mov_b32 m0, %2\n\ts_nop 0\n\tglobal_load_lds_dwordx4 %0, %1" ::"v"(voff), "s"(sbase), "s"(lds) : "memory", "m0");
}
template <int VO, int LO>
DEVI void dmaO(unsigned vb, const void* sbase, unsigned lds) {
    asm volatile("s_add_u32 m0, %2, %3\n\ts_nop 0\n\tglobal_load_lds_dwordx4 %0, %1" ::"v"(vb + VO), "s"(sbase), "s"(lds), "i"(LO) : "memory", "m0", "scc");
}
template <int LO>
DEVI void dma54(unsigned vb, const void* sbase, unsigned lds) {
    asm volatile("s_lshr_b64 exec, -1, 10\n\ts_add_u32 m0, %2, %3\n\ts_nop 0\n\tglobal_load_lds_dwordx4 %0, %1\n\ts_mov_b64 exec, -1"
                 ::"v"(vb), "s"(sbase), "s"(lds), "i"(LO) : "memory", "m0", "scc");
}
#define DPACE ""
#define DMA1(v, b, o) "s_add_u32 m0, %[sl], " o "\n\ts_nop 0\n\tglobal_load_lds_dwordx4 " v ", " b "\n\t" DPACE
DEVI void dmaA(const unsigned* vo, const void* xs, const void* b5, const void* wn, unsigned sld) {
    asm volatile(DMA1("%0", "%8", "0") DMA1("%1", "%8", "0xc00") DMA1("%2", "%8", "0x1800") DMA1("%3", "%8", "0x2400") DMA1("%4", "%8", "0x3000")
                 DMA1("%5", "%9", "0x3c00") DMA1("%6", "%10", "0x4800") DMA1("%7", "%10", "0x5400")
                 ::"v"(vo[0]), "v"(vo[1]), "v"(vo[2]), "v"(vo[3]), "v"(vo[4]), "v"(vo[5]), "v"(vo[6]), "v"(vo[7]), "s"(xs), "s"(b5), "s"(wn), [sl] "s"(sld) : "memory", "scc");
}
DEVI void dmaB(const unsigned* vo, const void* wn, const void* b10, unsigned sld, unsigned o10) {
    asm volatile(DMA1("%0", "%3", "0x6000") DMA1("%1", "%3", "0x6c00") DMA1("%2", "%4", "%5")
                 ::"v"(vo[8]), "v"(vo[9]), "v"(vo[10]), "s"(wn), "s"(b10), "s"(o10), [sl] "s"(sld) : "memory", "scc");
}
#define XCNT do { } while (0)
#define WAITVM(n) asm volatile("s_waitcnt vmcnt(%0)" ::"i"(n) : "memory")
#define BAR() do { asm volatile("s_waitcnt lgkmcnt(0)\n\ts_barrier" ::: "memory"); } while (0)
#define GST(k) do { } while (0)
#define LST(k) do { } while (0)
#define AST(k) do { } while (0)
#define PST(v) do { } while (0)
#define PST0(v) do { } while (0)
#define PQ(k) do { } while (0)
#define SBAR() do { asm volatile("s_barrier" ::: "memory"); } while (0)
#define EARLY2 0
#define BPRIO 2
#define CPF 1
#define TD 4
#define KVS 6
#define NPRE 1
#define B1SPLIT 0
#define KP0 7
#define QDR_RR 0
#define PFS 3
#define PFW 2
#define B0 1
#define EPL (PF == 1)
#define RAMP 7
#define NPA (PF == 1 ? 0 : 1)
#define NPB3 0
static_assert(NEWA || (B1SPLIT ? KVS >= 3 : KVS >= 2), "the kv stores of the loader waves must have been waited for");
#define CFENCE() asm volatile("" ::: "memory")
#define SB0() __builtin_amdgcn_sched_barrier(0)
#define RLX __ATOMIC_RELAXED, __HIP_MEMORY_SCOPE_AGENT
template <int V> struct IC { static constexpr int value = V; };
template <int N, int I = 0, class F> DEVI void static_for(F&& f) { if constexpr (I < N) { f(IC<I>{}); static_for<N, I + 1>(f); } }
DEVI unsigned rfl(unsigned v) { return __builtin_amdgcn_readfirstlane(v); }
DEVI unsigned umin4(u32x4 v) { const unsigned a = v[0] < v[1] ? v[0] : v[1], c = v[2] < v[3] ? v[2] : v[3]; return a < c ? a : c; }

template <int SLP = 1>
DEVI void spin4(unsigned a, unsigned need) {
#pragma nounroll
    for (unsigned s = 0; s < LSPIN_CAP; ++s) {
        if (__all(vlds32(a + (threadIdx.x & 3) * 4) >= need)) break;
        __builtin_amdgcn_s_sleep(SLP);
    }
    CFENCE();
}
DEVI void spin4v(unsigned addr, unsigned needv) {
#pragma nounroll
    for (unsigned s = 0; s < LSPIN_CAP; ++s) {
        if (__all(vlds32(addr) >= needv)) break;
        __builtin_amdgcn_s_sleep(1);
    }
    CFENCE();
}
DEVI void spin1(unsigned a, unsigned need) {
#pragma nounroll
    for (unsigned s = 0; s < LSPIN_CAP; ++s) {
        if (rfl(vlds32(a)) >= need) break;
        __builtin_amdgcn_s_sleep(1);
    }
    CFENCE();
}
DEVI void signal(unsigned a, unsigned v, int lane) { CFENCE(); if (lane == 0) vst32(a, v); CFENCE(); }
DEVI bool arrive(unsigned a, unsigned n, int lane) {
    CFENCE();
    unsigned old = 0;
    if (lane == 0) old = __hip_atomic_fetch_add((LAS unsigned*)(uintptr_t)a, 1u, __ATOMIC_RELAXED, __HIP_MEMORY_SCOPE_WORKGROUP);
    CFENCE();
    return lane == 0 && old == n - 1;
}
#define LSLP 2
#define CPRIO 0
#define TL 0
constexpr unsigned CU = 1;
#define LJ 2
#define ISSAT 26
#define GPRIO 3
#define OUTAUX 17
#define OAUX 16
#define NWB 7
#define YPI 6
#define GATEPOS 1
#define WOPF 1
#define QPI (FLB ? -1 : 1)
#define PZI 0
#define YDI 1
#define FLI 4
#define PZE 1
#define OFI 0
#define ASY 0
#define HSW 0
#define POLLS 2
#define ASE 1
#define DMAW 1
#define DS0 (FLB ? -1 : 2)
#define ZW2 7
#define FLB 1
#define UNI 1
#define RETRY1 0
#define DMASK 0
#define QRE 1
#define OMID 0
#define OLATE 0
#define YL2 0
#define OEARLY 0
#define APR2 1
#define FLW 0
#define YLI 6
#define DPI 4
#define PPH 0
#define EPAR 1
DEVI void poll_idx(const GAS u64* f, int idx, u64 nonce) {
#pragma nounroll
    for (unsigned spins = 0; spins < SPIN_CAP; ++spins) {
        u64 v = nonce;
        if (idx >= 0) v = __hip_atomic_load(f + idx, RLX);
        if (__all(v == nonce)) break;
        __builtin_amdgcn_s_sleep(POLLS);
    }
    __builtin_amdgcn_fence(__ATOMIC_ACQUIRE, "wavefront");
    CFENCE();
}

DEVI int wg_attn(int ai, int gp) { return (30 - 2 * ai + (gp & 1)) * 8 + (gp >> 1); }
DEVI int wg_pair(int p) { return p < 224 ? (p % 28) * 8 + p / 28 : (28 + (((p - 224) >> 1) & 3)) * 8 + ((p - 224) >> 3); }
DEVI int wg_tile(int br, int bc) { return (4 * bc + (br & 3)) * 8 + (br >> 2); }

__device__ constexpr int CMU[36] = {0,0,0,0,1,1,1,1,2,2,2,0,3,3,3,2,4,4,1,0,5,5,4,3,6,2,1,0,7,6,5,4,3,2,1,0};
__device__ constexpr int CNU[36] = {7,6,5,4,6,5,4,3,5,4,3,3,4,3,2,2,3,2,2,2,2,1,1,1,1,1,1,1,0,0,0,0,0,0,0,0};

__global__ __launch_bounds__(512, 2) void fused_kernel(const unsigned short* __restrict__ x, const unsigned short* __restrict__ wqkv, const unsigned short* __restrict__ wo,
                                                       const unsigned short* __restrict__ bq, const unsigned short* __restrict__ bk, const unsigned short* __restrict__ bv,
                                                       const unsigned short* __restrict__ shk, const unsigned short* __restrict__ ssk, const unsigned short* __restrict__ ssd,
                                                       unsigned short* __restrict__ out, char* __restrict__ ws) {
    __shared__ __attribute__((aligned(256))) char smem[LDS_BYTES];
    const unsigned L0 = (unsigned)(uintptr_t)(LAS char*)smem + 256;
    const int tid = threadIdx.x, lane = tid & 63, w = __builtin_amdgcn_readfirstlane(tid >> 6);
    const int b = blockIdx.x, xcd = b & 7, jj = b >> 3;
    const bool busy = jj < 28;
    const int ri = 4 * (xcd >> 1) + (jj & 3), cj = 7 * (xcd & 1) + (jj >> 2);
    const int br = 4 * xcd + (jj & 3), bc = jj >> 2;
    const int ai = (31 - jj) >> 1, gp = 2 * xcd + (jj & 1);
    const int pairB = busy ? xcd * 28 + jj : 225 + 2 * (xcd * 4 + jj - 28);
    const int l15 = lane & 15, g4 = lane >> 4, l31 = lane & 31, hh = lane >> 5;
    const __amdgpu_buffer_rsrc_t wsr = __builtin_amdgcn_make_buffer_rsrc(ws, 0, WS_END, 0x00020000);
    const __amdgpu_buffer_rsrc_t outr = __builtin_amdgcn_make_buffer_rsrc(out, 0, 2048 * 2304, 0x00020000);
    {
        const char* pc; unsigned len;
        asm volatile("s_getpc_b64 %0\n.Lcpf_here:\n\ts_mov_b32 %1, .Lcpf_end-.Lcpf_here" : "=s"(pc), "=s"(len));
        const unsigned o = (jj * 16 + lane) * 128;
        if (w == 0 && lane < 16 && o < len) touch(o, pc, L0 + L_DUMMY);
    }
    u64 ps0 = 0, ps1 = 0, ps2 = 0, ps3 = 0;
    PST(ps0);
    asm volatile("" ::"s"(x), "s"(wqkv), "s"(wo), "s"(bq), "s"(bk), "s"(bv), "s"(shk), "s"(ssk), "s"(ssd), "s"(out), "s"(ws));
    PST(ps1);
    const int tileA = busy ? xcd * 28 + jj : 224 + 2 * (xcd * 4 + jj - 28);
    const unsigned b1v = (lane >> 3) * 1152 + (lane & 7) * 16;
    auto b1_load = [&](int T, unsigned lb) {
        const int tb = T & 31, cb = T >> 5;
        if (w < 2) {
            const int r0 = tb ? 64 * tb - 8 : 0;
            const char* eb = w ? (const char*)shk + cb * 512 : (const char*)bk + r0 * 1152 + cb * 128;
            dmaS(w ? (unsigned)l31 * 16 : b1v, eb, lb + (w ? B1_S : 0));
        }
        const int ro = (64 * tb + 8 * w) * 1152 + cb * 128;
        PQ(5);
        dmaS(b1v, (const char*)bk + ro, lb + (w + 1) * B1P);
        PQ(6);
        dmaS(b1v, (const char*)bv + ro, lb + B1_V + w * B1P);
    };
    if (__builtin_expect(busy, 1)) {
        const unsigned vx = (lane >> 3) * 1152 + (((lane & 7) ^ (((w & 1) << 2) + (lane >> 4))) * 16);
        int wch = (lane & 15) ^ ((lane >> 4) << 2);
        if (cj == 13) wch &= 7;
        const unsigned wv = (lane >> 4) * 3456 + wch * 16;
        const char* xb = (const char*)x + (128 * ri + 8 * w) * 1152;
        const char* wb = (const char*)wqkv + cj * 256 + w * 13824;
        PQ(0);
        dmaO<0, 0>(vx, xb, L0 + w * 1024);
        PQ(1);
        dmaO<0, 16384>(wv, wb, L0 + w * 1024);
        PQ(2);
        dmaO<73728, 8192>(vx, xb, L0 + w * 1024);
        PQ(3);
        dmaO<110592, 24576>(wv, wb, L0 + w * 1024);
        PQ(4);
    } else {
        b1_load(tileA + 1, L0 + L_B1B);
    }
    b1_load(tileA, L0 + L_B1A);
    PST(ps2);
    PST(ps3);
    unsigned b1k[11], b1vv[8];
    u32x4 b1s;
    auto b1_read = [&](unsigned lb, int it) {
        const int oct = lane & 7, cp = 8 * it + (lane >> 3);
        const unsigned kb = lb + oct * B1P + cp * 4;
        b1s = lds128(lb + B1_S + cp * 16);
#pragma unroll
        for (int q = 0; q < 11; ++q) b1k[q] = lds32(kb + ((5 + q) >> 3) * B1P + ((5 + q) & 7) * 128);
#pragma unroll
        for (int t = 0; t < 8; ++t) b1vv[t] = lds32(kb + B1_V + t * 128);
    };
    auto b1_fin = [&](int T, int it) {
        const int oct = lane & 7, cp = 8 * it + (lane >> 3);
        const bool head = (T & 31) == 0 && oct == 0;
        unsigned kvp[8];
        f32x2 k[11];
#pragma unroll
        for (int q = 0; q < 11; ++q) { const unsigned v = (q < 3 && head) ? 0u : b1k[q]; k[q] = f32x2{blo(v), bhi(v)}; }
        const f32x2 s0 = {blo(b1s[0]), blo(b1s[2])}, s1 = {bhi(b1s[0]), bhi(b1s[2])}, s2 = {blo(b1s[1]), blo(b1s[3])}, s3 = {bhi(b1s[1]), bhi(b1s[3])};
#pragma unroll
        for (int t = 0; t < 8; ++t) {
            const unsigned v = b1vv[t];
            f32x2 a = k[t] * s3;
            a = __builtin_elementwise_fma(k[t + 1], s2, a);
            a = __builtin_elementwise_fma(k[t + 2], s1, a);
            a = __builtin_elementwise_fma(k[t + 3], s0, a);
            const unsigned ks = pk(a[0], a[1]);
            const f32x2 kv2 = f32x2{blo(ks), bhi(ks)} * f32x2{blo(v), bhi(v)};
            kvp[t] = pk(kv2[0], kv2[1]);
        }
        u32x4 r0, r1;
#pragma unroll
        for (int dd = 0; dd < 4; ++dd) {
            r0[dd] = __builtin_amdgcn_perm(kvp[6 - 2 * dd], kvp[7 - 2 * dd], 0x05040100u);
            r1[dd] = __builtin_amdgcn_perm(kvp[6 - 2 * dd], kvp[7 - 2 * dd], 0x07060302u);
        }
        const int off = WS_R0 + (64 * (T >> 5) + 2 * cp) * 4096 + (2040 - 64 * (T & 31) - 8 * oct) * 2;
        __builtin_amdgcn_raw_buffer_store_b128(r0, wsr, off, 0, 16);
        __builtin_amdgcn_raw_buffer_store_b128(r1, wsr, off + 4096, 0, 16);
    };
    auto mb = [&](int wg, int off) { return (GAS u64*)(ws + WS_MB + wg * MBS + off); };
    auto get_nonce = [&]() {
        const u32x2 v = *(volatile LAS const u32x2*)(uintptr_t)(L0 + SY_NONCE);
        return ((u64)rfl(v[1]) << 32) | rfl(v[0]);
    };
    auto oflags = [&](u64 nc) { if (lane < 16) __hip_atomic_store(mb(wg_tile(2 * ai + (lane & 1), (lane >> 1) & 7), MB_O + gp * 8), nc, RLX); };
    auto yflags = [&](u64 nc) {
        const int p = pairB - (busy ? 0 : hh), pc = p / 36;
        if (lane < 32 || !busy) __hip_atomic_store(mb(wg_tile(l31, pc), MB_Y + (p - 36 * pc) * 8), nc, RLX);
    };
    unsigned wov[7], qo[3], qmk[6], oam, oar, kA, kB, vA, vsel, qa, qb;
    int tk0, gr0, tk1, gr1, tk2, gr2;
    u64 anonce;
    auto opoison = [&]() {
        const u32x4 P = {NOTYET, NOTYET, NOTYET, NOTYET};
        __builtin_amdgcn_raw_buffer_store_b128(P, wsr, busy ? oam : 0x7ffffff0u, 0, 16);
        __builtin_amdgcn_raw_buffer_store_b128(P, wsr, busy ? oar : 0x7ffffff0u, 0, 16);
    };
    auto gather = [&](int sw) {
        const u64 nonce = get_nonce();
        if (!busy) spin4(L0 + SY_CONS, 1);
        if (sw == 0) {
            const int j = lane >> 1, rr = j < 6 ? ai - 2 + (j < 3 ? j : j - 3) : ai;
            poll_idx(mb(b, MB_Q), (lane < 14 && rr >= 0) ? lane : -1, nonce);
            signal(L0 + SY_QSEEN, 1, lane);
        } else spin1(L0 + SY_QSEEN, 1);
        STAMPW(24 + sw);
        const int p0 = (36 * gp - 4 * (gp & 1)) >> 3;
        const int R0 = WS_QKV + ((ai > 2 ? ai - 2 : 0) * 216 + p0) * 2048, R1 = WS_QKV + ((ai > 1 ? ai - 1 : 0) * 216 + p0) * 2048, R2 = WS_QKV + (ai * 216 + p0) * 2048;
        auto src = [&](int j) { return (j == 0 || j == 3 ? R0 : (j == 1 || j == 4 ? R1 : R2)) + (j < 3 ? 72 : (j < 6 ? 144 : 0)) * 2048; };
        auto dstb = [&](int j) { return j < 3 ? L_KBLK + j * BLK : L_VBLK + (j - 3) * BLK; };
        const int up = sw >> 1;
        const unsigned vm = lane * 16 + sw * 1024, vt = lane * 16 + (8 + (sw & 1)) * 1024;
        u32x4 g[18];
#pragma unroll
        for (int j = 0; j < 7; ++j) {
            g[2 * j] = __builtin_amdgcn_raw_buffer_load_b128(wsr, vm, src(j), 16);
            g[2 * j + 1] = __builtin_amdgcn_raw_buffer_load_b128(wsr, vm, src(j) + 4096, 16);
        }
#pragma unroll
        for (int i = 0; i < 4; ++i) g[14 + i] = __builtin_amdgcn_raw_buffer_load_b128(wsr, vt, up ? src(i < 3 ? 2 * i + 1 : 6) : src(2 * i), 16);
        SB0();
        opoison();
        SB0();
        const unsigned dm = L0 + up * PCS + (sw & 1) * 1024 + lane * 16, dt = L0 + 4 * PCS + (sw & 1) * 1024 + lane * 16;
#pragma unroll
        for (int j = 0; j < 7; ++j) { st128(dm + dstb(j), g[2 * j]); st128(dm + dstb(j) + 2 * PCS, g[2 * j + 1]); SB0(); }
#pragma unroll
        for (int i = 0; i < 4; ++i) st128(dt + (up ? dstb(i < 3 ? 2 * i + 1 : 6) : dstb(2 * i)), g[14 + i]);
        WAITVM(0);
        STAMPW(44 + sw);
    };

    auto att_setup = [&]() {
        const unsigned sel = (unsigned)(lane / 9) | 0x0c0c0c00u, ch16 = (unsigned)(lane % 9) * 16;
        static_for<6>([&](auto ii) {
            constexpr int i = decltype(ii)::value;
            constexpr auto prow = [](int sl) { const int t = sl >= 36 ? sl - 36 : sl, o = sl >= 36 ? 36 : 0;
                                               return o + (t < 16 ? (t & 1) * 8 + (t >> 1) : (t < 20 ? t : 20 + ((t - 20) & 1) * 8 + ((t - 20) >> 1))); };
            constexpr unsigned lo = prow(6 * i) | prow(6 * i + 1) << 8 | prow(6 * i + 2) << 16 | prow(6 * i + 3) << 24;
            constexpr unsigned hi = prow(6 * i + 4) | prow(6 * i + 5) << 8 | prow(6 * i + 6) << 16 | prow(6 * i + 7) << 24;
            wov[i] = __builtin_amdgcn_perm(hi, lo, sel) * 1152 + ch16;
        });
        wov[6] = wov[5] - ((w == (DMAW && (UNI || ai >= 2) ? 3 : 7) && lane >= 54) ? 36 * 1152 : 0);
        const int tl = tid & 255;
        tk0 = tl / 9; gr0 = tl - 9 * tk0; tk1 = (tl + 256) / 9; gr1 = tl + 256 - 9 * tk1; tk2 = (512 + lane) / 9; gr2 = 512 + lane - 9 * tk2;
        qo[0] = tk0 * 1152 + gr0 * 16; qo[1] = tk1 * 1152 + gr1 * 16; qo[2] = tk2 * 1152 + gr2 * 16;
        const int hd = (w >> 2) ^ HSW, qt = w & 3, cs = 4 * (gp & 1), lo = cs + 18 * hd;
        qa = L0 + L_QBLK + (2 * hd + hh) * PCS + (32 * qt + l31) * 16;
        qb = L0 + L_QBLK + (2 * hd + 2) * PCS + (32 * qt + l31) * 16 + hh * 8;
#pragma unroll
        for (int d = 0; d < 4; ++d) { const int c = 8 * (2 * hd + hh) + 2 * d; qmk[d] = (c >= lo && c < lo + 18) ? 0xffffffffu : 0u; }
#pragma unroll
        for (int d = 0; d < 2; ++d) { const int c = 8 * (2 * hd + 2) + 4 * hh + 2 * d; qmk[4 + d] = (c >= lo && c < lo + 18) ? 0xffffffffu : 0u; }
        kA = L0 + L_KBLK + (2 * hd + hh) * PCS + l31 * 16;
        kB = L0 + L_KBLK + (2 * hd + 2) * PCS + l31 * 16 + hh * 8;
        {
            const int q = (lane >> 2) & 3, p = lane & 3;
            const bool ones = (g4 & 1) && p > 0;
            const int c = (g4 & 1) ? cs + 16 : cs + (hd ? 20 : 0) + 4 * p;
            vA = ones ? L0 + L_ONES + 64 * ((hd ? 1 : 3) + hh) : L0 + L_VBLK + (c >> 3) * PCS + (4 * hh + q) * 16 + ((c >> 2) & 1) * 8;
            vsel = ones ? 0u : 0xffffffffu;
        }
        oam = WS_OM + (((8 * ai + 2 * qt + (l31 >> 4)) * 16 + gp) * 64 + (2 * hd + hh) * 16 + (l31 & 15)) * 16;
        oar = (hh == 0 && (l31 & 3) == 0) ? WS_OR + ((((4 * ai + qt) * 16 + gp) * 2 + hd) * 32 + l31) * 4 : 0x7ffffff0u;
        anonce = get_nonce();
#pragma unroll
        for (int i = 0; i < 7; ++i) asm volatile("" : "+v"(wov[i]));
#pragma unroll
        for (int i = 0; i < 6; ++i) asm volatile("" : "+v"(qmk[i]));
        asm volatile("" : "+v"(qo[0]), "+v"(qo[1]), "+v"(qo[2]), "+v"(tk0), "+v"(gr0), "+v"(tk1), "+v"(gr1), "+v"(tk2), "+v"(gr2));
        asm volatile("" : "+v"(oam), "+v"(oar), "+v"(kA), "+v"(kB), "+v"(vA), "+v"(vsel), "+v"(qa), "+v"(qb));
    };

    if (w >= 4) {
        const int d = w - 4;
        u32x4 kd0[3];
        u32x3 kd1[3];
        auto kload = [&](int pair) {
#pragma unroll
            for (int i = 0; i < 3; ++i) {
                const int u = d + 3 * i < 8 ? d + 3 * i : 7, cq = (u & 3) * 64 + lane;
                const char* p = (const char*)ssk + (2 * pair + (u >> 2)) * 4096 + cq * 16;
                kd0[i] = *(const u32x4*)p; kd1[i] = *(const u32x3*)(p + (cq < 255 ? 16 : 0));
            }
        };
        auto kcopy = [&](unsigned kb0) {
#pragma unroll
            for (int i = 0; i < 3; ++i) {
                const int u = d + 3 * i;
                if (u < 8) {
                    const unsigned kb = kb0 + (u >> 2) * KCH, a = kb + (4 + (u & 3) * 64 + lane) * 16;
                    const u32x4 d0 = kd0[i]; const u32x3 d1 = kd1[i];
                    st128(a, d0);
                    st128(a + KCS, u32x4{d0[1], d0[2], d0[3], d1[0]});
                    st128(a + 2 * KCS, u32x4{d0[2], d0[3], d1[0], d1[1]});
                    st128(a + 3 * KCS, u32x4{d0[3], d1[0], d1[1], d1[2]});
                    if ((u & 3) == 0) {
                        if (lane == 0) {
                            st128(kb + 3 * 16, u32x4{0, 0, 0, 0});
                            st128(kb + KCS + 3 * 16, u32x4{0, 0, 0, d0[0]});
                            st128(kb + 2 * KCS + 3 * 16, u32x4{0, 0, d0[0], d0[1]});
                            st128(kb + 3 * KCS + 3 * 16, u32x4{0, d0[0], d0[1], d0[2]});
                        } else if (lane < 13) {
                            const int e = lane - 1;
                            st128(kb + (e / 3) * KCS + (e % 3) * 16, u32x4{0, 0, 0, 0});
                        }
                    }
                }
            }
        };
        unsigned vo[11];
        const char* xb = (const char*)x + (size_t)(128 * ri) * 1152;
        const char* wb = (const char*)wqkv + cj * 256;
        const unsigned o10 = d < 2 ? 30720 : 0;
        auto issue = [&](int n) {
            const char* xs = xb + n * 128;
            const char* wn = wb + (size_t)n * (64 * 3456);
            dmaA(vo, xs, d == 0 ? xs : wn, wn, L0 + (n & 3) * 32768 + d * 1024);
            dmaB(vo, wn, d < 2 ? wn : xs, L0 + (n & 3) * 32768 + d * 1024, o10);
        };
        u64 lts = 0, gts = 0, nonce = 0;
        const bool ring = busy && d < 3;
        if (__builtin_expect(ring, 1)) {
            int wch = (lane & 15) ^ ((lane >> 4) << 2);
            if (cj == 13) wch &= 7;
            const unsigned wv = (lane >> 4) * 3456 + wch * 16;
            auto xoff = [&](int u) { return (unsigned)((lane >> 3) * 1152 + (((lane & 7) ^ (((u & 1) << 2) + (lane >> 4))) * 16) + u * 9216); };
            auto woff = [&](int u) { return wv + (u - 16) * 4 * 3456; };
            const unsigned m0_ = 0u - (unsigned)(d == 0), m2_ = 0u - (unsigned)(d == 2);
#pragma unroll
            for (int i = 0; i < 11; ++i) {
                const int u = d + 3 * i;
                vo[i] = i < 5 ? xoff(u) : (i == 5 ? (xoff(15) & m0_) | (woff(u) & ~m0_) : (i < 10 ? woff(u) : (xoff(2) & m2_) | (woff(u) & ~m2_)));
                asm volatile("" : "+v"(vo[i]));
            }
            LST(0);
#if NPA >= 1
            issue(1);
#endif
            LST(1);
            LST(2);
            WAITVM(NPA >= 1 ? 11 : 0);
            LST(3);
        } else if (d < 3) {
            WAITVM(0);
        } else {
            const u64 ep = __hip_atomic_load((const GAS u64*)ws, RLX);
            WAITVM(0);
            nonce = ep + 1;
            asm volatile("" : "+v"(nonce));
        }
        SBAR();
        if (__builtin_expect(ring, 1)) {
            LST(4);
#if NPA < 1
            issue(1);
#endif
            issue(2);
            LST(5);
        } else if (d == 3) {
            __builtin_amdgcn_s_setprio(GPRIO);
            touch(lane * 128, (const char*)ssk + (busy ? pairB : pairB - 1) * 8192, L0 + L_DUMMY);
            if (!busy) touch(lane * 128, (const char*)ssk + pairB * 8192, L0 + L_DUMMY);
            if (lane == 0) *(volatile LAS u32x2*)(uintptr_t)(L0 + SY_NONCE) = u32x2{(unsigned)nonce, (unsigned)(nonce >> 32)};
        }
        if (busy) {
            if (d < 3) WAITVM(NPB3 ? 22 : 11);
            SBAR();
            GST(48);
        }
#pragma nounroll
        for (int t = 0; t < (busy ? 0 : 2); ++t) {
            b1_read(L0 + (t ? L_B1B : L_B1A), d);
            b1_fin(tileA + t, d);
        }
        if (d == 0) STAMPW(29);
        if (d < 3) {
            if (busy) {
#pragma nounroll
                for (int s = 1; s < (RAMP < 6 ? RAMP : 6); ++s) {
                    issue(s + 2);
                    WAITVM(11);
                    if (s == 1) LST(6);
                    if (s == 2) LST(7);
                    SBAR();
                }
                if (RAMP < 6) {
                    if (RAMP < 5) {
                        issue(RAMP + 2); issue(RAMP + 3);
                        WAITVM(22); SBAR();
#pragma nounroll
                        for (int s = RAMP + 1; s < 5; ++s) { issue(s + 3); WAITVM(22); SBAR(); }
                    } else issue(7);
                    issue(8);
                    SB0();
                    kload(pairB);
                    SB0();
                    WAITVM(28); SBAR();
                    WAITVM(17); SBAR();
                } else {
                    issue(8);
                    SB0();
                    kload(pairB);
                    SB0();
                    WAITVM(17); SBAR();
                }
                WAITVM(6); SBAR();
                SBAR();
                CFENCE();
                kcopy(L0 + L_KB);
                signal(L0 + SY_PREP + d * 4, 2, lane);
                if (d == 1) {
                    const int i = lane / 6, r = lane - 6 * i, e = r & 1, dd = r >> 1;
                    const int G = (cj == 0 ? 0 : (128 * cj - 39) / 36) + i, c = 36 * G - 4 * (G & 1) + 39 * e;
                    const int sec = G >> 4, a2 = ri + dd;
                    const bool ok = i < 6 && G < 48 && (c >> 7) == cj && (sec == 0 ? dd == 0 : a2 < 16);
                    const int slot = sec == 0 ? 12 + e : 2 * (2 - dd + (sec == 2 ? 3 : 0)) + e;
                    GAS u64* f = mb(wg_attn(a2, G & 15), MB_Q + slot * 8);
                    const u64 nc = get_nonce();
                    spin4(L0 + SY_QDR, 1);
                    if (ok) __hip_atomic_store(f, nc, RLX);
                    STAMPW(2);
                }
            } else {
                kload(pairB - 1);
                WAITVM(0);
                SBAR();
                CFENCE();
                kcopy(L0 + L_KA);
                kload(pairB);
                signal(L0 + SY_PREP + d * 4, 1, lane);
                kcopy(L0 + L_KB);
                signal(L0 + SY_PREP + d * 4, 2, lane);
                __builtin_amdgcn_s_waitcnt(0x0F70);
            }
        } else {
            const int kt = tileA + hh;
            GAS u64* kf = mb(wg_pair(32 * (kt >> 5) + l31), MB_K + (kt & 31) * 8);
            auto kflags = [&]() {
                if (lane < 32 || !busy) __hip_atomic_store(kf, nonce, RLX);
                if (lane == 0) __hip_atomic_store((GAS u64*)(ws + WS_ARR) + b, nonce, RLX);
                STAMPW(11);
            };
            u32x4 rd[2][4]; unsigned rn[2][4];
            auto rload = [&](int pair) {
#pragma unroll
                for (int c = 0; c < 2; ++c)
#pragma unroll
                    for (int r = 0; r < 4; ++r) {
                        const int cq = r * 64 + lane, off = WS_R0 + (2 * pair + c) * 4096 + cq * 16;
                        rd[c][r] = __builtin_amdgcn_raw_buffer_load_b128(wsr, off, 0, 16);
                        rn[c][r] = __builtin_amdgcn_raw_buffer_load_b32(wsr, off + (cq < 255 ? 16 : 0), 0, 16);
                    }
            };
            auto rcopy = [&](unsigned rb0) {
#pragma unroll
                for (int c = 0; c < 2; ++c) {
                    const unsigned rb = rb0 + c * RCH;
#pragma unroll
                    for (int r = 0; r < 4; ++r) {
                        const int cq = r * 64 + lane;
                        const u32x4 dd = rd[c][r]; const unsigned nx = cq < 255 ? rn[c][r] : 0u;
                        st128(rb + cq * 16, dd);
                        st128(rb + RCS + cq * 16, u32x4{__builtin_amdgcn_alignbit(dd[1], dd[0], 16), __builtin_amdgcn_alignbit(dd[2], dd[1], 16), __builtin_amdgcn_alignbit(dd[3], dd[2], 16), __builtin_amdgcn_alignbit(nx, dd[3], 16)});
                    }
                    if (lane < 60) st128(rb + (lane >= 30 ? RCS : 0) + (256 + (lane >= 30 ? lane - 30 : lane)) * 16, u32x4{0, 0, 0, 0});
                }
            };
            if (busy) {
#pragma nounroll
                for (int s = 1; s <= KVS; ++s) { if (s == KVS) WAITVM(0); SBAR(); GST(48 + s); }
                kflags();
                const GAS u64* kp = mb(b, MB_K) + l31;
                int s = KVS + 1;
#pragma nounroll
                for (; s < KP0; ++s) { SBAR(); GST(48 + s); }
                bool got = false;
#pragma nounroll
                while (s < 7) {
                    const u64 v = __hip_atomic_load(kp, RLX);
                    SBAR(); GST(48 + s); SBAR(); GST(49 + s);
                    s += 2;
                    if (__all(v == nonce)) { got = true; break; }
                }
                if (!got) {
#pragma nounroll
                    for (; s < 8; ++s) { SBAR(); GST(48 + s); }
                    BAR();
                    GST(56);
                    s = 9;
                    poll_idx(mb(b, MB_K), lane < 32 ? lane : -1, nonce);
                }
                __builtin_amdgcn_fence(__ATOMIC_ACQUIRE, "wavefront");
                STAMPW(12);
                rload(pairB);
                if (s < 9) {
#pragma nounroll
                    for (; s < 8; ++s) { SBAR(); GST(48 + s); }
                    BAR();
                    GST(56);
                }
            } else {
                WAITVM(0);
                BAR();
                kflags();
                oflags(nonce);
                poll_idx(mb(b, MB_K), lane < 32 ? lane : -1, nonce);
                STAMPW(12);
                rload(pairB - 1); rcopy(L0 + L_RA); signal(L0 + SY_PREP + 12, 1, lane);
                rload(pairB);
                __builtin_amdgcn_s_waitcnt(0x0F70);
            }
            rcopy(L0 + L_RB);
            signal(L0 + SY_PREP + 12, 2, lane);
            __builtin_amdgcn_s_setprio(0);
            STAMPW(13);
        }
        att_setup();
        gather(d);
    } else {
        if (w == 0) {
            if (lane < 48) vst32(L0 + L_SYNC + lane * 4, 0u);
        } else if (w == 1) {
#pragma unroll
            for (int i = 0; i < 3; ++i) { const int q = i * 64 + lane; if (q < 140) st128(L0 + L_ONES + q * 16, u32x4{0x3f803f80u, 0x3f803f80u, 0x3f803f80u, 0x3f803f80u}); }
        }
        const int wt = w & 1, wc = w >> 1;
        unsigned xo[4], wof[2];
#pragma unroll
        for (int j = 0; j < 4; ++j) { xo[j] = (64 * wt + l31) * 128 + (((2 * j + hh) ^ ((l31 >> 1) & 7)) * 16); asm volatile("" : "+v"(xo[j])); }
        {
            const int q = (lane >> 2) & 3, p = lane & 3;
#pragma unroll
            for (int ct = 0; ct < 2; ++ct) {
                wof[ct] = 16384 + (8 * hh + q) * 256 + (((8 * wc + 4 * ct + 2 * (g4 & 1) + (p >> 1)) ^ (q << 2)) * 16) + (p & 1) * 8;
                asm volatile("" : "+v"(wof[ct]));
            }
        }
        f32x16 acc[2][2];
#pragma unroll
        for (int r = 0; r < 16; ++r) { acc[0][0][r] = 0.f; acc[0][1][r] = 0.f; acc[1][0][r] = 0.f; acc[1][1][r] = 0.f; }
        asm volatile("" : "+v"(acc[0][0]), "+v"(acc[0][1]), "+v"(acc[1][0]), "+v"(acc[1][1]));
        WAITVM(0);
        STAMP(28);
        auto warm2 = [&]() {
#pragma unroll
            for (int q = 0; q < ((9 - PFS) * 22 + 111) / 112; ++q) {
                const int n = jj + 28 * (w + 4 * q);
                const int s0 = n / 22, st = s0 + PFS, r = n - 22 * s0;
                const int idx = (r - 8) * 64 + lane, kr = idx / 14, c = idx - 14 * kr;
                const unsigned vx = (512 * (xcd >> 1) + 64 * r + lane) * 1152 + st * 128;
                const unsigned vw = (64 * st + kr) * 3456 + (xcd & 1) * 1792 + ((xcd & 1) && c == 13 ? 12 : c) * 128;
                if (st < 9) touch(r < 8 ? vx : vw, r < 8 ? (const char*)x : (const char*)wqkv, L0 + L_DUMMY);
            }
        };
        if (busy && PFW == 0) warm2();
        if (!busy) {
            BAR();
            {
                const u32x4 P = {NOTYET, NOTYET, NOTYET, NOTYET};
                const unsigned vl = lane * 16;
                __builtin_amdgcn_raw_buffer_store_b128(P, wsr, vl, WS_OM + ((8 * ai + 2 * w) * 16 + gp) * 1024, 16);
                __builtin_amdgcn_raw_buffer_store_b128(P, wsr, vl + 16384, WS_OM + ((8 * ai + 2 * w) * 16 + gp) * 1024, 16);
                __builtin_amdgcn_raw_buffer_store_b128(P, wsr, lane < 16 ? vl : 0x7ffffff0u, WS_OR + ((4 * ai + w) * 16 + gp) * 256, 16);
            }
            WAITVM(0);
            BAR();
        } else {
            const bool live = !(cj == 13 && wc == 1);
            if (live) {
                u32x4 xf[2][2];
                u32x2 wa[2][2], wc2[2][2];
                auto adv = [&](unsigned& a) { a = (a + 32768u) & 0x1ffffu; asm volatile("" : "+v"(a)); };
                auto ldx = [&](int pb, int j, int tt) { xf[pb][tt] = lds128(L0 + xo[j] + tt * 4096); };
                auto ldwa = [&](int j, int pb, int ct) { wa[pb][ct] = ldstr(L0 + wof[ct] + j * 4096); };
                auto ldwc = [&](int j, int pb, int ct) { wc2[pb][ct] = ldstr(L0 + wof[ct] + j * 4096 + 1024); };
                auto W = [&](int pb, int ct) { return u32x4{wa[pb][ct][0], wa[pb][ct][1], wc2[pb][ct][0], wc2[pb][ct][1]}; };
                BAR();
                if (PFW == 1) warm2();
                STAMP(3); STAMPC(40);
                b1_read(L0 + L_B1A, w);
                b1_fin(tileA, w);
                SB0();
                if (PFW == 2) warm2();
                SB0();
                STAMP(1);
                __builtin_amdgcn_s_setprio(CPRIO);
                ldx(0, 0, 0); ldwa(0, 0, 0); ldwc(0, 0, 0); ldx(0, 0, 1); ldwa(0, 0, 1); ldwc(0, 0, 1);
                adv(xo[0]);
#pragma nounroll
                for (int s = 0; s < 9; ++s) {
#pragma unroll
                    for (int j = 0; j < 4; ++j) {
                        const int pb = j & 1, nb = pb ^ 1, nj = (j + 1) & 3;
                        SB0();
                        acc[0][0] = mfma32(W(pb, 0), xf[pb][0], acc[0][0]);
                        SB0();
                        if (j == 3) {
                            if (s == KVS) WAITVM(0);
                            BAR();
                        }
                        ldx(nb, nj, 0); ldwa(nj, nb, 0); ldwc(nj, nb, 0);
                        SB0();
                        acc[0][1] = mfma32(W(pb, 0), xf[pb][1], acc[0][1]);
                        SB0();
                        ldx(nb, nj, 1); ldwa(nj, nb, 1);
                        adv(xo[nj]);
                        SB0();
                        acc[1][0] = mfma32(W(pb, 1), xf[pb][0], acc[1][0]);
                        SB0();
                        ldwc(nj, nb, 1);
                        if (j == 2) { adv(wof[0]); adv(wof[1]); }
                        SB0();
                        acc[1][1] = mfma32(W(pb, 1), xf[pb][1], acc[1][1]);
                    }
                }
                __builtin_amdgcn_s_setprio(0);
                STAMP(4); STAMPC(41);
#pragma unroll
                for (int ct = 0; ct < 2; ++ct) {
                    const int n0 = 128 * cj + 64 * wc + 32 * ct;
                    const float sc = n0 < 576 ? 0.34004634f : 1.0f;
#pragma unroll
                    for (int tt = 0; tt < 2; ++tt) {
#pragma unroll
                        for (int pi = 0; pi < 2; ++pi) {
                            const f32x16& a = acc[ct][tt];
                            const unsigned a0 = pk(a[8 * pi] * sc, a[8 * pi + 1] * sc), a1 = pk(a[8 * pi + 2] * sc, a[8 * pi + 3] * sc);
                            const unsigned b0 = pk(a[8 * pi + 4] * sc, a[8 * pi + 5] * sc), b1 = pk(a[8 * pi + 6] * sc, a[8 * pi + 7] * sc);
                            auto s0 = __builtin_amdgcn_permlane32_swap(a0, b0, false, false);
                            auto s1 = __builtin_amdgcn_permlane32_swap(a1, b1, false, false);
                            const int off = WS_QKV + ((ri * 216 + (n0 >> 3) + 2 * pi + hh) * 128 + 64 * wt + 32 * tt + l31) * 16;
                            __builtin_amdgcn_raw_buffer_store_b128(u32x4{s0[0], s1[0], s0[1], s1[1]}, wsr, off, 0, 16);
                        }
                    }
                }
            } else {
                BAR();
                if (PFW == 1) warm2();
                b1_read(L0 + L_B1A, w);
                b1_fin(tileA, w);
                SB0();
                if (PFW == 2) warm2();
#pragma nounroll
                for (int s = 0; s < 9; ++s) { if (s == KVS) WAITVM(0); BAR(); }
            }
            STAMP(5);
        }
        bool qdone = !busy;
        SB0();
        att_setup();
        SB0();
        auto qdrain = [&]() {
            WAITVM(0);
            signal(L0 + SY_QDR + w * 4, 1, lane);
            STAMP(8);
            opoison();
        };
        auto wowarm = [&]() {
            if (w < 3 && lane < 54) touch((unsigned)(jj * 162 + w * 54 + lane) * 128, wo, L0 + L_DUMMY2);
        };
        for (int pass = busy ? 1 : 0; pass < 2; ++pass) {
            if (!qdone && !__all(vlds32(L0 + SY_PREP + (lane & 3) * 4) >= (unsigned)(pass + 1))) { qdrain(); qdone = true; }
            spin4(L0 + SY_PREP, pass + 1);
            STAMP(6 + 3 * pass);
            const int slot = w >> 1, h = w & 1;
            const unsigned kb = L0 + (pass ? L_KB : L_KA) + slot * KCH, rb = L0 + (pass ? L_RB : L_RA) + slot * RCH;
            const int ch = 2 * (pairB - 1 + pass) + slot;
            const unsigned la = kb + ((l15 + 1) & 3) * KCS + (g4 + ((l15 + 1) >> 2)) * 16 + h * 256;
            const unsigned lb = rb + ((l15 & 1) ? 0 : RCS) + (28 - 4 * (l15 >> 1) + g4) * 16 + h * 256;
            f32x4 acc[8];
#pragma unroll
            for (int n = 0; n < 8; ++n) acc[n] = f32x4{0, 0, 0, 0};
            u32x4 A[2][8], B[2][8];
#pragma unroll
            for (int m = 0; m < 8; ++m) { A[0][m] = lds128(la + m * 512); B[0][m] = lds128(lb + (7 - m) * 512); }
#pragma unroll
            for (int rr = 0; rr < 4; ++rr) {
                const int cur = rr & 1, nx = cur ^ 1;
#pragma unroll
                for (int i = 0; i < 36; ++i) {
                    SB0();
                    acc[CMU[i] + CNU[i]] = mfma16(A[cur][CMU[i]], B[cur][CNU[i]], acc[CMU[i] + CNU[i]]);
                    SB0();
                    if (rr < 3 && (i & 1) == 0 && i < 32) {
                        const int m = i >> 2;
                        if (i & 2) B[nx][7 - m] = lds128(lb + (rr + 1) * 64 + m * 512);
                        else A[nx][m] = lds128(la + (rr + 1) * 64 + m * 512);
                    }
                }
                if (rr == QDR_RR && !qdone) { qdrain(); qdone = true; }
                if (rr == QDR_RR && pass == 1) wowarm();
            }
            if (pass == 0) signal(L0 + SY_CONS + w * 4, 1, lane);
            const unsigned mine = L0 + L_SCR + w * 4096 + lane * 16, theirs = L0 + L_SCR + (w ^ 1) * 4096 + lane * 16;
            if (!busy && pass == 1) spin1(L0 + SY_PR + (w ^ 1) * 4, 1);
            f32x4 y[4];
            if (h) {
#pragma unroll
                for (int n = 0; n < 4; ++n) stf4(mine + n * 1024, acc[n]);
            } else {
#pragma unroll
                for (int n = 0; n < 4; ++n) stf4(mine + n * 1024, acc[4 + n]);
            }
            signal(L0 + SY_PW + w * 4, pass + 1, lane);
            spin1(L0 + SY_PW + (w ^ 1) * 4, pass + 1);
            if (h) {
#pragma unroll
                for (int n = 0; n < 4; ++n) y[n] = acc[4 + n] + ldsf4(theirs + n * 1024);
            } else {
#pragma unroll
                for (int n = 0; n < 4; ++n) y[n] = acc[n] + ldsf4(theirs + n * 1024);
            }
            signal(L0 + SY_PR + w * 4, pass + 1, lane);
            const bool odd = lane & 1;
#pragma unroll
            for (int i = 0; i < 4; i += 2) {
                const unsigned a0 = pk(y[i][0], y[i][1]), a1 = pk(y[i][2], y[i][3]), c0 = pk(y[i + 1][0], y[i + 1][1]), c1 = pk(y[i + 1][2], y[i + 1][3]);
                const unsigned r0 = __builtin_amdgcn_mov_dpp(odd ? a0 : c0, 0xB1, 0xF, 0xF, true), r1 = __builtin_amdgcn_mov_dpp(odd ? a1 : c1, 0xB1, 0xF, 0xF, true);
                const u32x4 v = odd ? u32x4{r0, r1, c0, c1} : u32x4{a0, a1, r0, r1};
                __builtin_amdgcn_raw_buffer_store_b128(v, wsr, WS_Y + ch * 4096 + (4 * h + i + (odd ? 1 : 0)) * 512 + ((l15 >> 1) * 4 + g4) * 16, 0, 16);
            }
            STAMP(7 + 3 * pass);
        }
        WAITVM(0);
    }
    STAMPW(16 + w);
    BAR();
    STAMP(14);
    const u64 nonce = anonce;
    if (w == FLW) yflags(nonce);
    if (w == (FLW + 1) % 8 && busy) oflags(nonce);
    const char* wob = (const char*)wo + bc * 144 + w * (72 * 1152);
    const unsigned wod0 = L0 + (w < 3 ? L_WO1 + w * 10368 : L_WO2 + (w - 3) * 10368);
    auto wo_dma = [&](auto kk) {
        constexpr int k = decltype(kk)::value, gi = k / 6, i = k % 6;
        dmaO<0, gi * 5184 + i * 864>(wov[k == 11 ? 6 : i], wob + gi * (36 * 1152), wod0);
    };
    auto wo_dma2 = [&](auto kk) {
        constexpr int p = decltype(kk)::value, k = p % 12, gi = k / 6, i = k % 6;
        if constexpr (p < 12) dmaO<0, gi * 5184 + i * 864>(wov[i], wob + gi * (36 * 1152), wod0);
        else dmaO<0, gi * 5184 + i * 864>(wov[k == 11 ? 6 : i], wob + (4 * 72 + gi * 36) * 1152, L0 + L_WO2 + (w + 1) * 10368);
    };
    u32x4 qg0 = u32x4{0, 0, 0, 0}, qg1 = qg0, qg2 = qg0;
    const bool yw = w < 4 || w == ZW2, z2 = ZW2 < 4 && w == ZW2;
    const int cp[2] = {w < 4 ? (tid & 255) >> 3 : 32 + (l31 >> 3), 32 + (l31 >> 3)}, oct = lane & 7;
    unsigned dv[2] = {0, 0};
    auto q_prefetch = [&]() {
        const char* qb_ = (const char*)bq + 64 * br * 1152 + bc * 144;
        if (w < 4) {
            qg0 = *(const u32x4*)(qb_ + qo[0]);
            qg1 = *(const u32x4*)(qb_ + qo[1]);
        }
        if (w == 0) qg2 = *(const u32x4*)(qb_ + qo[2]);
        if (yw) dv[0] = *(const unsigned*)(ssd + 72 * bc + 2 * cp[0]);
        if (z2) dv[1] = *(const unsigned*)(ssd + 72 * bc + 2 * cp[1]);
    };
    u32x4 yv[2][4];
    auto yload1 = [&](int k) {
        const int chB = 72 * bc + 2 * cp[k], t0 = 64 * br + 8 * oct;
        yv[k][0] = __builtin_amdgcn_raw_buffer_load_b128(wsr, WS_Y + chB * 4096 + t0 * 2, 0, 16);
        yv[k][1] = __builtin_amdgcn_raw_buffer_load_b128(wsr, WS_Y + (chB + 1) * 4096 + t0 * 2, 0, 16);
        yv[k][2] = __builtin_amdgcn_raw_buffer_load_b128(wsr, WS_R0 + chB * 4096 + (2040 - t0) * 2, 0, 16);
        yv[k][3] = __builtin_amdgcn_raw_buffer_load_b128(wsr, WS_R0 + (chB + 1) * 4096 + (2040 - t0) * 2, 0, 16);
    };
    auto yload = [&]() { yload1(0); if (z2) yload1(1); };
    auto zstage1 = [&](int k) {
        const u32x4 y0 = yv[k][0], y1 = yv[k][1], kv0 = yv[k][2], kv1 = yv[k][3];
        const float D0 = blo(dv[k]), D1 = bhi(dv[k]);
        const unsigned za = L0 + L_Z + oct * 1184 + cp[k] * 4;
#pragma unroll
        for (int t = 0; t < 8; ++t) {
            const int slot = (t & 1) * 4 + (t >> 1);
            const float ya = (slot & 1) ? bhi(y0[slot >> 1]) : blo(y0[slot >> 1]);
            const float yb = (slot & 1) ? bhi(y1[slot >> 1]) : blo(y1[slot >> 1]);
            const int j = 7 - t;
            const float ka = (j & 1) ? bhi(kv0[j >> 1]) : blo(kv0[j >> 1]);
            const float kb = (j & 1) ? bhi(kv1[j >> 1]) : blo(kv1[j >> 1]);
            const unsigned kd = pk(ka * D0, kb * D1);
            st32(za + t * 144, pk(ya + blo(kd), yb + bhi(kd)));
        }
    };
    auto zstage = [&]() { zstage1(0); if (z2) zstage1(1); };
    u32x4 of[9];
    const int oT16 = 4 * br + (w & 3), okh = w >> 2;
    const unsigned ovm = lane * 16, ovr = (2 * g4 * 64 + 16 * (oT16 & 1) + l15) * 4;
    const int osm = WS_OM + (oT16 * 16 + 8 * okh) * 1024, osr = WS_OR + ((oT16 >> 1) * 16 + 8 * okh) * 256;
    auto oload = [&](int s) {
        if (s < 8) of[s] = __builtin_amdgcn_raw_buffer_load_b128(wsr, ovm + s * 1024, osm, 16);
        else {
#pragma unroll
            for (int d = 0; d < 4; ++d) of[8][d] = __builtin_amdgcn_raw_buffer_load_b32(wsr, ovr + d * 128, osr, 16);
        }
    };
    auto oloads = [&]() {
#pragma unroll
        for (int s = 0; s < 9; ++s) oload(s);
    };
    {
        const int hd = (w >> 2) ^ HSW, qt = w & 3;
        u32x4 qm = lds128(qa);
        u32x2 qr = lds64(qb);
#pragma unroll
        for (int d = 0; d < 4; ++d) qm[d] &= qmk[d];
#pragma unroll
        for (int d = 0; d < 2; ++d) qr[d] &= qmk[4 + d];
        CFENCE();
        if (lane == 0) __hip_atomic_fetch_add((LAS unsigned*)(uintptr_t)(L0 + SY_QRD), 1u, __ATOMIC_RELAXED, __HIP_MEMORY_SCOPE_WORKGROUP);
        CFENCE();
        const int qq = l31 - 4 * hh;
        const int t_first = ai >= 2 ? qt : (ai == 1 ? 4 : 8), t_last = 8 + qt;
        const bool has_first = ai >= 2;
        auto toff = [&](int t) { return (unsigned)((t >> 2) * BLK + (t & 3) * 512); };
        auto qk = [&](int t, const f32x16& c) {
            const unsigned o = toff(t);
            const u32x4 km = lds128(kA + o);
            const u32x2 kr = lds64(kB + o);
            return mfma32k8(kr, qr, mfma32(km, qm, c));
        };
        auto pv = [&](int t, const f32x16& p, const f32x16& o) {
            const unsigned va = vA + (toff(t) & vsel);
            const u32x2 t0 = ldstr(va), t1 = ldstr(va + 128), t2 = ldstr(va + 256), t3 = ldstr(va + 384);
            const u32x4 p0 = u32x4{pk(p[0], p[1]), pk(p[2], p[3]), pk(p[4], p[5]), pk(p[6], p[7])};
            const u32x4 p1 = u32x4{pk(p[8], p[9]), pk(p[10], p[11]), pk(p[12], p[13]), pk(p[14], p[15])};
            return mfma32(u32x4{t2[0], t2[1], t3[0], t3[1]}, p1, mfma32(u32x4{t0[0], t0[1], t1[0], t1[1]}, p0, o));
        };
        auto lastof = [&](int word, unsigned n) { return __builtin_amdgcn_readfirstlane(arrive(L0 + word, n, lane) ? 1 : 0) != 0; };
        auto arr = [&](int word) {
            unsigned old = 0;
            CFENCE();
            if (lane == 0) old = __hip_atomic_fetch_add((LAS unsigned*)(uintptr_t)(L0 + word), 1u, __ATOMIC_RELAXED, __HIP_MEMORY_SCOPE_WORKGROUP);
            CFENCE();
            return old;
        };
        unsigned any_ = 0, ano_ = 0;
        u64 fv = 0;
        bool yearly = false;
        const int nfl = yw ? 36 : 16, mbf = yw ? MB_Y : MB_O;
        auto fload = [&]() { fv = __hip_atomic_load(mb(b, mbf) + (lane < nfl ? lane : 0), RLX); };
        auto fpoll = [&]() { poll_idx(mb(b, mbf), lane < nfl ? lane : -1, nonce); };
        auto ndma = [](int i) { return i <= DS0 ? 0 : (DPI * (i - DS0) > 24 ? 24 : DPI * (i - DS0)); };
        static_assert(DS0 + (24 + DPI - 1) / DPI - 1 <= FLI && PZE, "");
        const float NINF = -__builtin_inff();
        f32x16 z16;
#pragma unroll
        for (int r = 0; r < 16; ++r) z16[r] = 0.f;
        f32x16 o, sd;
        float l, m;
        bool redo;
        if (UNI || __builtin_expect(ai >= 2, 1)) {
            const int nskip = !UNI || ai >= 2 ? 0 : (ai == 1 ? 4 : 8) - qt;
            u32x4 km[2]; u32x2 kr[2], vt[2][4];
            auto ldK = [&](int bf, int t) { const unsigned oo = toff(t); km[bf] = lds128(kA + oo); kr[bf] = lds64(kB + oo); };
            auto ldV = [&](int bf, int t) {
                const unsigned va = vA + (toff(t) & vsel);
                vt[bf][0] = ldstr(va); vt[bf][1] = ldstr(va + 128); vt[bf][2] = ldstr(va + 256); vt[bf][3] = ldstr(va + 384);
            };
            auto ex8 = [&](const f32x16& s, int h8) {
                return u32x4{pk(__builtin_amdgcn_exp2f(s[h8]), __builtin_amdgcn_exp2f(s[h8 + 1])), pk(__builtin_amdgcn_exp2f(s[h8 + 2]), __builtin_amdgcn_exp2f(s[h8 + 3])),
                             pk(__builtin_amdgcn_exp2f(s[h8 + 4]), __builtin_amdgcn_exp2f(s[h8 + 5])), pk(__builtin_amdgcn_exp2f(s[h8 + 6]), __builtin_amdgcn_exp2f(s[h8 + 7]))};
            };
            auto side = [&](int i) {
                static_assert(DMAW && DS0 + (24 + DPI - 1) / DPI - 1 <= FLI && YLI > FLI, "");
                if (i == QPI) q_prefetch();
                if (w < 4) static_for<24>([&](auto kk) { if (i == DS0 + decltype(kk)::value / DPI) wo_dma2(kk); });
                if (i == FLI) fload();
                if (i == YLI && yw) {
                    yearly = __all(fv == nonce);
                    if (yearly) { __builtin_amdgcn_fence(__ATOMIC_ACQUIRE, "wavefront"); yload(); }
                }
            };
            AST(10);
            ldK(0, t_last); ldK(1, qt); ldV(0, t_last); ldV(1, qt);
            sd = mfma32k8(kr[0], qr, mfma32(km[0], qm, z16));
            SB0();
            ldK(0, qt + 1);
            SB0();
            side(-1);
            SB0();
            m = NINF;
#pragma unroll
            for (int r = 0; r < 16; ++r) { const int c = (r & 3) + 8 * (r >> 2); sd[r] = c <= qq ? sd[r] : NINF; m = fmaxf(m, sd[r]); }
            m = fmaxf(m, __shfl_xor(m, 32));
            f32x16 nm, s[2], sm;
#pragma unroll
            for (int r = 0; r < 16; ++r) { nm[r] = nskip ? NINF : -m; sm[r] = sd[r] - m; }
            s[0] = mfma32(km[1], qm, nm);
            SB0();
            {
                const u32x4 p0 = ex8(sm, 0);
                SB0();
                s[0] = mfma32k8(kr[1], qr, s[0]);
                o = mfma32(u32x4{vt[0][0][0], vt[0][0][1], vt[0][1][0], vt[0][1][1]}, p0, z16);
                SB0();
                const u32x4 p1 = ex8(sm, 8);
                SB0();
                o = mfma32(u32x4{vt[0][2][0], vt[0][2][1], vt[0][3][0], vt[0][3][1]}, p1, o);
            }
            AST(11);
#pragma unroll
            for (int i = 0; i < 8; ++i) {
                const int kb2 = i & 1, vb = (i + 1) & 1;
                SB0();
                if (i < 7 && __builtin_expect(nskip == i + 1, 0)) {
#pragma unroll
                    for (int r = 0; r < 16; ++r) nm[r] = -m;
                    asm volatile("" : "+v"(nm));
                }
                if (i < 7) s[(i + 1) & 1] = mfma32(km[kb2], qm, nm);
                SB0();
                f32x16 sc = s[i & 1];
                if (i == 0) {
#pragma unroll
                    for (int r = 0; r < 16; ++r) { const int c = (r & 3) + 8 * (r >> 2); sc[r] = c > qq ? sc[r] : NINF; }
                }
                const u32x4 p0 = ex8(sc, 0);
                SB0();
                if (i < 7) s[(i + 1) & 1] = mfma32k8(kr[kb2], qr, s[(i + 1) & 1]);
                if (i < 6) ldK(kb2 ^ 1, qt + i + 2);
                if (i < 7) ldV(vb ^ 1, qt + i + 1);
                SB0();
                o = mfma32(u32x4{vt[vb][0][0], vt[vb][0][1], vt[vb][1][0], vt[vb][1][1]}, p0, o);
                SB0();
                const u32x4 p1 = ex8(sc, 8);
                SB0();
                o = mfma32(u32x4{vt[vb][2][0], vt[vb][2][1], vt[vb][3][0], vt[vb][3][1]}, p1, o);
                SB0();
                side(i);
                if (i == APR2 && w >= 4) __builtin_amdgcn_s_setprio(1);
                AST(i);
            }
            SB0();
            __builtin_amdgcn_s_setprio(0);
            AST(8);
            AST(9);
            l = o[12];
            redo = !__all(l <= 1048576.f);
        }
        if (__builtin_expect(redo, 0)) {
            for (int attempt = UNI || ai >= 2 ? 1 : 0; attempt < 2; ++attempt) {
                if (attempt == 1) {
                    for (int t = t_first; t < t_last; ++t) {
                        f32x16 s = qk(t, z16);
#pragma unroll
                        for (int r = 0; r < 16; ++r) { const int c = (r & 3) + 8 * (r >> 2); const bool keep = !(has_first && t == t_first) || c > qq; m = fmaxf(m, keep ? s[r] : NINF); }
                    }
                    m = fmaxf(m, __shfl_xor(m, 32));
                }
                f32x16 nm, p;
#pragma unroll
                for (int r = 0; r < 16; ++r) { nm[r] = -m; p[r] = __builtin_amdgcn_exp2f(sd[r] - m); }
                o = pv(t_last, p, z16);
                for (int t = t_first; t < t_last; ++t) {
                    f32x16 s = qk(t, nm);
                    if (has_first && t == t_first) {
#pragma unroll
                        for (int r = 0; r < 16; ++r) { const int c = (r & 3) + 8 * (r >> 2); s[r] = c > qq ? s[r] : NINF; }
                    }
#pragma unroll
                    for (int r = 0; r < 16; ++r) p[r] = __builtin_amdgcn_exp2f(s[r]);
                    o = pv(t, p, o);
                }
                l = o[12];
                if (__all(l <= 1048576.f)) break;
            }
        }
        if (!UNI && ai < 2) fload();
        STAMP(15);
        SB0();
        const unsigned qrd = vlds32(L0 + SY_QRD);
        SB0();
        const float il = __builtin_amdgcn_rcpf(l);
        const u32x4 om = u32x4{pkh(o[0] * il, o[1] * il), pkh(o[2] * il, o[3] * il), pkh(o[4] * il, o[5] * il), pkh(o[6] * il, o[7] * il)};
        __builtin_amdgcn_raw_buffer_store_b128(om, wsr, oam, 0, 16);
        const unsigned ra = pkh(o[8] * il, o[9] * il), rb2 = pkh(o[10] * il, o[11] * il), rv = hd ? rb2 : ra;
        const u32x4 rq = u32x4{rv, (unsigned)__builtin_amdgcn_mov_dpp(rv, 0x55, 0xF, 0xF, true), (unsigned)__builtin_amdgcn_mov_dpp(rv, 0xAA, 0xF, 0xF, true), (unsigned)__builtin_amdgcn_mov_dpp(rv, 0xFF, 0xF, 0xF, true)};
        __builtin_amdgcn_raw_buffer_store_b128(rq, wsr, oar, 0, 16);
        STAMP(20);
        SB0();
        int ye = __builtin_amdgcn_readfirstlane(yearly ? 1 : 0), fok = __builtin_amdgcn_readfirstlane(__all(fv == nonce) ? 1 : 0);
        asm volatile("" : "+s"(ye), "+s"(fok));
        if (ye) {
            STAMP(30);
            if (__builtin_expect(rfl(qrd) < 8, 0))
            spin1(L0 + SY_QRD, 8);
            zstage();
            asm volatile("" ::"v"(qg0), "v"(qg1), "v"(qg2));
        } else {
            if (__builtin_expect(!fok, 0)) fpoll();
            __builtin_amdgcn_fence(__ATOMIC_ACQUIRE, "wavefront");
            if (yw) {
                yload();
                STAMP(30);
                if (__builtin_expect(rfl(qrd) < 8, 0))
                spin1(L0 + SY_QRD, 8);
                zstage();
                asm volatile("" ::"v"(qg0), "v"(qg1), "v"(qg2));
            }
        }
    }
    {
        const int tt = w & 3, kh = w >> 2, T16 = 4 * br + tt;
        const int q4 = (lane >> 2) & 3, p4 = lane & 3, hd = g4 >> 1, hq = g4 & 1;
        const unsigned mlane = L0 + (20 * hd + 8 * hq + 2 * q4) * 144 + p4 * 8;
        const unsigned mlane4 = L0 + (20 * hd + 8 * hq + 2 * q4) * 144 + 128 + (p4 & 1) * 8;
        const int gr = 8 * kh + 2 * g4;
        const unsigned rlane = L0 + (gr < 6 ? L_WO1 + gr * 5184 : L_WO2 + (gr - 6) * 5184) + (16 + q4) * 144;
        u32x2 fa[NWB][5], fc[NWB][5];
        auto ldw = [&](int bf, int s) {
            if (s < 8) {
                const int gq = 8 * kh + s;
                const unsigned gb = gq < 6 ? L_WO1 + gq * 5184 : L_WO2 + (gq - 6) * 5184;
                const unsigned m0 = mlane + gb, m4 = mlane4 + gb;
#pragma unroll
                for (int j = 0; j < 4; ++j) { fa[bf][j] = ldstr(m0 + j * 32); fc[bf][j] = ldstr(m0 + j * 32 + 144); }
                fa[bf][4] = ldstr(m4); fc[bf][4] = ldstr(m4 + 144);
            } else {
#pragma unroll
                for (int j = 0; j < 5; ++j) {
                    const unsigned ad = rlane + (j < 4 ? j * 32 + p4 * 8 : 128 + (p4 & 1) * 8);
                    fa[bf][j] = ldstr(ad); fc[bf][j] = ldstr(ad + 5184);
                }
            }
        };
        BAR();
        STAMP(21);
#pragma unroll
        for (int s = 0; s < NWB; ++s) {
            ldw(s, s);
            if (OMID && s + 1 == OMID) { SB0(); oloads(); SB0(); }
        }
        SB0();
        auto gate = [&](const u32x4& qv, int tk, int gr) {
            const u32x4 z = lds128(L0 + L_Z + tk * 144 + (tk >> 3) * 32 + gr * 16);
            u32x4 r;
#pragma unroll
            for (int d = 0; d < 4; ++d) r[d] = pk(blo(qv[d]) * blo(z[d]), bhi(qv[d]) * bhi(z[d]));
            return r;
        };
        auto gatecalc = [&]() {
            if (w < 4) { qg0 = gate(qg0, tk0, gr0); qg1 = gate(qg1, tk1, gr1); }
            if (w == 0) qg2 = gate(qg2, tk2, gr2);
        };
        auto gates = [&]() {
            auto st = [&](const u32x4& r, int tk, int gr) { __builtin_amdgcn_raw_buffer_store_b128(r, outr, (64 * br + tk) * 2304 + 1152 + bc * 144 + gr * 16, 0, OUTAUX); };
            if (w < 4) { st(qg0, tk0, gr0); st(qg1, tk1, gr1); }
            if (w == 0) st(qg2, tk2, gr2);
        };
        oloads();
        SB0();
        STAMP(22);
        gatecalc();
        SB0();
        f32x4 acc[5];
#pragma unroll
        for (int j = 0; j < 5; ++j) acc[j] = f32x4{0, 0, 0, 0};
        auto top = [&](int s) { const unsigned a = of[s][0] > of[s][1] ? of[s][0] : of[s][1], c = of[s][2] > of[s][3] ? of[s][2] : of[s][3]; return a > c ? a : c; };
        auto step = [&](int s) {
            const int bf = s % NWB;
#pragma unroll
            for (int j = 0; j < 5; ++j) acc[j] = mfma16(u32x4{fa[bf][j][0], fa[bf][j][1], fc[bf][j][0], fc[bf][j][1]}, of[s], acc[j]);
            if (s + NWB < 9) ldw(bf, s + NWB);
        };
        int done = 0;
#pragma unroll
        for (int s = 0; s < 9; ++s) {
            SB0();
            if (done == s && !__any(top(s) == NOTYET)) { step(s); done = s + 1; }
        }
        STAMP(25);
        if (__builtin_expect(done < 9, 0)) {
#pragma nounroll
            for (unsigned spins = 0; spins < SPIN_CAP; ++spins) {
                unsigned bad = 0;
#pragma unroll
                for (int s = 0; s < 9; ++s) bad |= __any(top(s) == NOTYET) ? 1u << s : 0u;
                if (!bad) break;
                XCNT;
#pragma unroll
                for (int s = 0; s < 9; ++s) if (bad & (1u << s)) oload(s);
                __builtin_amdgcn_s_waitcnt(0x0F70);
            }
#pragma unroll
            for (int s = 0; s < 9; ++s) if (s >= done) step(s);
        }
        SB0();
        const unsigned sc = L0 + L_X + tt * 5120 + lane * 16;
        if (kh == 0) { stf4(sc + 2048, acc[2]); stf4(sc + 3072, acc[3]); }
        else { stf4(sc, acc[0]); stf4(sc + 1024, acc[1]); stf4(sc + 4096, acc[4]); }
        if (GATEPOS == 1) gates();
        STAMP(26);
        BAR();
        STAMP(27);
        const int op = (16 * T16 + l15) * 2304 + bc * 144;
        auto st2 = [&](const f32x4& va, const f32x4& vb) {
            const unsigned a0 = pk(va[0], va[1]), a1 = pk(va[2], va[3]), b0 = pk(vb[0], vb[1]), b1 = pk(vb[2], vb[3]);
            auto s0 = __builtin_amdgcn_permlane16_swap(a0, b0, false, false);
            auto s1 = __builtin_amdgcn_permlane16_swap(a1, b1, false, false);
            return u32x4{s0[0], s1[0], s0[1], s1[1]};
        };
        if (kh == 0) {
            const f32x4 v0 = acc[0] + ldsf4(sc), v1 = acc[1] + ldsf4(sc + 1024), v4 = acc[4] + ldsf4(sc + 4096);
            __builtin_amdgcn_raw_buffer_store_b128(st2(v0, v1), outr, op + (g4 & 1) * 32 + (g4 >> 1) * 16, 0, OUTAUX);
            const u32x4 r4 = st2(v4, v4);
            if (g4 == 0) __builtin_amdgcn_raw_buffer_store_b128(r4, outr, op + 128, 0, OUTAUX);
        } else {
            const f32x4 v2 = acc[2] + ldsf4(sc + 2048), v3 = acc[3] + ldsf4(sc + 3072);
            __builtin_amdgcn_raw_buffer_store_b128(st2(v2, v3), outr, op + 64 + (g4 & 1) * 32 + (g4 >> 1) * 16, 0, OUTAUX);
        }
    }
    STAMP(23);
    if (b == 0 && w == 7) {
        const GAS u64* ar = (const GAS u64*)(ws + WS_ARR) + lane;
#pragma nounroll
        for (unsigned spins = 0; spins < SPIN_CAP; ++spins) {
            const u64 a0 = __hip_atomic_load(ar, RLX), a1 = __hip_atomic_load(ar + 64, RLX), a2 = __hip_atomic_load(ar + 128, RLX), a3 = __hip_atomic_load(ar + 192, RLX);
            if (__all(a0 == nonce && a1 == nonce && a2 == nonce && a3 == nonce)) break;
            __builtin_amdgcn_s_sleep(POLLS);
        }
        if (lane == 0) __hip_atomic_store((GAS u64*)ws, nonce, RLX);
    }
    asm volatile(".Lcpf_end:");
}

extern "C" void kernel_launch(void* const* d_in, const int* in_sizes, int n_in, void* d_out, int out_size, void* d_ws, size_t ws_size, hipStream_t stream) {
    fused_kernel<<<256, 512, 0, stream>>>((const unsigned short*)d_in[0], (const unsigned short*)d_in[1], (const unsigned short*)d_in[2], (const unsigned short*)d_in[3],
                                          (const unsigned short*)d_in[4], (const unsigned short*)d_in[5], (const unsigned short*)d_in[6], (const unsigned short*)d_in[7],
                                          (const unsigned short*)d_in[8], (unsigned short*)d_out, (char*)d_ws);
}
```
